```python
import torch

def _softmax(x, axis=-1):
    return torch.softmax(x, dim=axis)

def _gelu(x):
    return torch.nn.functional.gelu(x, approximate='tanh')

def _silu(x):
    return torch.nn.functional.silu(x)

def _sigmoid(x):
    return torch.sigmoid(x)

def _layernorm(x, axis=-1, eps=1e-05):
    m = x.mean(dim=axis, keepdim=True)
    v = x.var(dim=axis, keepdim=True, unbiased=False)
    return (x - m) / torch.sqrt(v + eps)

def _scatter_add(tgt, idx, src, axis=0):
    return tgt.clone().index_add_(axis, idx, src)

def _segment_sum(src, seg, n):
    out = torch.zeros((n,) + src.shape[1:], dtype=src.dtype)
    return out.index_add_(0, seg, src)

def _pad(x, pw):
    flat = tuple((v for pair in reversed(pw) for v in pair))
    return torch.nn.functional.pad(x, flat)
kernel_name = 'gated_mix__memory_aug_recurrent_mem_tr__dense_activation_gelu__prompt'

def setup_inputs():

    _w = lambda a: (lambda t: t.to(torch.bfloat16) if t.is_floating_point() else t)(torch.as_tensor(a))

    def _A_setup():
        B, S, Dm = (1, 2048, 576)
        H, Dh = (32, 128)
        M, seg = (8, 32)
        seg = min(seg, S)
        S = S // seg * seg
        return {'x': _w((lambda *s: torch.randn(*s, dtype=torch.bfloat16) * 0.05)(B, S, Dm)), 'mem0': _w((lambda *s: torch.randn(*s, dtype=torch.bfloat16) * 0.4)(B, M, Dm)), 'Wq': _w((lambda *s: torch.randn(*s, dtype=torch.bfloat16) * 6.4)(Dm, H, Dh) * Dm ** (-0.5)), 'Wk': _w((lambda *s: torch.randn(*s, dtype=torch.bfloat16) * 6.4)(Dm, H, Dh) * Dm ** (-0.5)), 'Wv': _w((lambda *s: torch.randn(*s, dtype=torch.bfloat16) * 0.4)(Dm, H, Dh) * Dm ** (-0.5)), 'Wo': _w((lambda *s: torch.randn(*s, dtype=torch.bfloat16) * 0.4)(H, Dh, Dm) * (H * Dh) ** (-0.5)), 'seg_len': seg}
    _A_in = _A_setup()

    def _B_setup():
        B, S, D, F = (1, 2048, 576, 4608)
        return {'x': _w((lambda *s: torch.randn(*s, dtype=torch.bfloat16) * 0.05)(B, S, D)), 'w1': _w((lambda *s: torch.randn(*s, dtype=torch.bfloat16) * 3.2)(D, F) * D ** (-0.5)), 'b1': _w((lambda *s: torch.randn(*s, dtype=torch.bfloat16) * 3.2)(F) * 0.01), 'w2': _w((lambda *s: torch.randn(*s, dtype=torch.bfloat16) * 0.2)(F, D) * F ** (-0.5)), 'b2': _w((lambda *s: torch.randn(*s, dtype=torch.bfloat16) * 0.4)(D) * 0.01)}
    _B_in = _B_setup()
    return {'A_x': _A_in['x'], 'A_mem0': _A_in['mem0'], 'A_Wq': _A_in['Wq'], 'A_Wk': _A_in['Wk'], 'A_Wv': _A_in['Wv'], 'A_Wo': _A_in['Wo'], 'A_seg_len': _A_in['seg_len'], 'B_x': _B_in['x'], 'B_w1': _B_in['w1'], 'B_b1': _B_in['b1'], 'B_w2': _B_in['w2'], 'B_b2': _B_in['b2'], 'gate_w': _w((lambda *s: torch.randn(*s, dtype=torch.bfloat16) * 0.05)(576) * 16.0)}

def reference(A_x, A_mem0, A_Wq, A_Wk, A_Wv, A_Wo, A_seg_len, B_x, B_w1, B_b1, B_w2, B_b2, gate_w):

    def _A_ref(x, mem0, Wq, Wk, Wv, Wo, seg_len):
        B, S, Dm = x.shape
        H, Dh = (Wq.shape[1], Wq.shape[2])
        M = mem0.shape[1]
        scale = Dh ** (-0.5)
        n_seg = S // seg_len
        L = M + seg_len
        idx = torch.arange(L)
        mask = (idx[:, None] < M) | (idx[:, None] >= idx[None, :])
        mem = mem0
        outs = []
        for s in range(n_seg):
            xs = x[:, s * seg_len:(s + 1) * seg_len, :].clone()
            h = torch.concatenate([mem, xs], axis=1)
            q = torch.einsum('btd,dhe->bhte', h, Wq)
            k = torch.einsum('btd,dhe->bhte', h, Wk)
            v = torch.einsum('btd,dhe->bhte', h, Wv)
            scores = torch.einsum('bhqe,bhke->bhqk', q, k) * scale
            scores = torch.where(mask[None, None], scores, -1000000000.0)
            p = _softmax(scores, axis=-1)
            o = torch.einsum('bhqk,bhke->bhqe', p, v)
            o = torch.einsum('bhte,hed->btd', o, Wo)
            mem = o[:, :M, :].clone()
            outs.append(o[:, M:, :])
        return torch.concatenate(outs, axis=1)

    def _B_ref(x, w1, b1, w2, b2):
        h = torch.einsum('bsd,df->bsf', x, w1) + b1
        h = _gelu(h)
        return torch.einsum('bsf,fd->bsd', h, w2) + b2
    x = A_x
    g = _sigmoid((x * gate_w).sum(-1, keepdims=True))
    a = _A_ref(x, A_mem0, A_Wq, A_Wk, A_Wv, A_Wo, A_seg_len)
    b = _B_ref(x, B_w1, B_b1, B_w2, B_b2)
    return g * a + (1 - g) * b
```

```cpp
#include <hip/hip_runtime.h>
#include <hip/hip_bf16.h>
#include <stdint.h>
#include <string.h>

typedef unsigned u32;
typedef unsigned short u16;
typedef unsigned long long u64;
typedef u32 v2u __attribute__((ext_vector_type(2)));
typedef u32 v4u __attribute__((ext_vector_type(4)));
typedef u32 v8u __attribute__((ext_vector_type(8)));
typedef float f32x2 __attribute__((ext_vector_type(2)));
typedef float f32x4 __attribute__((ext_vector_type(4)));
typedef __bf16 bf16x2 __attribute__((ext_vector_type(2)));
typedef __bf16 bf16x8 __attribute__((ext_vector_type(8)));
typedef __bf16 bf16x16 __attribute__((ext_vector_type(16)));
typedef short s16x4 __attribute__((ext_vector_type(4)));
typedef short s16x8 __attribute__((ext_vector_type(8)));
typedef __amdgpu_buffer_rsrc_t rsrc_t;
#define LAS __attribute__((address_space(3)))
#define GAS __attribute__((address_space(1)))
#define DEVI __device__ __forceinline__

constexpr int DM = 576, NH = 32, NSEG = 64, FF = 4608, NTILE = 36;
constexpr u32 SENT = 0xFFFFFFFFu;
constexpr int SC1 = 16;
constexpr int CHUNK = 16384;
constexpr int SLOT = 42 * CHUNK;
constexpr int P_OFF = 36 * CHUNK, MEM_OFF = 40 * CHUNK;
constexpr int HINT_S = 41 * CHUNK + 1024, HINT_V = HINT_S + SLOT, HINT_R = HINT_V + SLOT;
constexpr int CTRL = 3 * SLOT;
constexpr int RING_BYTES = 2 << 20;
constexpr int L_TICKET = 0, L_OVF = 8, L_ABORT = 9, L_PAR = 10, L_XP = 16, L_YP = 48, L_SP = 80, L_VP = 112, L_FP = 144;
constexpr int KQ_OFF = RING_BYTES;
constexpr int BZ_OFF = KQ_OFF + NSEG * NH * 16384;
constexpr int CZ_OFF = BZ_OFF + NSEG * NTILE * NH * 1024;
constexpr int PX_OFF = CZ_OFF + NSEG * NTILE * NH * 256;
constexpr int FP_OFF = PX_OFF + NSEG * NH * 2560;
constexpr int G_OFF = FP_OFF + 3 * 2048 * DM * 4;
constexpr int DBG_OFF = G_OFF + 8192;
constexpr int WS_USED = DBG_OFF + (4 << 20);
constexpr int SPIN_CAP = 1 << 18;
constexpr int LDS_BYTES = 112 * 1024;

struct Params {
  const u16 *x, *mem0, *wq, *wk, *wv, *wo, *w1, *b1, *w2, *b2, *gw;
  u16* out;
  unsigned char* ws;
};

DEVI float bf2f(u32 b) { return __uint_as_float(b << 16); }
DEVI u32 pack2(float lo, float hi) { f32x2 v = {lo, hi}; bf16x2 b = __builtin_convertvector(v, bf16x2); return __builtin_bit_cast(u32, b); }
DEVI u32 guard(u32 v) { return v < 0xFFFFFFFEu ? v : 0xFFFFFFFEu; }
DEVI void bar() { asm volatile("s_waitcnt lgkmcnt(0)\n\ts_barrier" ::: "memory"); }
DEVI void drain() { asm volatile("s_waitcnt vmcnt(0)" ::: "memory"); }
DEVI f32x4 smf64(v4u a, v8u b, f32x4 c, int idx) { return __builtin_amdgcn_smfmac_f32_16x16x64_bf16(__builtin_bit_cast(bf16x8, a), __builtin_bit_cast(bf16x16, b), c, idx, 0, 0); }
DEVI f32x4 smf32(v2u a, v4u b, f32x4 c, int idx) { return __builtin_amdgcn_smfmac_f32_16x16x32_bf16(__builtin_bit_cast(s16x4, a), __builtin_bit_cast(s16x8, b), c, idx, 0, 0); }
DEVI f32x4 mfma(v4u a, v4u b, f32x4 c) { return __builtin_amdgcn_mfma_f32_16x16x32_bf16(__builtin_bit_cast(bf16x8, a), __builtin_bit_cast(bf16x8, b), c, 0, 0, 0); }
DEVI v8u cat(v4u a, v4u b) { return __builtin_shufflevector(a, b, 0, 1, 2, 3, 4, 5, 6, 7); }
DEVI v4u lo4(v8u a) { return __builtin_shufflevector(a, a, 0, 1, 2, 3); }
DEVI v4u hi4(v8u a) { return __builtin_shufflevector(a, a, 4, 5, 6, 7); }
DEVI float swapadd(float a, float b) {
  auto r = __builtin_amdgcn_permlane32_swap(__float_as_uint(a), __float_as_uint(b), false, false);
  return __uint_as_float(r[0]) + __uint_as_float(r[1]);
}
template <int CTRL_> DEVI float dpp(float v) { return __uint_as_float(__builtin_amdgcn_update_dpp(0, __float_as_uint(v), CTRL_, 0xF, 0xF, true)); }
DEVI float row16_max(float v) { v = fmaxf(v, dpp<0xB1>(v)); v = fmaxf(v, dpp<0x4E>(v)); v = fmaxf(v, dpp<0x141>(v)); v = fmaxf(v, dpp<0x140>(v)); return v; }
DEVI float row16_sum(float v) { v += dpp<0xB1>(v); v += dpp<0x4E>(v); v += dpp<0x141>(v); v += dpp<0x140>(v); return v; }
DEVI float x16_max(float v) { auto r = __builtin_amdgcn_permlane16_swap(__float_as_uint(v), __float_as_uint(v), false, false); return fmaxf(__uint_as_float(r[0]), __uint_as_float(r[1])); }
DEVI float x32_max(float v) { auto r = __builtin_amdgcn_permlane32_swap(__float_as_uint(v), __float_as_uint(v), false, false); return fmaxf(__uint_as_float(r[0]), __uint_as_float(r[1])); }
DEVI float x16_sum(float v) { auto r = __builtin_amdgcn_permlane16_swap(__float_as_uint(v), __float_as_uint(v), false, false); return __uint_as_float(r[0]) + __uint_as_float(r[1]); }
DEVI float x32_sum(float v) { auto r = __builtin_amdgcn_permlane32_swap(__float_as_uint(v), __float_as_uint(v), false, false); return __uint_as_float(r[0]) + __uint_as_float(r[1]); }
DEVI v4u ld128(rsrc_t r, int vo, int so) { return __builtin_amdgcn_raw_buffer_load_b128(r, vo, so, SC1); }
DEVI v2u ld64(rsrc_t r, int vo, int so) { return __builtin_amdgcn_raw_buffer_load_b64(r, vo, so, SC1); }
DEVI u32 ld32(rsrc_t r, int vo, int so) { return __builtin_amdgcn_raw_buffer_load_b32(r, vo, so, SC1); }
DEVI void st128(v4u v, rsrc_t r, int vo, int so) { __builtin_amdgcn_raw_buffer_store_b128(v, r, vo + so, 0, SC1); }
DEVI void st64(v2u v, rsrc_t r, int vo, int so) { __builtin_amdgcn_raw_buffer_store_b64(v, r, vo, so, SC1); }
DEVI void st32(u32 v, rsrc_t r, int vo, int so) { __builtin_amdgcn_raw_buffer_store_b32(v, r, vo, so, SC1); }
DEVI u32 umax(u32 a, u32 b) { return a > b ? a : b; }
DEVI u32 mx4(v4u v) { return umax(umax(v.x, v.y), umax(v.z, v.w)); }
DEVI int uni(int v) { return __builtin_amdgcn_readfirstlane(v); }
DEVI void fence_c() { asm volatile("" ::: "memory"); }

#define HPACE "1"
#define DCHK "v_max3_u32 %2, v240, v241, v242\n\tv_max3_u32 %3, v244, v245, v246\n\tv_max3_u32 %2, %2, v243, v247\n\tv_max_u32 %2, %2, %3\n\tv_cmp_eq_u32 vcc, -1, %2\n\t"
#define DMORE
#define NOUNROLL _Pragma("clang loop unroll(disable)")
#define SB() __builtin_amdgcn_sched_barrier(0)
constexpr int OOB = 0x7FFFFFF0;
DEVI bool any_sent(u32 m) { return __builtin_amdgcn_ballot_w64(m == SENT) != 0; }
DEVI bool give_up(rsrc_t r, int& rounds) {
  if (++rounds > SPIN_CAP / 256) { st32(0u, r, CTRL + L_ABORT * 128, 0); return true; }
  if (uni(ld32(r, CTRL + L_ABORT * 128, 0)) != (int)SENT) { rounds = SPIN_CAP; return true; }
  return false;
}
#define POLL(BODY)                                                          \
  NOUNROLL for (;;) {                                                       \
    bool bad; int cnt_ = rounds >= SPIN_CAP / 256 ? 0 : 255;                \
    NOUNROLL do { fence_c(); BODY; } while (bad && --cnt_ >= 0);            \
    if (__builtin_expect(!bad, 1)) break;                                   \
    if (give_up(r, rounds)) break;                                          \
  }                                                                         \
  fence_c();
DEVI void hint_wait(rsrc_t r, int vo, int so, int want, int& hv) {
  int cnt = 4096;
  asm volatile(
      "v_mov_b32 %0, -1\n\ts_nop 3\n\t"
      "1:\n\t"
      "buffer_load_dword %0, %2, %3, %4 offen sc1\n\t"
      "s_sleep " HPACE "\n\t"
      "v_cmp_gt_i32 vcc, %5, %0\n\t"
      "s_sub_u32 %1, %1, 1\n\t"
      "s_cbranch_scc1 2f\n\t"
      "s_cbranch_vccnz 1b\n\t"
      "2:"
      : "=&v"(hv), "+s"(cnt)
      : "v"(vo), "s"(r), "s"(so), "s"(want)
      : "memory", "vcc", "scc");
}
DEVI bool data_wait2(rsrc_t r, int o0, int o1, int so, v4u& a, v4u& b) {
  int cnt = 4096;
  u32 t0, t1;
  asm volatile(
      "v_mov_b32 v240, -1\n\tv_mov_b32 v241, -1\n\tv_mov_b32 v242, -1\n\tv_mov_b32 v243, -1\n\t"
      "v_mov_b32 v244, -1\n\tv_mov_b32 v245, -1\n\tv_mov_b32 v246, -1\n\tv_mov_b32 v247, -1\n\t"
      "1:\n\t"
      "buffer_load_dwordx4 v[240:243], %5, %7, %8 offen sc1\n\t"
      "buffer_load_dwordx4 v[244:247], %6, %7, %8 offen sc1\n\t"
      "s_sub_u32 %4, %4, 1\n\t"
      "s_cbranch_scc1 2f\n\t"
      DCHK "s_cbranch_vccz 2f\n\t"
      DCHK "s_cbranch_vccz 2f\n\t"
      DCHK "s_cbranch_vccz 2f\n\t"
      DCHK "s_cbranch_vccz 2f\n\t"
      DCHK "s_cbranch_vccz 2f\n\t"
      DCHK "s_cbranch_vccz 2f\n\t"
      DMORE
      DCHK "s_cbranch_vccnz 1b\n\t"
      "2:"
      : "=&{v[240:243]}"(a), "=&{v[244:247]}"(b), "=&v"(t0), "=&v"(t1), "+s"(cnt)
      : "v"(o0), "v"(o1), "s"(r), "s"(so)
      : "memory", "vcc", "scc");
  return cnt >= 0;
}
DEVI void wait_prog(rsrc_t r, int line0, int n, int want, int lane, int& seen, int& rounds) {
  if (__builtin_amdgcn_ballot_w64(seen < want) != 0) {
    const int vo = CTRL + (line0 + (lane < n ? lane : 0)) * 128;
    POLL(seen = (int)ld32(r, vo, 0); bad = __builtin_amdgcn_ballot_w64(seen < want) != 0; if (bad) __builtin_amdgcn_s_sleep(2));
  }
}

extern __shared__ __attribute__((aligned(16))) unsigned char smem_g[];
#define TS(v)
#define NPOLL
#define PUT(k, v) do {} while (0)
#define DUMP(slot) do {} while (0)
#define SLP_S 16
#define SLP_BG 0
#define EXPER 0
#define DLY_S 2
#define DLY_R 8
#define HINTS 6
#define SLP_R 0

DEVI void load_w1_sparse(const u16* W, int ld, int lane, v8u (&wt)[2][9]) {
  const int n = lane & 15, gb = lane >> 4;
#pragma unroll
  for (int ks = 0; ks < 9; ++ks) {
#pragma unroll
    for (int hf = 0; hf < 2; ++hf) {
#pragma unroll
      for (int t = 0; t < 4; ++t) {
        const int j = 4 * ks + 2 * hf + (gb >> 1);
        const int d = 32 * (j >> 1) + (j & 1) + 2 * (8 * (t & 1) + 2 * (2 * (gb & 1) + (t >> 1)));
        const u32 a = *(const GAS u32*)(W + (size_t)d * ld + 2 * n);
        const u32 b = *(const GAS u32*)(W + (size_t)(d + 2) * ld + 2 * n);
        wt[0][ks][4 * hf + t] = __builtin_amdgcn_perm(b, a, 0x05040100u);
        wt[1][ks][4 * hf + t] = __builtin_amdgcn_perm(b, a, 0x07060302u);
      }
    }
  }
}
DEVI int tile_of(int c, int i) { return i < 8 ? 9 * c + (c & 1) + i : (c & 1 ? 9 * c : 9 * c + 8); }
DEVI void load_w2(const u16* W, int c, int lane, v8u (&wt)[9][2]) {
  const int n = lane & 15, gb = lane >> 4;
  const u32 sel = (c & 1) ? 0x07060302u : 0x05040100u;
#pragma unroll
  for (int ks = 0; ks < 2; ++ks) {
#pragma unroll
    for (int q = 0; q < 8; ++q) {
      const int e = 64 * ks + 32 * (q >> 2) + 8 * gb + 2 * (q & 3);
#pragma unroll
      for (int pr = 0; pr < 5; ++pr) {
        const int col = 32 * (tile_of(c, 2 * pr) >> 1) + 2 * n;
        const u32 a = *(const GAS u32*)(W + (size_t)e * DM + col);
        const u32 b = *(const GAS u32*)(W + (size_t)(e + 1) * DM + col);
        if (pr < 4) {
          wt[2 * pr][ks][q] = __builtin_amdgcn_perm(b, a, 0x05040100u);
          wt[2 * pr + 1][ks][q] = __builtin_amdgcn_perm(b, a, 0x07060302u);
        } else {
          wt[8][ks][q] = __builtin_amdgcn_perm(b, a, sel);
        }
      }
    }
  }
}

constexpr int LM_MEM = 0, LM_Q = 9216, LM_K = LM_Q + 2048, LM_RED = 0, LM_KQ = 16384;

DEVI int wait_parity(rsrc_t r, int& rounds) {
  int v;
  POLL(v = uni(ld32(r, CTRL + L_PAR * 128, 0)); bad = v == (int)SENT; if (bad) __builtin_amdgcn_s_sleep(4));
  return (v & 1) * 8192;
}
DEVI int next_slot(int sb, int par) { return sb + SLOT >= par + 3 * SLOT ? par : sb + SLOT; }

struct MemGather {
  int o0, o1, l0, l1, ho;
  DEVI void init(int wi, int lane) {
    ho = HINT_R + ((lane & 7) < 4 ? 4 * wi + (lane & 3) : 24 + 2 * wi + (lane & 1)) * 128;
    ho = lane < 6 ? ho : OOB;
    const int a = wi * 1024 + lane * 16, b = 6144 + wi * 512 + lane * 16;
    o0 = MEM_OFF + a; l0 = LM_MEM + a;
    o1 = lane < 32 ? MEM_OFF + (b >> 13) * CHUNK + (b & 8191) : OOB;
    l1 = lane < 32 ? LM_MEM + b : LM_KQ - 1024 + lane * 16;
  }
  DEVI void run(rsrc_t r, LAS unsigned char* L, int sb, int par, int s, int lane, int& rounds) {
    v4u a, b;
    NOUNROLL for (;;) {
      if (__builtin_expect(data_wait2(r, o0, o1, sb, a, b), 1)) break;
      rounds += 15;
      if (give_up(r, rounds)) break;
    }
    *(LAS v4u*)(L + l0) = a;
    *(LAS v4u*)(L + l1) = b;
    ka = a; kb = b;
  }
  v4u ka, kb;
  DEVI void release() { asm volatile("s_waitcnt vmcnt(0)" :: "{v[240:243]}"(ka), "{v[244:247]}"(kb) : "memory"); }
};

DEVI f32x4 zero4() { f32x4 z = {0, 0, 0, 0}; asm volatile("" : "+v"(z)); return z; }
template <int RSTEP, class F>
DEVI void stage1(LAS unsigned char* L, const v8u (&wt)[2][9], f32x4 a0, f32x4 a1, int lane, int idx, int woff, F mid) {
  v4u af[9];
#pragma unroll
  for (int ks = 0; ks < 9; ++ks) af[ks] = *(LAS v4u*)(L + LM_MEM + ks * 1024 + lane * 16);
  SB();
#pragma unroll
  for (int ks = 0; ks < 9; ++ks) {
    a0 = smf64(af[ks], wt[0][ks], a0, idx); a1 = smf64(af[ks], wt[1][ks], a1, idx);
    if (ks == 5) { SB(); mid(); SB(); }
  }
  *(LAS u32*)(L + woff) = pack2(a0[0] + a0[1], a1[0] + a1[1]);
  *(LAS u32*)(L + woff + RSTEP) = pack2(a0[2] + a0[3], a1[2] + a1[3]);
}
DEVI int afrag_woff(int wq, int lane) {
  const int n = lane & 15, g = lane >> 4;
  return (wq >> 1) * 1024 + ((2 * (wq & 1) + (n >> 3)) * 16 + 4 * g + (n & 1)) * 16 + ((n & 7) >> 1) * 4;
}
DEVI float row16_max_asm(float v) {
  asm volatile("s_nop 1\n\tv_max_f32_dpp %0, %0, %0 quad_perm:[1,0,3,2] row_mask:0xf bank_mask:0xf\n\t"
               "s_nop 1\n\tv_max_f32_dpp %0, %0, %0 quad_perm:[2,3,0,1] row_mask:0xf bank_mask:0xf\n\t"
               "s_nop 1\n\tv_max_f32_dpp %0, %0, %0 row_half_mirror row_mask:0xf bank_mask:0xf\n\t"
               "s_nop 1\n\tv_max_f32_dpp %0, %0, %0 row_mirror row_mask:0xf bank_mask:0xf"
               : "+v"(v));
  return v;
}

constexpr float SCL = 0.08838834764831845f * 1.4426950408889634f;

DEVI void role_S(const Params& p, rsrc_t r, LAS unsigned char* L, int h) {
  const int lane = threadIdx.x & 63, w = uni(threadIdx.x >> 6), n = lane & 15, g = lane >> 4;
  const int idx = (lane & 1) ? 0xEEEE : 0x4444;
  v8u wt[2][9];
  load_w1_sparse((w < 4 ? p.wq : p.wk) + h * 128 + 32 * (w & 3), 4096, lane, wt);
  int rounds = 0;
  const int par = wait_parity(r, rounds);
  const int woff = w < 4 ? LM_Q + afrag_woff(w, lane) : LM_K + ((w - 4) >> 1) * 1024 + ((n >> 2) * 8 + 2 * g) * 32 + (w & 1) * 16 + (n & 3) * 4;
  MemGather mg; mg.init(w, lane);
  int sb = par;
  if (w < 2) {
    const int pvo = P_OFF + (h >> 3) * CHUNK + (h & 7) * 1024 + w * 512;
    const int pxo = pvo + (n >> 3) * 128 + g * 32 + (n & 7) * 4;
    const int pmo = n < 4 ? pvo + 256 + (n >> 1) * 32 + g * 8 + (n & 1) * 4 : OOB;
    const int cn = (n & 8) | ((n & 3) << 1) | ((n >> 2) & 1);
    const int kmc = ((n & 3) << 1) | ((n >> 2) & 1);
    int sprev = par + 2 * SLOT;
    for (int s = 0; s < NSEG; ++s) {
      const f32x4 z0 = zero4(), z1 = zero4();
      f32x4 sa = zero4(), sbb = zero4(), sm = zero4();
      mg.run(r, L, sb, par, s, lane, rounds);
      TS(t0);
      bar();
      LAS unsigned char* kq = L + LM_KQ + (s & 1) * 16384;
      v4u kf[2][4];
      stage1<32>(L, wt, z0, z1, lane, idx, woff, [&]() {
#pragma unroll
        for (int k = 0; k < 8; ++k) kf[k >> 2][k & 3] = *(LAS v4u*)(kq + k * 1024 + (g * 16 + cn) * 16);
      });
      mg.release();
      TS(t1);
      bar();
      {
        v4u qa[2], km[4];
#pragma unroll
        for (int k = 0; k < 2; ++k) qa[k] = *(LAS v4u*)(L + LM_Q + k * 1024 + (lane ^ (2 * w)) * 16);
#pragma unroll
        for (int k = 0; k < 4; ++k) km[k] = *(LAS v4u*)(L + LM_K + (k >> 1) * 1024 + (g * 8 + kmc) * 32 + (k & 1) * 16);
        SB();
        sa = smf64(qa[0], cat(kf[0][0], kf[0][1]), sa, idx);
        sbb = smf64(qa[0], cat(kf[1][0], kf[1][1]), sbb, idx);
        sa = smf64(qa[1], cat(kf[0][2], kf[0][3]), sa, idx);
        sm = smf64(qa[0], cat(km[0], km[1]), sm, idx);
        sbb = smf64(qa[1], cat(kf[1][2], kf[1][3]), sbb, idx);
        sm = smf64(qa[1], cat(km[2], km[3]), sm, idx);
        SB();
        const float va = sa[0] + sa[1], vb = sbb[0] + sbb[1], vm = sm[0] + sm[1];
        const float mx = row16_max_asm(fmaxf(fmaxf(va, vb), vm));
        const float ea = __builtin_amdgcn_exp2f((va - mx) * SCL), eb = __builtin_amdgcn_exp2f((vb - mx) * SCL), em = __builtin_amdgcn_exp2f((vm - mx) * SCL);
        const float em2 = dpp<0x104>(em);
        const float inv = __builtin_amdgcn_rcpf(row16_sum(ea + eb + (n < 8 ? em : 0.f)));
        st32(guard(pack2(ea * inv, eb * inv)), r, pxo, sb);
        st32(guard(pack2(em * inv, em2 * inv)), r, pmo, sb);
        SB();
        TS(t2);
        PUT(w, t0); PUT(8 + w, t1); PUT(10 + w, t2);
        st32(SENT, r, pxo, sprev); st32(SENT, r, pmo, sprev);
      }
      v4u kp[4], qf[4];
#pragma unroll
      for (int k = 0; k < 8; ++k) kf[k >> 2][k & 3] = *(LAS v4u*)(kq + k * 1024 + lane * 16);
#pragma unroll
      for (int k = 0; k < 4; ++k) qf[k] = *(LAS v4u*)(kq + 8192 + (w * 4 + k) * 1024 + lane * 16);
#pragma unroll
      for (int k = 0; k < 4; ++k) kp[k] = *(LAS v4u*)(L + LM_K + (k >> 1) * 1024 + (g * 8 + (n & 7)) * 32 + (k & 1) * 16);
      f32x4 xa = {0, 0, 0, 0}, xb = {0, 0, 0, 0}, xm = {0, 0, 0, 0};
#pragma unroll
      for (int k = 0; k < 4; ++k) { xa = mfma(kf[0][k], qf[k], xa); xb = mfma(kf[1][k], qf[k], xb); xm = mfma(kp[k], qf[k], xm); }
      const int qi = 2 * n + w;
      float mx = fmaxf(fmaxf(xm[0], xm[1]), fmaxf(xm[2], xm[3]));
#pragma unroll
      for (int k = 0; k < 4; ++k) {
        xa[k] = (8 * g + 2 * k <= qi) ? xa[k] : -INFINITY;
        xb[k] = (8 * g + 2 * k + 1 <= qi) ? xb[k] : -INFINITY;
        mx = fmaxf(mx, fmaxf(xa[k], xb[k]));
      }
      mx = x32_max(x16_max(mx));
      float sum = 0.f, summ = 0.f;
#pragma unroll
      for (int k = 0; k < 4; ++k) {
        xa[k] = __builtin_amdgcn_exp2f((xa[k] - mx) * SCL); xb[k] = __builtin_amdgcn_exp2f((xb[k] - mx) * SCL); xm[k] = __builtin_amdgcn_exp2f((xm[k] - mx) * SCL);
        sum += xa[k] + xb[k]; summ += xm[k];
      }
      sum += g < 2 ? summ : 0.f;
      const float inv = __builtin_amdgcn_rcpf(x32_sum(x16_sum(sum)));
      v4u px; v2u pm2;
#pragma unroll
      for (int k = 0; k < 4; ++k) px[k] = pack2(xa[k] * inv, xb[k] * inv);
      pm2.x = pack2(xm[0] * inv, xm[1] * inv); pm2.y = pack2(xm[2] * inv, xm[3] * inv);
      const int pxb = PX_OFF + ((s * NH + h) * 2 + w) * 1280;
      st128(px, r, lane * 16, pxb);
      st64(pm2, r, g < 2 ? 1024 + n * 16 + g * 8 : OOB, pxb);
      drain();
      sprev = sb;
      sb = next_slot(sb, par);
    }
  } else if (w < 6) {
    for (int s = 0; s < NSEG; ++s) {
      const f32x4 z0 = zero4(), z1 = zero4();
      mg.run(r, L, sb, par, s, lane, rounds);
      TS(t0);
      bar();
      stage1<32>(L, wt, z0, z1, lane, idx, woff, []() {});
      bar();
      mg.release();
      PUT(w, t0);
      __builtin_amdgcn_s_sleep(SLP_S);
      sb = next_slot(sb, par);
    }
  } else {
    int seen = -1;
    v4u t[8];
    const int fo = (w - 6) * 8192 + lane * 16;
    const int hso = (w == 7 && lane == 0) ? HINT_S + h * 128 : OOB;
    auto req = [&](int s) {
      wait_prog(r, L_XP + h, 1, s, lane, seen, rounds);
#pragma unroll
      for (int k = 0; k < 8; ++k) t[k] = ld128(r, fo + k * 1024, KQ_OFF + (s * NH + h) * 16384);
    };
    auto put = [&](int s) {
#pragma unroll
      for (int k = 0; k < 8; ++k) *(LAS v4u*)(L + LM_KQ + (s & 1) * 16384 + fo + k * 1024) = t[k];
    };
    req(0); put(0); req(1);
    for (int s = 0; s < NSEG; ++s) {
      const f32x4 z0 = zero4(), z1 = zero4();
      bar();
      st32(s, r, hso, par);
      stage1<32>(L, wt, z0, z1, lane, idx, woff, []() {});
      bar();
      if (w == 7 && s > 0 && lane == 0) st32(s - 1, r, CTRL + (L_SP + h) * 128, 0);
      __builtin_amdgcn_s_sleep(10);
      put(s + 1);
      req(s + 2 < NSEG ? s + 2 : NSEG - 1);
    }
  }
  bar();
  if (w == 0 && lane == 0) st32(NSEG - 1, r, CTRL + (L_SP + h) * 128, 0);
  DUMP(h);
}

DEVI void role_V(const Params& p, rsrc_t r, LAS unsigned char* L, int h) {
  const int lane = threadIdx.x & 63, w = uni(threadIdx.x >> 6), n = lane & 15, g = lane >> 4;
  const int idx = (lane & 1) ? 0xEEEE : 0x4444;
  int rounds = 0;
  MemGather mg; mg.init(w, lane);
  if (w < 4) {
    v8u wt[2][9];
    load_w1_sparse(p.wv + h * 128 + 32 * w, 4096, lane, wt);
      const int par = wait_parity(r, rounds);
    const int woff = LM_Q + afrag_woff(w, lane);
    int sb = par;
    for (int s = 0; s < NSEG; ++s) {
      const f32x4 z0 = zero4(), z1 = zero4();
      mg.run(r, L, sb, par, s, lane, rounds);
      TS(t0);
      bar();
      stage1<32>(L, wt, z0, z1, lane, idx, woff, []() {});
      TS(t1);
      bar();
      mg.release();
      PUT(w, t0); PUT(8 + (w & 1), t1);
      __builtin_amdgcn_s_sleep(SLP_S);
      if (w == 0 && s > 0 && lane == 0) st32(s - 1, r, CTRL + (L_VP + h) * 128, 0);
      sb = next_slot(sb, par);
    }
  } else {
    const int c = w - 4;
    v8u wt[9][2];
    load_w2(p.wo + (size_t)h * 128 * DM, c, lane, wt);
      const int par = wait_parity(r, rounds);
    const int lo = h * 256 + lane * 4;
    const int hso = (c == 3 && lane == 0) ? HINT_V + h * 128 : OOB;
    int lr[3], lz[3];
#pragma unroll
    for (int q = 0; q < 3; ++q) {
      const int tl = q < 2 ? tile_of(c, 4 * q) + g : tile_of(c, 8);
      const bool on = q < 2 || g == 0;
      lr[q] = on ? tl * CHUNK + h * 256 + n * 16 : OOB;
      lz[q] = on ? tl * 8192 + h * 256 + n * 16 : OOB;
    }
    const int t0o = tile_of(c, 0) * CHUNK, t8o = tile_of(c, 8) * CHUNK;
    int sb = par, sprev = par + 2 * SLOT;
    for (int s = 0; s < NSEG; ++s) {
      f32x4 a[9];
#pragma unroll
      for (int i = 0; i < 9; ++i) a[i] = zero4();
      if (c < 2) { mg.run(r, L, sb, par, s, lane, rounds); bar(); mg.release(); } else bar();
      st32(s, r, hso, par);
      bar();
      v4u va[2];
#pragma unroll
      for (int k = 0; k < 2; ++k) va[k] = *(LAS v4u*)(L + LM_Q + k * 1024 + lane * 16);
      u32 o[9];
      auto fin = [&](int i) {
        o[i] = guard(pack2(a[i][0] + a[i][1], a[i][2] + a[i][3]));
        st32(o[i], r, lo, sb + (i < 8 ? t0o + i * CHUNK : t8o));
      };
      a[0] = smf64(va[0], wt[0][0], a[0], idx);
#pragma unroll
      for (int i = 0; i < 9; ++i) {
        if (i < 8) a[i + 1] = smf64(va[0], wt[i + 1][0], a[i + 1], idx);
        a[i] = smf64(va[1], wt[i][1], a[i], idx);
        if (i >= 2) fin(i - 2);
        SB();
      }
      fin(7); fin(8);
      SB();
      TS(t2);
      PUT(10 + c, t2);
      __builtin_amdgcn_s_sleep(SLP_BG);
#pragma unroll
      for (int q = 0; q < 3; ++q) {
        const u32 u0 = o[4 * q], u1 = o[q < 2 ? 4 * q + 1 : 8], u2 = o[q < 2 ? 4 * q + 2 : 8], u3 = o[q < 2 ? 4 * q + 3 : 8];
        const auto e0 = __builtin_amdgcn_permlane32_swap(u0, u2, false, false);
        const auto e1 = __builtin_amdgcn_permlane32_swap(u1, u3, false, false);
        const auto f0 = __builtin_amdgcn_permlane16_swap(e0[0], e1[0], false, false);
        const auto f1 = __builtin_amdgcn_permlane16_swap(e0[1], e1[1], false, false);
        st128(v4u{f0[0], f0[1], f1[0], f1[1]}, r, lz[q], CZ_OFF + s * NTILE * 8192);
      }
#pragma unroll
      for (int q = 0; q < 3; ++q) st128(v4u{SENT, SENT, SENT, SENT}, r, lr[q], sprev);
      drain();
      sprev = sb;
      sb = next_slot(sb, par);
    }
  }
  bar();
  if (w == 0 && lane == 0) st32(NSEG - 1, r, CTRL + (L_VP + h) * 128, 0);
  DUMP(32 + h);
}

DEVI u64 cyc() { u64 t; asm volatile("s_memtime %0\n\ts_waitcnt lgkmcnt(0)" : "=s"(t)::"memory"); return t; }
DEVI float swapadd16(float a, float b) {
  auto r = __builtin_amdgcn_permlane16_swap(__float_as_uint(a), __float_as_uint(b), false, false);
  return __uint_as_float(r[0]) + __uint_as_float(r[1]);
}

DEVI void role_R(const Params& p, rsrc_t r, LAS unsigned char* L, int j) {
  const int lane = threadIdx.x & 63, w = uni(threadIdx.x >> 6), n = lane & 15, g = lane >> 4;
  const int idx = (lane & 1) ? 0xEEEE : 0x4444;
  int rounds = 0;
  if (j == 0 && w == 0) {
    u32 best[2] = {~0u, ~0u};
    if (lane == 0) {
      for (int it = 0; it < 3; ++it)
        for (int q = 0; q < 2; ++q) {
          GAS u32* a = (GAS u32*)(p.ws + MEM_OFF + CHUNK + q * 8192 + 7168);
          const u64 t0 = cyc();
          u32 v = __hip_atomic_fetch_add(a, 1u, __ATOMIC_RELAXED, __HIP_MEMORY_SCOPE_AGENT);
          asm volatile("s_waitcnt vmcnt(0)" ::"v"(v) : "memory");
          const u32 d = (u32)(cyc() - t0);
          best[q] = d < best[q] ? d : best[q];
        }
      st32(best[1] < best[0] ? 1u : 0u, r, CTRL + L_PAR * 128, 0);
    }
  }
  const int par = wait_parity(r, rounds);
  int pxo[2];
#pragma unroll
  for (int q = 0; q < 2; ++q) { const int h = 4 * w + 2 * q + (g >> 1); pxo[q] = P_OFF + (h >> 3) * CHUNK + (h & 7) * 1024 + (n >> 3) * 512 + (g & 1) * 128 + (n & 7) * 16; }
  int pmo[2];
#pragma unroll
  for (int q = 0; q < 2; ++q) { const int h = 4 * w + 2 * (g >> 1) + q; pmo[q] = P_OFF + (h >> 3) * CHUNK + (h & 7) * 1024 + (n >> 3) * 512 + 256 + (g & 1) * 32 + (n & 7) * 4; }
  const int co = j * CHUNK + w * 1024 + (g >> 1) * 512 + (g & 1) * 128 + n * 4;
  const int mo = MEM_OFF + (j >> 5) * CHUNK + (j & 31) * 256 + lane * 4;
  const int redw = LM_RED + w * 512 + ((g & 1) * 4 + (g >> 1)) * 64 + n * 4;
  const int redr = LM_RED + lane * 8;
  const int hio = (HINTS & 8) ? ((lane < 32 ? HINT_S : HINT_V) + (4 * w + ((lane >> 3) & 3)) * 128) : HINT_S + (4 * w + (lane >> 4)) * 128;
  const int hro = (w == 7 && lane == 0) ? HINT_R + j * 128 : OOB;
  int sb = par, snext = par + SLOT, seen = -1;
  if (w == 7) {
    const u16* src = p.mem0 + (lane >> 3) * DM + 32 * (j >> 1) + (j & 1) + 4 * (lane & 7);
    st32(guard((u32)src[0] | ((u32)src[2] << 16)), r, mo, sb);
  }
  for (int s = 0; s < NSEG - 1; ++s) {
    wait_prog(r, L_YP + 4 * w, 4, s, lane, seen, rounds);
    v4u bz[4];
#pragma unroll
    for (int q = 0; q < 4; ++q) bz[q] = ld128(r, (4 * w + q) * 1024 + lane * 16, BZ_OFF + (s * NTILE + j) * 32768);
    v4u cz, px[2]; v2u pm;
    f32x4 a = zero4(), b = zero4();
    __builtin_amdgcn_s_sleep(SLP_R);
    int hv;
    hint_wait(r, hio, par, s, hv);
    __builtin_amdgcn_s_sleep(DLY_R);
    TS(tf);
    st32(s, r, hro, par);
    POLL(cz.x = ld32(r, co, sb); cz.y = ld32(r, co + 64, sb); cz.z = ld32(r, co + 256, sb); cz.w = ld32(r, co + 320, sb);
         px[0] = ld128(r, pxo[0], sb); px[1] = ld128(r, pxo[1], sb); pm.x = ld32(r, pmo[0], sb); pm.y = ld32(r, pmo[1], sb);
         NPOLL
         bad = any_sent(umax(umax(mx4(cz), mx4(px[0])), umax(mx4(px[1]), umax(pm.x, pm.y)))));
    asm volatile("" :: "v"(hv));
    TS(t0);
    a = smf64(px[0], cat(bz[0], bz[1]), a, idx);
    b = smf32(pm, cz, b, idx);
    a = smf64(px[1], cat(bz[2], bz[3]), a, idx);
    LAS unsigned char* red = L + (s & 1) * 4096;
    *(LAS float*)(red + redw) = (b[0] + b[1]) + (a[0] + a[1]);
    *(LAS float*)(red + redw + 128) = (b[2] + b[3]) + (a[2] + a[3]);
    bar();
    if (w == 7) {
      f32x2 t[8];
#pragma unroll
      for (int q = 0; q < 8; ++q) t[q] = *(LAS f32x2*)(red + redr + q * 512);
      const f32x2 u = ((t[0] + t[1]) + (t[2] + t[3])) + ((t[4] + t[5]) + (t[6] + t[7]));
      st32(guard(pack2(u.x, u.y)), r, mo, snext);
      TS(t2);
      PUT(10, t2);
      st32(SENT, r, mo, sb);
    }
    PUT(w, t0);
    if (w == 7) PUT(14, tf);
    if (w == 3) PUT(15, tf);
    sb = snext;
    snext = next_slot(snext, par);
  }
  DUMP(64 + j);
}

DEVI rsrc_t mk_rsrc(const void* ptr, int bytes) { return __builtin_amdgcn_make_buffer_rsrc((void*)ptr, 0, bytes, 0x00020000); }
DEVI v4u pl128(rsrc_t r, int vo, int so) { return __builtin_amdgcn_raw_buffer_load_b128(r, vo, so, 0); }
DEVI u32 pl32(rsrc_t r, int vo, int so) { return __builtin_amdgcn_raw_buffer_load_b32(r, vo, so, 0); }
DEVI void load_w1_dense(const u16* W, int ld, int lane, v4u (&wt)[2][18]) {
  const int n = lane & 15, gb = lane >> 4;
#pragma unroll
  for (int ks = 0; ks < 18; ++ks) {
#pragma unroll
    for (int t = 0; t < 4; ++t) {
      const int d = 32 * ks + 8 * gb + 2 * t;
      const u32 a = *(const GAS u32*)(W + (size_t)d * ld + 2 * n);
      const u32 b = *(const GAS u32*)(W + (size_t)(d + 1) * ld + 2 * n);
      wt[0][ks][t] = __builtin_amdgcn_perm(b, a, 0x05040100u);
      wt[1][ks][t] = __builtin_amdgcn_perm(b, a, 0x07060302u);
    }
  }
}
DEVI int ximg_rd(int lane) { const int n = lane & 15, g = lane >> 4; return n * 64 + (g ^ ((n >> 3) << 1)) * 16; }
DEVI int ximg_piece(int lane) { return ((lane & 3) ^ ((lane >> 5) << 1)) * 16; }
DEVI void proj32(LAS unsigned char* xb, const v4u (&wt)[2][18], v4u (&o)[2]) {
  f32x4 acc[2][2];
#pragma unroll
  for (int i = 0; i < 4; ++i) acc[i >> 1][i & 1] = f32x4{0, 0, 0, 0};
#pragma unroll
  for (int ks = 0; ks < 18; ++ks) {
#pragma unroll
    for (int m = 0; m < 2; ++m) {
      const v4u xf = *(LAS v4u*)(xb + (m * 18 + ks) * 1024);
      acc[0][m] = mfma(wt[0][ks], xf, acc[0][m]);
      acc[1][m] = mfma(wt[1][ks], xf, acc[1][m]);
    }
  }
#pragma unroll
  for (int m = 0; m < 2; ++m)
#pragma unroll
    for (int q = 0; q < 4; ++q) o[m][q] = pack2(acc[0][m][q], acc[1][m][q]);
}

DEVI void role_X(const Params& p, rsrc_t r, LAS unsigned char* L, int h) {
  const int lane = threadIdx.x & 63, w = uni(threadIdx.x >> 6);
  const rsrc_t rx = mk_rsrc(p.x, 2048 * DM * 2);
  int go[5], lo[5];
#pragma unroll
  for (int k = 0; k < 5; ++k) {
    const int f = w + 8 * k < 36 ? w + 8 * k : w, m = f >= 18, ks = f - 18 * m;
    go[k] = (2 * (lane >> 2) + m) * (DM * 2) + ks * 64 + ximg_piece(lane);
    lo[k] = f * 1024 + lane * 16;
  }
  v4u st[5];
#pragma unroll
  for (int k = 0; k < 5; ++k) st[k] = pl128(rx, go[k], 0);
  v4u wt[2][18];
  load_w1_dense((w < 4 ? p.wk : p.wq) + h * 128 + 32 * (w & 3), 4096, lane, wt);
#pragma unroll
  for (int k = 0; k < 5; ++k) *(LAS v4u*)(L + lo[k]) = st[k];
  bar();
  const int rd = ximg_rd(lane), oo = (w >> 2) * 8192 + (w & 3) * 1024 + lane * 16;
  const int fo = (w == 0 && lane == 0) ? CTRL + (L_XP + h) * 128 : OOB;
  for (int s = 0; s < NSEG; ++s) {
    const int sn = s + 1 < NSEG ? s + 1 : s;
#pragma unroll
    for (int k = 0; k < 5; ++k) st[k] = pl128(rx, go[k], sn * 32 * DM * 2);
    SB();
    v4u o[2];
    proj32(L + (s & 1) * 36864 + rd, wt, o);
    SB();
#pragma unroll
    for (int k = 0; k < 5; ++k) *(LAS v4u*)(L + ((s + 1) & 1) * 36864 + lo[k]) = st[k];
    SB();
    st128(o[0], r, oo, KQ_OFF + (s * NH + h) * 16384);
    st128(o[1], r, oo + 4096, KQ_OFF + (s * NH + h) * 16384);
    if (s < 2 || s == NSEG - 1) { drain(); bar(); st32(s, r, fo, 0); }
    else { asm volatile("s_waitcnt vmcnt(2)" ::: "memory"); bar(); st32(s - 1, r, fo, 0); }
  }
}

DEVI void role_Y(const Params& p, rsrc_t r, LAS unsigned char* L, int h) {
  const int lane = threadIdx.x & 63, w = uni(threadIdx.x >> 6);
  constexpr int VB = 73728;
  if (w < 4) {
    const rsrc_t rx = mk_rsrc(p.x, 2048 * DM * 2);
    const int row = lane >> 2;
    const int go = (8 * (row >> 2) + (row & 3) + 4 * (w >> 1)) * (DM * 2) + (w & 1) * 9 * 64 + ximg_piece(lane);
    const int lo = w * 9 * 1024 + lane * 16;
    v4u st[9];
#pragma unroll
    for (int k = 0; k < 9; ++k) st[k] = pl128(rx, go + k * 64, 0);
    v4u wt[2][18];
    load_w1_dense(p.wv + h * 128 + 32 * w, 4096, lane, wt);
#pragma unroll
    for (int k = 0; k < 9; ++k) *(LAS v4u*)(L + lo + k * 1024) = st[k];
    bar();
    const int rd = ximg_rd(lane);
    const int fo = (w == 0 && lane == 0) ? CTRL + (L_YP + h) * 128 : OOB;
    for (int s = 0; s < NSEG; ++s) {
      const int sn = s + 1 < NSEG ? s + 1 : s;
#pragma unroll
      for (int k = 0; k < 9; ++k) st[k] = pl128(rx, go + k * 64, sn * 32 * DM * 2);
      SB();
      v4u o[2];
      proj32(L + (s & 1) * 36864 + rd, wt, o);
      *(LAS v4u*)(L + VB + (s & 1) * 8192 + w * 1024 + lane * 16) = o[0];
      *(LAS v4u*)(L + VB + (s & 1) * 8192 + 4096 + w * 1024 + lane * 16) = o[1];
      SB();
#pragma unroll
      for (int k = 0; k < 9; ++k) *(LAS v4u*)(L + ((s + 1) & 1) * 36864 + lo + k * 1024) = st[k];
      bar();
      st32(s - 1, r, fo, 0);
    }
    bar();
    st32(NSEG - 1, r, fo, 0);
  } else {
    const int c = w - 4;
    v8u wt[9][2];
    load_w2(p.wo + (size_t)h * 128 * DM, c, lane, wt);
    __builtin_amdgcn_s_setprio(1);
    bar();
    bar();
    const int oo = h * 1024 + lane * 16;
    const int t0o = tile_of(c, 0) * 32768, t8o = tile_of(c, 8) * 32768;
    for (int s = 0; s < NSEG; ++s) {
      v4u vf[2][4];
#pragma unroll
      for (int k = 0; k < 8; ++k) vf[k >> 2][k & 3] = *(LAS v4u*)(L + VB + (s & 1) * 8192 + k * 1024 + lane * 16);
#pragma unroll
      for (int i = 0; i < 9; ++i) {
        f32x4 a0 = {0, 0, 0, 0}, a1 = {0, 0, 0, 0};
#pragma unroll
        for (int k = 0; k < 2; ++k) {
          a0 = mfma(vf[0][2 * k], lo4(wt[i][k]), a0); a1 = mfma(vf[1][2 * k], lo4(wt[i][k]), a1);
          a0 = mfma(vf[0][2 * k + 1], hi4(wt[i][k]), a0); a1 = mfma(vf[1][2 * k + 1], hi4(wt[i][k]), a1);
        }
        st128(v4u{pack2(a0[0], a0[1]), pack2(a0[2], a0[3]), pack2(a1[0], a1[1]), pack2(a1[2], a1[3])}, r, oo,
              BZ_OFF + s * NTILE * 32768 + (i < 8 ? t0o + i * 32768 : t8o));
      }
      drain();
      bar();
    }
  }
}

DEVI float gelu_tanh(float x) {
  const float e = __builtin_amdgcn_exp2f(x * (-2.302208198f - 0.1029432397f * x * x));
  return x * __builtin_amdgcn_rcpf(1.f + e);
}

DEVI void role_F(const Params& p, rsrc_t r, LAS unsigned char* L, int tt, int sp, int c0, int c1) {
  const int lane = threadIdx.x & 63, w = uni(threadIdx.x >> 6), n = lane & 15, g = lane >> 4;
  constexpr int HB = 73728;
  const rsrc_t rx = mk_rsrc(p.x, 2048 * DM * 2);
  {
    const int go = (tt * 64 + (w >> 1) * 16 + (lane >> 2)) * (DM * 2) + (w & 1) * 9 * 64 + ximg_piece(lane);
    v4u st[9];
#pragma unroll
    for (int k = 0; k < 9; ++k) st[k] = pl128(rx, go + k * 64, 0);
#pragma unroll
    for (int k = 0; k < 9; ++k) *(LAS v4u*)(L + (w * 9 + k) * 1024 + lane * 16) = st[k];
  }
  if (w < 4) {
    const rsrc_t rw = mk_rsrc(p.w1, DM * FF * 2), rb = mk_rsrc(p.b1, FF * 2);
    const int vo = (8 * g * FF + 32 * w + 2 * n) * 2;
    constexpr int WIN = 6;
    u32 raw[WIN][8];
    auto req = [&](int slot, int c, int ks) {
#pragma unroll
      for (int q = 0; q < 8; ++q) raw[slot][q] = pl32(rw, vo, ((32 * ks + q) * FF + 128 * c) * 2);
    };
#pragma unroll
    for (int k = 0; k < WIN - 1; ++k) req(k, c0, k);
    bar();
    const int rd = ximg_rd(lane);
    for (int c = c0; c < c1; ++c) {
      const int cn = c + 1 < c1 ? c + 1 : c;
      const v4u bias = pl128(rb, (32 * w + 8 * g) * 2, c * 256);
      f32x4 acc[2][4];
#pragma unroll
      for (int i = 0; i < 8; ++i) acc[i >> 2][i & 3] = f32x4{0, 0, 0, 0};
#pragma unroll
      for (int ks = 0; ks < 18; ++ks) {
        const int kn = ks + WIN - 1;
        req(kn % WIN, kn < 18 ? c : cn, kn % 18);
        v4u xf[4], a0, a1;
#pragma unroll
        for (int m = 0; m < 4; ++m) xf[m] = *(LAS v4u*)(L + (m * 18 + ks) * 1024 + rd);
#pragma unroll
        for (int t = 0; t < 4; ++t) {
          a0[t] = __builtin_amdgcn_perm(raw[ks % WIN][2 * t + 1], raw[ks % WIN][2 * t], 0x05040100u);
          a1[t] = __builtin_amdgcn_perm(raw[ks % WIN][2 * t + 1], raw[ks % WIN][2 * t], 0x07060302u);
        }
#pragma unroll
        for (int m = 0; m < 4; ++m) { acc[0][m] = mfma(a0, xf[m], acc[0][m]); acc[1][m] = mfma(a1, xf[m], acc[1][m]); }
        SB();
      }
#pragma unroll
      for (int m = 0; m < 4; ++m) {
        v4u o;
#pragma unroll
        for (int q = 0; q < 4; ++q)
          o[q] = pack2(gelu_tanh(acc[0][m][q] + bf2f(bias[q] & 0xFFFFu)), gelu_tanh(acc[1][m][q] + __uint_as_float(bias[q] & 0xFFFF0000u)));
        *(LAS v4u*)(L + HB + ((c - c0) & 1) * 16384 + (m * 4 + w) * 1024 + lane * 16) = o;
      }
      bar();
    }
    if (sp == 0) {
      const rsrc_t rg = mk_rsrc(p.gw, DM * 2);
      const int o = lane < 36 ? lane * 32 : OOB;
      const v4u g0 = pl128(rg, o, 0), g1 = pl128(rg, o + 16, 0);
      float mine = 0.f;
      for (int t = 0; t < 16; ++t) {
        const v4u x0 = pl128(rx, o, (tt * 64 + w * 16 + t) * DM * 2), x1 = pl128(rx, o + 16, (tt * 64 + w * 16 + t) * DM * 2);
        float a = 0.f;
#pragma unroll
        for (int q = 0; q < 4; ++q) {
          a += bf2f(x0[q] & 0xFFFFu) * bf2f(g0[q] & 0xFFFFu) + __uint_as_float(x0[q] & 0xFFFF0000u) * __uint_as_float(g0[q] & 0xFFFF0000u);
          a += bf2f(x1[q] & 0xFFFFu) * bf2f(g1[q] & 0xFFFFu) + __uint_as_float(x1[q] & 0xFFFF0000u) * __uint_as_float(g1[q] & 0xFFFF0000u);
        }
        a = x32_sum(x16_sum(row16_sum(a)));
        mine = lane == t ? a : mine;
      }
      const float gv = __builtin_amdgcn_rcpf(1.f + __builtin_amdgcn_exp2f(-1.4426950408889634f * mine));
      st32(__float_as_uint(gv), r, lane < 16 ? G_OFF + (tt * 64 + w * 16 + lane) * 4 : OOB, 0);
    }
  } else {
    const int c4 = w - 4;
    const rsrc_t rw = mk_rsrc(p.w2, DM * FF * 2);
    const int vo = (8 * g * DM + 144 * c4 + 8 * n) * 2, vo9 = (8 * g * DM + 144 * c4 + 128 + n) * 2;
    v4u raw[8]; u32 r9[8];
    auto req = [&](int k) {
#pragma unroll
      for (int q = 0; q < 8; ++q) raw[q] = pl128(rw, vo, (32 * k + q) * DM * 2);
#pragma unroll
      for (int q = 0; q < 8; ++q) r9[q] = __builtin_amdgcn_raw_buffer_load_b16(rw, vo9, (32 * k + q) * DM * 2, 0);
    };
    req(4 * c0);
    f32x4 acc[9][4];
#pragma unroll
    for (int i = 0; i < 36; ++i) acc[i >> 2][i & 3] = f32x4{0, 0, 0, 0};
    bar();
    bar();
    for (int c = c0; c < c1; ++c) {
#pragma unroll
      for (int ks = 0; ks < 4; ++ks) {
        v4u b[9];
#pragma unroll
        for (int t = 0; t < 8; ++t)
#pragma unroll
          for (int q = 0; q < 4; ++q) b[t][q] = __builtin_amdgcn_perm(raw[2 * q + 1][t >> 1], raw[2 * q][t >> 1], (t & 1) ? 0x07060302u : 0x05040100u);
#pragma unroll
        for (int q = 0; q < 4; ++q) b[8][q] = __builtin_amdgcn_perm(r9[2 * q + 1], r9[2 * q], 0x05040100u);
        SB();
        const int kn = 4 * c + ks + 1;
        req(kn < 4 * c1 ? kn : kn - 1);
        SB();
#pragma unroll
        for (int m = 0; m < 4; ++m) {
          const v4u hf = *(LAS v4u*)(L + HB + ((c - c0) & 1) * 16384 + (m * 4 + ks) * 1024 + lane * 16);
#pragma unroll
          for (int t = 0; t < 9; ++t) acc[t][m] = mfma(hf, b[t], acc[t][m]);
        }
        SB();
      }
      if (c + 1 < c1) bar();
    }
    const int oo = FP_OFF + ((sp * 2048 + tt * 64 + 4 * g) * DM + 144 * c4) * 4;
#pragma unroll
    for (int m = 0; m < 4; ++m)
#pragma unroll
      for (int q = 0; q < 4; ++q) {
        const int ro = (16 * m + q) * DM * 4;
        st128(v4u{__float_as_uint(acc[0][m][q]), __float_as_uint(acc[1][m][q]), __float_as_uint(acc[2][m][q]), __float_as_uint(acc[3][m][q])}, r, oo + n * 32, ro);
        st128(v4u{__float_as_uint(acc[4][m][q]), __float_as_uint(acc[5][m][q]), __float_as_uint(acc[6][m][q]), __float_as_uint(acc[7][m][q])}, r, oo + n * 32 + 16, ro);
        st32(__float_as_uint(acc[8][m][q]), r, oo + 512 + n * 4, ro);
      }
  }
  drain();
  bar();
  if (threadIdx.x == 0) st32(0u, r, CTRL + (L_FP + tt * 3 + sp) * 128, 0);
}

#define NEARW 1
DEVI int n_split(int tt) { return (NEARW && tt < 28) ? 3 : 2; }

DEVI void role_O(const Params& p, rsrc_t r, LAS unsigned char* L, int cg, int s0, int ds) {
  const int lane = threadIdx.x & 63, w = uni(threadIdx.x >> 6), n = lane & 15, g = lane >> 4;
  int rounds = 0, seenY = -1, seenS = -1;
  const int fm = w & 1, ftp = (w >> 1) & 1, frh = w >> 2;
  const int col = 64 * cg + 32 * ftp + 2 * n;
  const u32 b2 = *(const GAS u32*)(p.b2 + col);
  const int svo = CTRL + ((lane & 4 ? L_VP : L_SP) + 4 * w + (lane & 3)) * 128;
  for (int s = s0; s < NSEG; s += ds) {
    wait_prog(r, L_YP + 4 * w, 4, s, lane, seenY, rounds);
    v4u bz[4][4];
#pragma unroll
    for (int t = 0; t < 4; ++t)
#pragma unroll
      for (int q = 0; q < 4; ++q) bz[t][q] = ld128(r, (4 * w + q) * 1024 + lane * 16, BZ_OFF + (s * NTILE + 4 * cg + t) * 32768);
    const int tt = s >> 1, ns = n_split(tt);
    int seenF = -1;
    wait_prog(r, L_FP + tt * 3, ns, 0, lane, seenF, rounds);
    const int tok = s * 32 + 2 * (4 * g + 2 * frh) + fm;
    f32x2 fb[2];
    float gt[2];
#pragma unroll
    for (int q = 0; q < 2; ++q) {
      const int o = FP_OFF + ((tok + 2 * q) * DM + col) * 4;
      const v2u u0 = ld64(r, o, 0), u1 = ld64(r, o, 2048 * DM * 4), u2 = ld64(r, ns == 3 ? o : OOB, 2 * 2048 * DM * 4);
      fb[q] = __builtin_bit_cast(f32x2, u0) + __builtin_bit_cast(f32x2, u1) + __builtin_bit_cast(f32x2, u2);
      gt[q] = __uint_as_float(ld32(r, G_OFF + (tok + 2 * q) * 4, 0));
    }
    if (__builtin_amdgcn_ballot_w64(seenS < s) != 0) {
      POLL(seenS = (int)ld32(r, svo, 0); bad = __builtin_amdgcn_ballot_w64(seenS < s) != 0; if (bad) __builtin_amdgcn_s_sleep(4));
    }
    v4u px[2][4], pm[2], cz[4];
#pragma unroll
    for (int m = 0; m < 2; ++m) {
#pragma unroll
      for (int q = 0; q < 4; ++q) px[m][q] = ld128(r, ((4 * w + q) * 2 + m) * 1280 + lane * 16, PX_OFF + s * NH * 2560);
      pm[m] = ld128(r, ((4 * w + g) * 2 + m) * 1280 + 1024 + n * 16, PX_OFF + s * NH * 2560);
    }
#pragma unroll
    for (int t = 0; t < 4; ++t) cz[t] = ld128(r, w * 1024 + lane * 16, CZ_OFF + (s * NTILE + 4 * cg + t) * 8192);
#pragma unroll
    for (int t = 0; t < 4; ++t)
#pragma unroll
      for (int m = 0; m < 2; ++m) {
        f32x4 a = {0, 0, 0, 0};
#pragma unroll
        for (int q = 0; q < 4; ++q) a = mfma(px[m][q], bz[t][q], a);
        a = mfma(pm[m], cz[t], a);
        *(LAS f32x4*)(L + w * 8192 + (t * 2 + m) * 1024 + lane * 16) = a;
      }
    bar();
    f32x2 e = {0, 0}, o = {0, 0};
#pragma unroll
    for (int q = 0; q < 8; ++q) {
      e += *(LAS f32x2*)(L + q * 8192 + (4 * ftp + fm) * 1024 + lane * 16 + frh * 8);
      o += *(LAS f32x2*)(L + q * 8192 + (4 * ftp + 2 + fm) * 1024 + lane * 16 + frh * 8);
    }
#pragma unroll
    for (int q = 0; q < 2; ++q) {
      const float y0 = gt[q] * e[q] + (1.f - gt[q]) * (fb[q].x + bf2f(b2 & 0xFFFFu));
      const float y1 = gt[q] * o[q] + (1.f - gt[q]) * (fb[q].y + __uint_as_float(b2 & 0xFFFF0000u));
      *(GAS u32*)(p.out + (tok + 2 * q) * DM + col) = pack2(y0, y1);
    }
    bar();
  }
}

DEVI int get_role(rsrc_t r, const Params& p, LAS unsigned char* L) {
  if (threadIdx.x == 0) {
    const int xcc = __builtin_amdgcn_s_getreg(20 | (3 << 11)) & 7;
    GAS u32* ctl = (GAS u32*)(p.ws + CTRL);
    int t = (int)__hip_atomic_fetch_add(ctl + (L_TICKET + xcc) * 32, 1u, __ATOMIC_RELAXED, __HIP_MEMORY_SCOPE_AGENT) + 1;
    int role = xcc * 32 + t;
    if (t >= 32) {
      int o = (int)__hip_atomic_fetch_add(ctl + L_OVF * 32, 1u, __ATOMIC_RELAXED, __HIP_MEMORY_SCOPE_AGENT) + 1;
      role = -1;
      for (int spins = 0; spins < SPIN_CAP && role < 0; ++spins) {
        int tot = 0, k = o, cand = -1;
        for (int x = 0; x < 8; ++x) {
          const int c = (int)__hip_atomic_load(ctl + (L_TICKET + x) * 32, __ATOMIC_RELAXED, __HIP_MEMORY_SCOPE_AGENT) + 1;
          tot += c;
          const int fr = c < 32 ? 32 - c : 0;
          if (cand < 0 && k < fr) cand = x * 32 + c + k;
          k -= fr;
        }
        if (tot == 256) role = cand;
        else __builtin_amdgcn_s_sleep(8);
      }
    }
    *(LAS int*)(L + LDS_BYTES - 16) = role;
  }
  __syncthreads();
  return uni(*(LAS int*)(L + LDS_BYTES - 16));
}

__global__ void __launch_bounds__(512, 2) chain_kernel(Params p) {
  LAS unsigned char* L = (LAS unsigned char*)smem_g;
  rsrc_t r = __builtin_amdgcn_make_buffer_rsrc(p.ws, 0, WS_USED, 0x00020000);
  const int role = get_role(r, p, L);
  const int x = (role >> 5) & 3, t = role & 31;
  int wi = -1;
  if (role >= 128) {
    if (t < 8) role_S(p, r, L, x * 8 + t);
    else if (t < 16) role_V(p, r, L, x * 8 + t - 8);
    else if (t < 25) role_R(p, r, L, x * 9 + t - 16);
    else wi = 64 + x * 7 + t - 25;
  } else if (role >= 0) {
    if (t < 8) role_X(p, r, L, x * 8 + t);
    else if (t < 16) role_Y(p, r, L, x * 8 + t - 8);
    else wi = x * 16 + t - 16;
  }
  if (wi >= 0 && (NEARW || wi < 64)) {
    const int tt = wi & 31, sp = wi >> 5, ns = n_split(tt), per = 36 / ns;
    role_F(p, r, L, tt, sp, sp * per, sp * per + per);
    constexpr int NG = NEARW ? 10 : 7;
    if (wi < NG * 9) role_O(p, r, L, wi % 9, wi / 9, NG);
  }
}

extern "C" void kernel_launch(void* const* d_in, const int* in_sizes, int n_in, void* d_out, int out_size, void* d_ws, size_t ws_size, hipStream_t stream) {
  Params p;
  p.x = (const u16*)d_in[0]; p.mem0 = (const u16*)d_in[1]; p.wq = (const u16*)d_in[2]; p.wk = (const u16*)d_in[3]; p.wv = (const u16*)d_in[4]; p.wo = (const u16*)d_in[5];
  p.w1 = (const u16*)d_in[8]; p.b1 = (const u16*)d_in[9]; p.w2 = (const u16*)d_in[10]; p.b2 = (const u16*)d_in[11]; p.gw = (const u16*)d_in[12];
  p.out = (u16*)d_out;
  p.ws = (unsigned char*)(((uintptr_t)d_ws + RING_BYTES - 1) & ~(uintptr_t)(RING_BYTES - 1));
  hipFuncSetAttribute((const void*)chain_kernel, hipFuncAttributeMaxDynamicSharedMemorySize, LDS_BYTES);
  hipMemsetAsync(p.ws, 0xFF, RING_BYTES, stream);
  chain_kernel<<<256, 512, LDS_BYTES, stream>>>(p);
}
```

```cpp
#include <hip/hip_runtime.h>
#include <hip/hip_bf16.h>
#include <stdint.h>
#include <string.h>

typedef unsigned u32;
typedef unsigned short u16;
typedef unsigned long long u64;
typedef u32 v2u __attribute__((ext_vector_type(2)));
typedef u32 v4u __attribute__((ext_vector_type(4)));
typedef u32 v8u __attribute__((ext_vector_type(8)));
typedef float f32x2 __attribute__((ext_vector_type(2)));
typedef float f32x4 __attribute__((ext_vector_type(4)));
typedef __bf16 bf16x2 __attribute__((ext_vector_type(2)));
typedef __bf16 bf16x8 __attribute__((ext_vector_type(8)));
typedef __bf16 bf16x16 __attribute__((ext_vector_type(16)));
typedef short s16x4 __attribute__((ext_vector_type(4)));
typedef short s16x8 __attribute__((ext_vector_type(8)));
typedef __amdgpu_buffer_rsrc_t rsrc_t;
#define LAS __attribute__((address_space(3)))
#define GAS __attribute__((address_space(1)))
#define DEVI __device__ __forceinline__

constexpr int DM = 576, NH = 32, NSEG = 64, FF = 4608, NTILE = 36;
constexpr u32 SENT = 0xFFFFFFFFu;
constexpr int SC1 = 16;
constexpr int CHUNK = 16384;
constexpr int SLOT = 42 * CHUNK;
constexpr int P_OFF = 36 * CHUNK, MEM_OFF = 40 * CHUNK;
constexpr int T_HS = 41 * CHUNK + 1024, T_HV = T_HS + SLOT;
constexpr int RING_BYTES = 2 << 20;
constexpr int CTRL = 3 * SLOT, INIT_BYTES = CTRL + 65536;
constexpr int L_TICKET = 0, L_OVF = 8, L_ABORT = 9, L_PAR = 10, L_SP = 16, L_VP = 48, L_TP = 80, L_XP = 128, L_YP = 192, L_FP = 256;
constexpr int KQ_OFF = RING_BYTES + 65536;
constexpr int BZ_OFF = KQ_OFF + NSEG * NH * 16384;
constexpr int CZ_OFF = BZ_OFF + NSEG * NTILE * NH * 1024;
constexpr int PX_OFF = CZ_OFF + NSEG * NTILE * NH * 256;
constexpr int FP_OFF = PX_OFF + NSEG * NH * 2560;
constexpr int W1T_OFF = FP_OFF + 4 * 2048 * DM * 4;
constexpr int W2T_OFF = W1T_OFF + DM * FF * 2;
constexpr int G_OFF = W2T_OFF + DM * FF * 2;
constexpr int DBG_OFF = G_OFF + 8192;
constexpr int WS_USED = DBG_OFF + (4 << 20);
constexpr int SPIN_CAP = 1 << 18;
constexpr int LDS_BYTES = 112 * 1024;

struct Params {
  const u16 *x, *mem0, *wq, *wk, *wv, *wo, *w1, *b1, *w2, *b2, *gw;
  u16* out;
  unsigned char* ws;
};

DEVI float bf2f(u32 b) { return __uint_as_float(b << 16); }
DEVI u32 pack2(float lo, float hi) { f32x2 v = {lo, hi}; bf16x2 b = __builtin_convertvector(v, bf16x2); return __builtin_bit_cast(u32, b); }
DEVI u32 guard(u32 v) { return v < 0xFFFFFFFEu ? v : 0xFFFFFFFEu; }
DEVI void bar() { asm volatile("s_waitcnt lgkmcnt(0)\n\ts_barrier" ::: "memory"); }
DEVI void drain() { asm volatile("s_waitcnt vmcnt(0)" ::: "memory"); }
DEVI f32x4 smf64(v4u a, v8u b, f32x4 c, int idx) { return __builtin_amdgcn_smfmac_f32_16x16x64_bf16(__builtin_bit_cast(bf16x8, a), __builtin_bit_cast(bf16x16, b), c, idx, 0, 0); }
DEVI f32x4 smf32(v2u a, v4u b, f32x4 c, int idx) { return __builtin_amdgcn_smfmac_f32_16x16x32_bf16(__builtin_bit_cast(s16x4, a), __builtin_bit_cast(s16x8, b), c, idx, 0, 0); }
DEVI f32x4 mfma(v4u a, v4u b, f32x4 c) { return __builtin_amdgcn_mfma_f32_16x16x32_bf16(__builtin_bit_cast(bf16x8, a), __builtin_bit_cast(bf16x8, b), c, 0, 0, 0); }
DEVI v8u cat(v4u a, v4u b) { return __builtin_shufflevector(a, b, 0, 1, 2, 3, 4, 5, 6, 7); }
DEVI v4u lo4(v8u a) { return __builtin_shufflevector(a, a, 0, 1, 2, 3); }
DEVI v4u hi4(v8u a) { return __builtin_shufflevector(a, a, 4, 5, 6, 7); }
DEVI float swapadd(float a, float b) {
  auto r = __builtin_amdgcn_permlane32_swap(__float_as_uint(a), __float_as_uint(b), false, false);
  return __uint_as_float(r[0]) + __uint_as_float(r[1]);
}
template <int CTRL_> DEVI float dpp(float v) { return __uint_as_float(__builtin_amdgcn_update_dpp(0, __float_as_uint(v), CTRL_, 0xF, 0xF, true)); }
DEVI float row16_max(float v) { v = fmaxf(v, dpp<0xB1>(v)); v = fmaxf(v, dpp<0x4E>(v)); v = fmaxf(v, dpp<0x141>(v)); v = fmaxf(v, dpp<0x140>(v)); return v; }
DEVI float row16_sum(float v) { v += dpp<0xB1>(v); v += dpp<0x4E>(v); v += dpp<0x141>(v); v += dpp<0x140>(v); return v; }
DEVI float x16_max(float v) { auto r = __builtin_amdgcn_permlane16_swap(__float_as_uint(v), __float_as_uint(v), false, false); return fmaxf(__uint_as_float(r[0]), __uint_as_float(r[1])); }
DEVI float x32_max(float v) { auto r = __builtin_amdgcn_permlane32_swap(__float_as_uint(v), __float_as_uint(v), false, false); return fmaxf(__uint_as_float(r[0]), __uint_as_float(r[1])); }
DEVI float x16_sum(float v) { auto r = __builtin_amdgcn_permlane16_swap(__float_as_uint(v), __float_as_uint(v), false, false); return __uint_as_float(r[0]) + __uint_as_float(r[1]); }
DEVI float x32_sum(float v) { auto r = __builtin_amdgcn_permlane32_swap(__float_as_uint(v), __float_as_uint(v), false, false); return __uint_as_float(r[0]) + __uint_as_float(r[1]); }
DEVI v4u ld128(rsrc_t r, int vo, int so) { return __builtin_amdgcn_raw_buffer_load_b128(r, vo, so, SC1); }
DEVI v2u ld64(rsrc_t r, int vo, int so) { return __builtin_amdgcn_raw_buffer_load_b64(r, vo, so, SC1); }
DEVI u32 ld32(rsrc_t r, int vo, int so) { return __builtin_amdgcn_raw_buffer_load_b32(r, vo, so, SC1); }
DEVI void st128(v4u v, rsrc_t r, int vo, int so) { __builtin_amdgcn_raw_buffer_store_b128(v, r, vo + so, 0, SC1); }
DEVI void st64(v2u v, rsrc_t r, int vo, int so) { __builtin_amdgcn_raw_buffer_store_b64(v, r, vo, so, SC1); }
DEVI void st32(u32 v, rsrc_t r, int vo, int so) { __builtin_amdgcn_raw_buffer_store_b32(v, r, vo, so, SC1); }
DEVI rsrc_t mk_rsrc(const void* ptr, int bytes) { return __builtin_amdgcn_make_buffer_rsrc((void*)ptr, 0, bytes, 0x00020000); }
DEVI v4u pl128(rsrc_t r, int vo, int so) { return __builtin_amdgcn_raw_buffer_load_b128(r, vo, so, 0); }
DEVI u32 pl32(rsrc_t r, int vo, int so) { return __builtin_amdgcn_raw_buffer_load_b32(r, vo, so, 0); }
DEVI u32 umax(u32 a, u32 b) { return a > b ? a : b; }
DEVI u32 mx4(v4u v) { return umax(umax(v.x, v.y), umax(v.z, v.w)); }
DEVI int uni(int v) { return __builtin_amdgcn_readfirstlane(v); }
DEVI int opaque(int v) { asm volatile("" : "+v"(v)); return v; }
DEVI void fence_c() { asm volatile("" ::: "memory"); }

#define XSLP 4
#define LATE 4
#define FSLP 9
#define PRIO0 1
#define EXT_UP 6
#define HPACE "1"
#define PSLP 12
#define RSLP 28
#define MAR_S 4
#define MAR_R 8
#define PEN 15
#define TRK_UP 8
#define TRK_SH 1
#define NOUNROLL _Pragma("clang loop unroll(disable)")
#define SB() __builtin_amdgcn_sched_barrier(0)
constexpr int OOB = 0x7FFFFFF0;
DEVI bool any_sent(u32 m) { return __builtin_amdgcn_ballot_w64(m == SENT) != 0; }
DEVI bool give_up(rsrc_t r, int& rounds) {
  if (++rounds > SPIN_CAP / 256) { st32(0u, r, CTRL + L_ABORT * 128, 0); return true; }
  if (uni(ld32(r, CTRL + L_ABORT * 128, 0)) != (int)SENT) { rounds = SPIN_CAP; return true; }
  return false;
}
#define POLL(BODY)                                                                                   \
  NOUNROLL for (int cnt_ = 1;; ++cnt_) {                                                             \
    bool bad;                                                                                        \
    fence_c();                                                                                       \
    BODY;                                                                                            \
    if (__builtin_expect(!bad, 1)) break;                                                            \
    if (((cnt_ & 255) == 0 || rounds >= SPIN_CAP / 256) && give_up(r, rounds)) break;                \
  }                                                                                                  \
  fence_c();
DEVI u32 rt7() { return (u32)__builtin_amdgcn_s_memrealtime() << 7; }
#define HC1 "v_and_b32 %1, 127, %0\n\tv_cmp_ne_u32 vcc, %6, %1\n\ts_cbranch_vccz 2f\n\t"
#define HC4 HC1 HC1 HC1 HC1
#define HCAD HC4 HC4 HC4 HC4
DEVI u32 hint_wait(rsrc_t r, int vo, int so, int step) {
  int cnt = 1024;
  u32 hv, t;
  asm volatile(
      "v_mov_b32 %0, -1\n\ts_nop 3\n\t"
      "1:\n\t"
      "buffer_load_dword %0, %3, %4, %5 offen sc1\n\t"
      HCAD
      "s_sub_u32 %2, %2, 1\n\t"
      "s_cbranch_scc0 1b\n\t"
      "2:"
      : "=&v"(hv), "=&v"(t), "+s"(cnt)
      : "v"(vo), "s"(r), "s"(so), "s"(step)
      : "memory", "vcc", "scc");
  return hv;
}
template <int HI>
DEVI void time_wait(u32 tv) {
  int cnt = 64;
  u32 t;
  u64 now;
  asm volatile(
      "1:\n\t"
      "s_memrealtime s[96:97]\n\t"
      "s_sub_u32 %1, %1, 1\n\t"
      "s_cbranch_scc1 2f\n\t"
      "s_waitcnt lgkmcnt(0)\n\t"
      "s_lshl_b32 s96, s96, 7\n\t"
      "v_sub_u32 %0, s96, %3\n\t"
      "v_cmp_gt_i32 s[96:97], 0, %0\n\t"
      "s_cmp_lg_u32 s%c4, 0\n\t"
      "s_cbranch_scc1 1b\n\t"
      "2:\n\t"
      "s_waitcnt lgkmcnt(0)"
      : "=&v"(t), "+s"(cnt), "=&{s[96:97]}"(now)
      : "v"(tv), "i"(96 + HI)
      : "memory", "scc");
}
DEVI int track(int dur, int m) { return (m > 0 && m < (200 << 7)) ? dur + ((m - dur) >> 2) : dur; }
#define DCHK "v_max3_u32 %2, v240, v241, v242\n\tv_max3_u32 %3, v244, v245, v246\n\tv_max3_u32 %2, %2, v243, v247\n\tv_max_u32 %2, %2, %3\n\tv_cmp_eq_u32 vcc, -1, %2\n\t"
#define DC1 DCHK "s_cbranch_vccz 2f\n\t"
#define DC2 DC1 DC1
#define DCAD DC2 DC2 DC2
template <int TWO>
DEVI bool data_wait(rsrc_t r, int vo, int so, v4u& a, v4u& b) {
  int cnt = 4096;
  u32 t0, t1;
  asm volatile(
      "v_mov_b32 v240, -1\n\tv_mov_b32 v241, -1\n\tv_mov_b32 v242, -1\n\tv_mov_b32 v243, -1\n\t"
      "v_mov_b32 v244, %8\n\tv_mov_b32 v245, %8\n\tv_mov_b32 v246, %8\n\tv_mov_b32 v247, %8\n\t"
      "1:\n\t"
      "buffer_load_dwordx4 v[240:243], %5, %6, %7 offen sc1\n\t"
      ".if %8\n\t"
      "buffer_load_dwordx4 v[244:247], %5, %6, %7 offen offset:1024 sc1\n\t"
      ".endif\n\t"
      "s_sub_u32 %4, %4, 1\n\t"
      "s_cbranch_scc1 2f\n\t"
      DCAD
      DCHK "s_cbranch_vccnz 1b\n\t"
      "2:"
      : "=&{v[240:243]}"(a), "=&{v[244:247]}"(b), "=&v"(t0), "=&v"(t1), "+s"(cnt)
      : "v"(vo), "s"(r), "s"(so), "i"(-TWO)
      : "memory", "vcc", "scc");
  return cnt >= 0;
}
DEVI void wait_prog(rsrc_t r, int line0, int n, int want, int lane, int& seen, int& rounds) {
  if (__builtin_amdgcn_ballot_w64(seen < want) != 0) {
    const int vo = CTRL + (line0 + (lane < n ? lane : 0)) * 128;
    POLL(seen = (int)ld32(r, vo, 0); bad = __builtin_amdgcn_ballot_w64(seen < want) != 0; if (bad) __builtin_amdgcn_s_sleep(2));
  }
}

DEVI void wait_prog2(rsrc_t r, int line0, int n, int want, int lane, int& seen0, int& seen1, int& rounds) {
  int sn = (want & 1) ? seen1 : seen0;
  wait_prog(r, line0 + (want & 1) * 32, n, want, lane, sn, rounds);
  if (want & 1) seen1 = sn; else seen0 = sn;
}
extern __shared__ __attribute__((aligned(16))) unsigned char smem_g[];
#define TS(v)
#define TS2(v)
#define PUT2(k, v) do {} while (0)
#define FAILCNT
#define CY(v)
#define NPOLL
#define PUT(k, v) do {} while (0)
#define DUMP(slot) do {} while (0)
#define SLP_S 16
#define SLP_BG 0
#define EXPER 0
#define DLY_S 2
#define DLY_R 8
#define HINTS 6
#define SLP_R 0

DEVI void load_w1_sparse(const u16* W, int col, int lane, v8u (&wt)[2][9]) {
  const int n = lane & 15, gb = lane >> 4;
  const rsrc_t rw = mk_rsrc(W, DM * 4096 * 2);
  const int vo = (((gb >> 1) + 8 * (gb & 1)) * 4096 + col + 2 * n) * 2;
#pragma unroll
  for (int ks = 0; ks < 9; ++ks) {
#pragma unroll
    for (int q = 0; q < 8; ++q) {
      const int d = 64 * ks + 32 * (q >> 2) + 16 * (q & 1) + 4 * ((q >> 1) & 1);
      const u32 a = pl32(rw, vo, d * 8192), b = pl32(rw, vo, (d + 2) * 8192);
      wt[0][ks][q] = __builtin_amdgcn_perm(b, a, 0x05040100u);
      wt[1][ks][q] = __builtin_amdgcn_perm(b, a, 0x07060302u);
    }
  }
}
DEVI int tile_of(int c, int i) { return i < 8 ? 9 * c + (c & 1) + i : (c & 1 ? 9 * c : 9 * c + 8); }
DEVI void load_w2(const u16* W, int h, int c, int lane, v8u (&wt)[9][2]) {
  const int n = lane & 15, gb = lane >> 4;
  const u32 sel = (c & 1) ? 0x07060302u : 0x05040100u;
  const rsrc_t rw = mk_rsrc(W, DM * 4096 * 2);
  const int vo = ((h * 128 + 8 * gb) * DM + 16 * tile_of(c, 0) + 2 * n) * 2, v8 = ((h * 128 + 8 * gb) * DM + 32 * (tile_of(c, 8) >> 1) + 2 * n) * 2;
#pragma unroll
  for (int ks = 0; ks < 2; ++ks) {
#pragma unroll
    for (int q = 0; q < 8; ++q) {
      const int e = 64 * ks + 32 * (q >> 2) + 2 * (q & 3);
#pragma unroll
      for (int pr = 0; pr < 4; ++pr) {
        const u32 a = pl32(rw, vo + pr * 64, e * DM * 2), b = pl32(rw, vo + pr * 64, (e + 1) * DM * 2);
        wt[2 * pr][ks][q] = __builtin_amdgcn_perm(b, a, 0x05040100u);
        wt[2 * pr + 1][ks][q] = __builtin_amdgcn_perm(b, a, 0x07060302u);
      }
      wt[8][ks][q] = __builtin_amdgcn_perm(pl32(rw, v8, (e + 1) * DM * 2), pl32(rw, v8, e * DM * 2), sel);
    }
  }
}

constexpr int AIMG = 1088, QIMG = 2 * AIMG, KIMG = 2304, IMG = QIMG + KIMG;
constexpr int LM_IMG = 0, LM_MEM = 5120, LM_RED = 0, LM_T = 14336, LM_KQ = 16384, LM_BZ = 4096;

DEVI int wait_parity(rsrc_t r, int& rounds) {
  int v;
  POLL(v = uni(ld32(r, CTRL + L_PAR * 128, 0)); bad = v == (int)SENT; if (bad) __builtin_amdgcn_s_sleep(4));
  return (v & 1) * 8192;
}
DEVI int next_slot(int sb, int par) { return sb + SLOT >= par + 3 * SLOT ? par : sb + SLOT; }

DEVI f32x4 zero4() { f32x4 z = {0, 0, 0, 0}; asm volatile("" : "+v"(z)); return z; }
DEVI f32x2 fold(f32x4 a) { return f32x2{a[0], a[1]} + f32x2{a[2], a[3]}; }
template <int TWO>
struct Gather {
  v4u a, b;
  DEVI void run(rsrc_t r, LAS unsigned char* L, int l16, int c0, int sb, int& rounds) {
    NOUNROLL for (;;) {
      if (__builtin_expect(data_wait<TWO>(r, l16, sb + MEM_OFF + (TWO ? c0 * 1024 : CHUNK), a, b), 1)) break;
      rounds += 15;
      if (give_up(r, rounds)) break;
    }
    *(LAS v4u*)(L + LM_MEM + c0 * 1024 + l16) = a;
    if (TWO) *(LAS v4u*)(L + LM_MEM + c0 * 1024 + 1024 + l16) = b;
  }
  DEVI void release() { asm volatile("s_waitcnt vmcnt(0)" ::"{v[240:243]}"(a), "{v[244:247]}"(b) : "memory"); }
};
template <int RSTEP, class F>
DEVI void stage1(LAS unsigned char* L, const v8u (&wt)[2][9], f32x4 a0, f32x4 a1, int l16, int idx, LAS unsigned char* wp, F mid) {
  v4u af[9];
#pragma unroll
  for (int ks = 0; ks < 9; ++ks) af[ks] = *(LAS v4u*)(L + LM_MEM + ks * 1024 + l16);
  SB();
#pragma unroll
  for (int ks = 0; ks < 9; ++ks) {
    a0 = smf64(af[ks], wt[0][ks], a0, idx); a1 = smf64(af[ks], wt[1][ks], a1, idx);
    if (ks == 5) { SB(); mid(); SB(); }
  }
  const f32x2 p0 = fold(a0), p1 = fold(a1);
  *(LAS u32*)wp = pack2(p0.x, p1.x);
  *(LAS u32*)(wp + RSTEP) = pack2(p0.y, p1.y);
}
DEVI int aimg_w(int wq, int lane) { const int n = lane & 15, g = lane >> 4; return (wq >> 1) * AIMG + (2 * (wq & 1) + (n >> 3)) * 272 + g * 64 + (n & 1) * 32 + ((n & 7) >> 1) * 4; }
DEVI int aimg_r(int lane) { return (lane >> 4) * 272 + (lane & 15) * 16; }
DEVI int kimg_w(int wk, int lane) { const int n = lane & 15, g = lane >> 4; return (wk >> 1) * 1152 + (n >> 2) * 288 + g * 64 + (wk & 1) * 16 + (n & 3) * 4; }
DEVI int kimg_r(int k, int g, int key) { return (k >> 1) * 1152 + g * 288 + key * 32 + (k & 1) * 16; }
DEVI float row16_max_asm(float v) {
  asm volatile("s_nop 1\n\tv_max_f32_dpp %0, %0, %0 quad_perm:[1,0,3,2] row_mask:0xf bank_mask:0xf\n\t"
               "s_nop 1\n\tv_max_f32_dpp %0, %0, %0 quad_perm:[2,3,0,1] row_mask:0xf bank_mask:0xf\n\t"
               "s_nop 1\n\tv_max_f32_dpp %0, %0, %0 row_half_mirror row_mask:0xf bank_mask:0xf\n\t"
               "s_nop 1\n\tv_max_f32_dpp %0, %0, %0 row_mirror row_mask:0xf bank_mask:0xf"
               : "+v"(v));
  return v;
}

constexpr float SCL = 0.08838834764831845f * 1.4426950408889634f;

DEVI void role_S(const Params& p, rsrc_t r, LAS unsigned char* L, int h) {
  const int lane = threadIdx.x & 63, w = uni(threadIdx.x >> 6), n = lane & 15, g = lane >> 4;
  const int idx = (lane & 2) ? 0xEEEE : 0x4444, l16 = opaque(lane * 16);
  int rounds = 0;
  v8u wt[2][9];
  load_w1_sparse(w < 4 ? p.wq : p.wk, h * 128 + 32 * (w & 3), lane, wt);
  const int par = wait_parity(r, rounds);
  int sb = par;
  if (w < 2) {
    LAS unsigned char* wa = L + LM_IMG + aimg_w(w, lane);
    const int pvo = P_OFF + (h >> 3) * CHUNK + (h & 7) * 1024 + w * 512;
    const int pxo = pvo + (n >> 3) * 128 + (g >> 1) * 64 + ((n >> 2) & 1) * 32 + (g & 1) * 16 + (n & 3) * 4;
    const int pmo = n < 4 ? pvo + 256 + (g >> 1) * 32 + (n >> 1) * 16 + (g & 1) * 8 + (n & 1) * 4 : OOB;
    const int cn = (n & 8) | ((n & 3) << 1) | ((n >> 2) & 1);
    const int kmc = ((n & 1) << 2) | (n & 2) | ((n >> 2) & 1);
    const float mk = n < 8 ? 1.f : 0.f;
    int sprev = par + 2 * SLOT;
    for (int s = 0; s < NSEG; ++s) {
      const f32x4 z0 = zero4(), z1 = zero4();
      f32x4 sa = zero4(), sbb = zero4(), sm = zero4();
      LAS unsigned char* kq = L + LM_KQ + (s & 1) * 16384;
      v4u kf[2][4];
      Gather<1> ga;
      ga.run(r, L, l16, 2 * w, sb, rounds);
      TS(t0);
      bar();
      stage1<16>(L, wt, z0, z1, l16, idx, wa, [&]() {
#pragma unroll
        for (int k = 0; k < 8; ++k) kf[k >> 2][k & 3] = *(LAS v4u*)(kq + k * 1024 + (g * 16 + cn) * 16);
      });
      ga.release();
      bar();
      TS(t1);
      v4u qa[2], km[4];
#pragma unroll
      for (int k = 0; k < 2; ++k) qa[k] = *(LAS v4u*)(L + LM_IMG + k * AIMG + aimg_r(lane ^ w));
#pragma unroll
      for (int k = 0; k < 4; ++k) km[k] = *(LAS v4u*)(L + LM_IMG + QIMG + kimg_r(k, g, kmc));
      SB();
      sa = smf64(qa[0], cat(kf[0][0], kf[0][1]), sa, idx);
      sbb = smf64(qa[0], cat(kf[1][0], kf[1][1]), sbb, idx);
      sa = smf64(qa[1], cat(kf[0][2], kf[0][3]), sa, idx);
      sm = smf64(qa[0], cat(km[0], km[1]), sm, idx);
      sbb = smf64(qa[1], cat(kf[1][2], kf[1][3]), sbb, idx);
      sm = smf64(qa[1], cat(km[2], km[3]), sm, idx);
      SB();
      const float va = sa[0] + sa[2], vb = sbb[0] + sbb[2], vm = sm[0] + sm[2];
      const float mx = row16_max_asm(fmaxf(fmaxf(va, vb), vm));
      const float ea = __builtin_amdgcn_exp2f((va - mx) * SCL), eb = __builtin_amdgcn_exp2f((vb - mx) * SCL), em = __builtin_amdgcn_exp2f((vm - mx) * SCL);
      const float em2 = dpp<0x104>(em);
      const float inv = __builtin_amdgcn_rcpf(row16_sum(__builtin_fmaf(em, mk, ea + eb)));
      st32(guard(pack2(ea * inv, eb * inv)), r, pxo, sb);
      st32(guard(pack2(em * inv, em2 * inv)), r, pmo, sb);
      SB();
      *(LAS u32*)(L + LM_T + w * 4) = rt7();
      TS(t2);
      PUT(w, t0); PUT(8 + w, t1); PUT(10 + w, t2);
      st32(SENT, r, pxo, sprev); st32(SENT, r, pmo, sprev);
      __builtin_amdgcn_s_sleep(PSLP - 8);
      sprev = sb;
      sb = next_slot(sb, par);
    }
  } else if (w < 4) {
    LAS unsigned char* wa = L + LM_IMG + aimg_w(w, lane);
    const int m = w - 2, qi = 2 * n + m;
    const int po = PX_OFF + (h * 2 + m) * 1280;
    for (int s = 0; s < NSEG; ++s) {
      const f32x4 z0 = zero4(), z1 = zero4();
      bar();
      stage1<16>(L, wt, z0, z1, l16, idx, wa, []() {});
      drain();
      bar();
      __builtin_amdgcn_s_sleep(XSLP);
      LAS unsigned char* kq = L + LM_KQ + (s & 1) * 16384 + l16;
      f32x4 xa = {0, 0, 0, 0}, xb = {0, 0, 0, 0}, xm = {0, 0, 0, 0};
#pragma unroll
      for (int k = 0; k < 4; ++k) {
        const v4u qf = *(LAS v4u*)(kq + 8192 + (m * 4 + k) * 1024);
        xa = mfma(*(LAS v4u*)(kq + k * 1024), qf, xa);
        xb = mfma(*(LAS v4u*)(kq + (4 + k) * 1024), qf, xb);
        xm = mfma(*(LAS v4u*)(L + LM_IMG + QIMG + kimg_r(k, g, n & 7)), qf, xm);
      }
      float mx = fmaxf(fmaxf(xm[0], xm[1]), fmaxf(xm[2], xm[3]));
#pragma unroll
      for (int k = 0; k < 4; ++k) {
        xa[k] = (8 * g + 2 * k <= qi) ? xa[k] : -INFINITY;
        xb[k] = (8 * g + 2 * k + 1 <= qi) ? xb[k] : -INFINITY;
        mx = fmaxf(mx, fmaxf(xa[k], xb[k]));
      }
      mx = x32_max(x16_max(mx));
      float sum = 0.f, summ = 0.f;
#pragma unroll
      for (int k = 0; k < 4; ++k) {
        xa[k] = __builtin_amdgcn_exp2f((xa[k] - mx) * SCL); xb[k] = __builtin_amdgcn_exp2f((xb[k] - mx) * SCL); xm[k] = __builtin_amdgcn_exp2f((xm[k] - mx) * SCL);
        sum += xa[k] + xb[k]; summ += xm[k];
      }
      sum += g < 2 ? summ : 0.f;
      const float inv = __builtin_amdgcn_rcpf(x32_sum(x16_sum(sum)));
      v4u px; v2u pm2;
#pragma unroll
      for (int k = 0; k < 4; ++k) px[k] = pack2(xa[k] * inv, xb[k] * inv);
      pm2.x = pack2(xm[0] * inv, xm[1] * inv); pm2.y = pack2(xm[2] * inv, xm[3] * inv);
      st128(px, r, po + l16, s * NH * 2560);
      st64(pm2, r, g < 2 ? po + 1024 + n * 16 + g * 8 : OOB, s * NH * 2560);
    }
    drain();
  } else if (w < 6) {
    LAS unsigned char* wa = L + LM_IMG + QIMG + kimg_w(w - 4, lane);
    for (int s = 0; s < NSEG; ++s) {
      const f32x4 z0 = zero4(), z1 = zero4();
      Gather<1> ga;
      ga.run(r, L, l16, 2 * w - 4, sb, rounds);
      TS(t0);
      bar();
      stage1<32>(L, wt, z0, z1, l16, idx, wa, []() {});
      bar();
      ga.release();
      PUT(w, t0);
      __builtin_amdgcn_s_sleep(PSLP);
      sb = next_slot(sb, par);
    }
  } else if (w == 6) {
    LAS unsigned char* wa = L + LM_IMG + QIMG + kimg_w(2, lane);
    for (int s = 0; s < NSEG; ++s) {
      const f32x4 z0 = zero4(), z1 = zero4();
      Gather<0> ga;
      ga.run(r, L, l16, 8, sb, rounds);
      TS(t0);
      bar();
      stage1<32>(L, wt, z0, z1, l16, idx, wa, []() {});
      bar();
      ga.release();
      PUT(w, t0);
      __builtin_amdgcn_s_sleep(PSLP);
      if (s > 0 && lane == 0) st32(s - 1, r, CTRL + (L_SP + h) * 128, 0);
      sb = next_slot(sb, par);
    }
  } else {
    LAS unsigned char* wa = L + LM_IMG + QIMG + kimg_w(3, lane);
    int seen0 = -1, seen1 = -1, dur = 60 << 7;
    auto fetch = [&](int s) {
      wait_prog2(r, L_XP + h, 1, s, lane, seen0, seen1, rounds);
      v4u t[16];
#pragma unroll
      for (int k = 0; k < 16; ++k) t[k] = ld128(r, l16 + (k & 3) * 1024, KQ_OFF + (s * NH + h) * 16384 + (k >> 2) * 4096);
#pragma unroll
      for (int k = 0; k < 16; ++k) *(LAS v4u*)(L + LM_KQ + (s & 1) * 16384 + l16 + k * 1024) = t[k];
    };
    fetch(0);
    const int ho = lane == 0 ? T_HS + h * 128 : OOB;
    for (int s = 0; s < NSEG; ++s) {
      const f32x4 z0 = zero4(), z1 = zero4();
      bar();
      const u32 tb = rt7();
      st32(((tb + dur) & ~127u) | s, r, ho, par);
      stage1<32>(L, wt, z0, z1, l16, idx, wa, []() {});
      bar();
      if (s + 1 < NSEG) fetch(s + 1);
      dur = track(dur, max((int)(uni(*(LAS u32*)(L + LM_T)) - tb), (int)(uni(*(LAS u32*)(L + LM_T + 4)) - tb)));
    }
  }
  bar();
  if (w == 0 && lane == 0) st32(NSEG - 1, r, CTRL + (L_SP + h) * 128, 0);
  DUMP(h);
}

DEVI void role_V(const Params& p, rsrc_t r, LAS unsigned char* L, int h) {
  const int lane = threadIdx.x & 63, w = uni(threadIdx.x >> 6), n = lane & 15, g = lane >> 4;
  const int idx = (lane & 2) ? 0xEEEE : 0x4444, l16 = opaque(lane * 16);
  int rounds = 0;
  if (w < 4) {
    v8u wt[2][9];
    load_w1_sparse(p.wv, h * 128 + 32 * w, lane, wt);
    const int par = wait_parity(r, rounds);
    LAS unsigned char* wa = L + LM_IMG + aimg_w(w, lane);
    int sb = par;
    for (int s = 0; s < NSEG; ++s) {
      const f32x4 z0 = zero4(), z1 = zero4();
      Gather<1> ga;
      ga.run(r, L, l16, 2 * w, sb, rounds);
      bar();
      stage1<16>(L, wt, z0, z1, l16, idx, wa, []() {});
      TS(t0);
      bar();
      ga.release();
      PUT(w, t0);
      __builtin_amdgcn_s_sleep(PSLP + 4);
      if (w == 1 && s > 0 && lane == 0) st32(s - 1, r, CTRL + (L_VP + h) * 128, 0);
      sb = next_slot(sb, par);
    }
  } else {
    const int c = w - 4;
    v8u wt[9][2];
    load_w2(p.wo, h, c, lane, wt);
    const int par = wait_parity(r, rounds);
    int lr[3], lz[3];
#pragma unroll
    for (int q = 0; q < 3; ++q) {
      const int tl = q < 2 ? tile_of(c, 4 * q) + g : tile_of(c, 8);
      const bool on = q < 2 || g == 0;
      lr[q] = on ? tl * CHUNK + h * 256 + n * 16 : OOB;
      lz[q] = on ? CZ_OFF + tl * 8192 + h * 256 + n * 16 : OOB;
    }
    int sb = par, sprev = par + 2 * SLOT, dur = 55 << 7;
    const int ho = (c == 3 && lane == 0) ? T_HV + h * 128 : OOB;
    for (int s = 0; s < NSEG; ++s) {
      f32x4 a[9];
#pragma unroll
      for (int i = 0; i < 9; ++i) a[i] = zero4();
      int so[3];
#pragma unroll
      for (int q = 0; q < 3; ++q) { so[q] = lr[q] + sb; asm volatile("" : "+v"(so[q])); }
      LAS unsigned char* img = L + LM_IMG + aimg_r(lane);
      if (c == 0) { Gather<0> ga; ga.run(r, L, l16, 8, sb, rounds); bar(); ga.release(); }
      else bar();
      const u32 tb = rt7();
      drain();
      st32(((tb + dur) & ~127u) | s, r, ho, par);
      bar();
      v4u va[2];
#pragma unroll
      for (int k = 0; k < 2; ++k) va[k] = *(LAS v4u*)(img + k * AIMG);
      u32 o[9];
      v4u ov[3];
      auto fin = [&](int i) { const f32x2 f = fold(a[i]); o[i] = guard(pack2(f.x, f.y)); };
      auto xch = [&](int q, u32 u0, u32 u1, u32 u2, u32 u3) {
        const auto e0 = __builtin_amdgcn_permlane32_swap(u0, u2, false, false);
        const auto e1 = __builtin_amdgcn_permlane32_swap(u1, u3, false, false);
        const auto f0 = __builtin_amdgcn_permlane16_swap(e0[0], e1[0], false, false);
        const auto f1 = __builtin_amdgcn_permlane16_swap(e0[1], e1[1], false, false);
        ov[q] = v4u{f0[0], f0[1], f1[0], f1[1]};
        st128(ov[q], r, so[q], 0);
      };
      constexpr int ord[9] = {8, 0, 1, 2, 3, 4, 5, 6, 7};
      a[8] = smf64(va[0], wt[8][0], a[8], idx);
#pragma unroll
      for (int i = 0; i < 9; ++i) {
        if (i < 8) a[ord[i + 1]] = smf64(va[0], wt[ord[i + 1]][0], a[ord[i + 1]], idx);
        a[ord[i]] = smf64(va[1], wt[ord[i]][1], a[ord[i]], idx);
        if (i >= 2) fin(ord[i - 2]);
        if (i == 3) xch(2, o[8], o[8], o[8], o[8]);
        if (i == 7) xch(0, o[0], o[1], o[2], o[3]);
        SB();
      }
      fin(6); fin(7);
      xch(1, o[4], o[5], o[6], o[7]);
      SB();
      dur = track(dur, (int)(rt7() - tb));
      TS(t2);
      PUT(10 + (c & 1), t2);
#pragma unroll
      for (int q = 0; q < 3; ++q) st128(ov[q], r, lz[q], s * NTILE * 8192);
#pragma unroll
      for (int q = 0; q < 3; ++q) st128(v4u{SENT, SENT, SENT, SENT}, r, lr[q], sprev);
      sprev = sb;
      sb = next_slot(sb, par);
    }
    drain();
  }
  bar();
  if (w == 0 && lane == 0) st32(NSEG - 1, r, CTRL + (L_VP + h) * 128, 0);
  DUMP(32 + h);
}

DEVI u64 cyc() { u64 t; asm volatile("s_memtime %0\n\ts_waitcnt lgkmcnt(0)" : "=s"(t)::"memory"); return t; }

DEVI void role_R(const Params& p, rsrc_t r, LAS unsigned char* L, int j) {
  const int lane = threadIdx.x & 63, w = uni(threadIdx.x >> 6), n = lane & 15, g = lane >> 4;
  int rounds = 0;
  if (j == 0 && w == 4) {
    u32 best[2] = {~0u, ~0u};
    if (lane == 0) {
      for (int it = 0; it < 3; ++it)
        for (int q = 0; q < 2; ++q) {
          GAS u32* a = (GAS u32*)(p.ws + MEM_OFF + CHUNK + q * 8192 + 7168);
          const u64 t0 = cyc();
          u32 v = __hip_atomic_fetch_add(a, 1u, __ATOMIC_RELAXED, __HIP_MEMORY_SCOPE_AGENT);
          asm volatile("s_waitcnt vmcnt(0)" ::"v"(v) : "memory");
          const u32 d = (u32)(cyc() - t0);
          best[q] = d < best[q] ? d : best[q];
        }
      st32(best[1] < best[0] ? 1u : 0u, r, CTRL + L_PAR * 128, 0);
    }
  }
  const int par = wait_parity(r, rounds);
  int sb = par;
  if (w < 4) {
    const int idx = (lane & 2) ? 0xEEEE : 0x4444;
    const int pxo = opaque(P_OFF + w * CHUNK + (g >> 1) * 1024 + (n >> 3) * 512 + (g & 1) * 128 + (n & 7) * 16);
    const int pmo = P_OFF + w * CHUNK + g * 1024 + (n >> 3) * 512 + 256 + (n & 7) * 8;
    const int co = j * CHUNK + w * 2048 + lane * 16;
    const int redw = LM_RED + w * 512 + (g & 1) * 256 + (n >> 3) * 64 + (g >> 1) * 32 + (n & 7) * 4;
    const int hio = (lane < 32 ? T_HS : T_HV) + (8 * w + ((lane >> 2) & 7)) * 128;
    bar();
    for (int s = 0; s < NSEG - 1; ++s) {
      v4u bz[8];
#pragma unroll
      for (int q = 0; q < 8; ++q) bz[q] = *(LAS v4u*)(L + LM_BZ + (s & 1) * 32768 + (8 * w + q) * 1024 + lane * 16);
      __builtin_amdgcn_s_sleep(RSLP);
      const u32 hv = hint_wait(r, hio, par, s);
      TS(tb);
      const u32 tv = hv + MAR_R * 128;
      time_wait<1>(tv);
      v4u c0 = ld128(r, co, sb), c1 = ld128(r, co + 1024, sb);
      time_wait<0>(tv);
      TS(tf);
      auto attempt = [&]() {
        v4u px[4]; v2u pm[2];
#pragma unroll
        for (int q = 0; q < 2; ++q) pm[q] = ld64(r, pmo + q * 4096, sb);
#pragma unroll
        for (int q = 0; q < 4; ++q) px[q] = ld128(r, pxo + q * 2048, sb);
        f32x4 a = zero4(), b = zero4();
        SB();
        a = smf32(pm[0], c0, a, idx); b = smf32(pm[1], c1, b, idx);
        u32 mp = umax(umax(mx4(c0), mx4(c1)), umax(umax(pm[0].x, pm[0].y), umax(pm[1].x, pm[1].y)));
        SB();
        a = smf64(px[0], cat(bz[0], bz[1]), a, idx); mp = umax(mp, mx4(px[0])); SB();
        b = smf64(px[1], cat(bz[2], bz[3]), b, idx); mp = umax(mp, mx4(px[1])); SB();
        a = smf64(px[2], cat(bz[4], bz[5]), a, idx); mp = umax(mp, mx4(px[2])); SB();
        b = smf64(px[3], cat(bz[6], bz[7]), b, idx); mp = umax(mp, mx4(px[3]));
        const f32x2 f = fold(a) + fold(b);
        *(LAS float*)(L + (s & 1) * 2048 + redw) = f.x;
        *(LAS float*)(L + (s & 1) * 2048 + redw + 128) = f.y;
        return any_sent(mp);
      };
      if (__builtin_expect(attempt(), 0)) {
        POLL(FAILCNT c0 = ld128(r, co, sb); c1 = ld128(r, co + 1024, sb); bad = attempt());
      }
      asm volatile("" ::"v"(hv));
      TS(t0);
      bar();
      PUT(w, t0); PUT(4 + w, tf); if (w == 1) PUT(12, tb);
      __builtin_amdgcn_s_sleep(4);
      sb = next_slot(sb, par);
    }
  } else {
    const int mo = MEM_OFF + (j >> 5) * CHUNK + (j & 31) * 256 + lane * 4, l16 = opaque(lane * 16);
    int snext = par + SLOT, seen0 = -1, seen1 = -1;
    auto fetch = [&](int s) {
      wait_prog2(r, L_YP + 8 * (w - 4), 8, s, lane, seen0, seen1, rounds);
      v4u t[8];
#pragma unroll
      for (int q = 0; q < 8; ++q) t[q] = ld128(r, l16 + (q & 3) * 1024, BZ_OFF + (s * NTILE + j) * 32768 + (w - 4) * 8192 + (q >> 2) * 4096);
#pragma unroll
      for (int q = 0; q < 8; ++q) *(LAS v4u*)(L + LM_BZ + (s & 1) * 32768 + (8 * (w - 4) + q) * 1024 + l16) = t[q];
    };
    if (w == 4) {
      const u16* src = p.mem0 + ((lane >> 4) * 2 + ((lane >> 2) & 1)) * DM + 32 * (j >> 1) + (j & 1) + 16 * ((lane >> 3) & 1) + 4 * (lane & 3);
      st32(guard((u32)src[0] | ((u32)src[2] << 16)), r, mo, sb);
      fetch(0);
      bar();
      __builtin_amdgcn_s_setprio(2);
      for (int s = 0; s < NSEG - 1; ++s) {
        LAS unsigned char* red = L + LM_RED + (s & 1) * 2048 + lane * 8;
        fetch(s + 1);
        drain();
        bar();
        f32x2 t[4];
#pragma unroll
        for (int q = 0; q < 4; ++q) t[q] = *(LAS f32x2*)(red + q * 512);
        const f32x2 u = (t[0] + t[1]) + (t[2] + t[3]);
        st32(guard(pack2(u.x, u.y)), r, mo, snext);
        SB();
        TS2(t2);
        st32(SENT, r, mo, sb);
        PUT2(10, t2);
        sb = snext;
        snext = next_slot(snext, par);
      }
    } else {
      fetch(0);
      bar();
      for (int s = 0; s < NSEG - 1; ++s) {
        fetch(s + 1);
        bar();
        __builtin_amdgcn_s_sleep(4);
      }
    }
  }
  DUMP(64 + j);
}

DEVI void load_w1_dense(const u16* W, int col, int lane, v4u (&wt)[2][18]) {
  const int n = lane & 15, gb = lane >> 4;
  const rsrc_t rw = mk_rsrc(W, DM * 4096 * 2);
  const int vo = (8 * gb * 4096 + col + 2 * n) * 2;
#pragma unroll
  for (int ks = 0; ks < 18; ++ks) {
#pragma unroll
    for (int t = 0; t < 4; ++t) {
      const int d = 32 * ks + 2 * t;
      const u32 a = pl32(rw, vo, d * 8192), b = pl32(rw, vo, (d + 1) * 8192);
      wt[0][ks][t] = __builtin_amdgcn_perm(b, a, 0x05040100u);
      wt[1][ks][t] = __builtin_amdgcn_perm(b, a, 0x07060302u);
    }
  }
}
DEVI int ximg_rd(int lane) { const int n = lane & 15, g = lane >> 4; return n * 64 + (g ^ ((n >> 3) << 1)) * 16; }
DEVI int ximg_piece(int lane) { return ((lane & 3) ^ ((lane >> 5) << 1)) * 16; }
template <int DEPTH>
DEVI void proj32(LAS unsigned char* xb, const v4u (&wt)[2][18], v4u (&o)[2]) {
  f32x4 acc[2][2];
#pragma unroll
  for (int i = 0; i < 4; ++i) acc[i >> 1][i & 1] = zero4();
  v4u xf[DEPTH][2];
#pragma unroll
  for (int ks = 0; ks < DEPTH - 1; ++ks) { xf[ks][0] = *(LAS v4u*)(xb + ks * 1024); xf[ks][1] = *(LAS v4u*)(xb + (18 + ks) * 1024); }
  SB();
#pragma unroll
  for (int ks = 0; ks < 18; ++ks) {
    const int kn = ks + DEPTH - 1;
    if (kn < 18) { xf[kn % DEPTH][0] = *(LAS v4u*)(xb + kn * 1024); xf[kn % DEPTH][1] = *(LAS v4u*)(xb + (18 + kn) * 1024); }
    SB();
#pragma unroll
    for (int m = 0; m < 2; ++m) {
      acc[0][m] = mfma(wt[0][ks], xf[ks % DEPTH][m], acc[0][m]);
      acc[1][m] = mfma(wt[1][ks], xf[ks % DEPTH][m], acc[1][m]);
    }
    SB();
  }
#pragma unroll
  for (int m = 0; m < 2; ++m)
#pragma unroll
    for (int q = 0; q < 4; ++q) o[m][q] = pack2(acc[0][m][q], acc[1][m][q]);
}

struct XStage {
  rsrc_t rx; int go, lo; v4u st[9];
  DEVI void init(const Params& p, int tok, int q, int lane) {
    rx = mk_rsrc(p.x, 2048 * DM * 2);
    go = tok * (DM * 2) + (q & 1) * 9 * 64 + ximg_piece(lane);
    lo = q * 9 * 1024 + lane * 16;
  }
  DEVI void req(int s) {
#pragma unroll
    for (int k = 0; k < 9; ++k) st[k] = pl128(rx, go + k * 64, (s < NSEG ? s : NSEG - 1) * 32 * DM * 2);
  }
  DEVI void put(LAS unsigned char* L) {
#pragma unroll
    for (int k = 0; k < 9; ++k) *(LAS v4u*)(L + lo + k * 1024) = st[k];
  }
};

DEVI void role_X(const Params& p, rsrc_t r, LAS unsigned char* L, int h, int s0) {
  const int lane = threadIdx.x & 63, w = uni(threadIdx.x >> 6);
  const int rd = ximg_rd(lane), oo = KQ_OFF + h * 16384 + (w >> 2) * 8192 + (w & 3) * 1024 + lane * 16;
  v4u wt[2][18];
  if (w < 4) {
    XStage xs; xs.init(p, 2 * (lane >> 2) + (w >> 1), w, lane);
    xs.req(s0);
    load_w1_dense(p.wk, h * 128 + 32 * w, lane, wt);
    xs.put(L);
    xs.req(s0 + 2);
    __builtin_amdgcn_s_setprio(1);
    bar();
    const int fo = (w == 0 && lane == 0) ? CTRL + (L_XP + s0 * 32 + h) * 128 : OOB;
    for (int s = s0, it = 0; s < NSEG; s += 2, ++it) {
      v4u o[2];
      proj32<3>(L + (it & 1) * 36864 + rd, wt, o);
      st128(o[0], r, oo, s * NH * 16384);
      st128(o[1], r, oo + 4096, s * NH * 16384);
      SB();
      xs.put(L + ((it + 1) & 1) * 36864);
      SB();
      xs.req(s + 4);
      asm volatile("s_waitcnt vmcnt(9)" ::: "memory");
      bar();
      st32(it < 1 || s + 2 >= NSEG ? s : s - 2, r, fo, 0);
    }
    __builtin_amdgcn_s_setprio(0);
  } else {
    load_w1_dense(p.wq, h * 128 + 32 * (w & 3), lane, wt);
    bar();
    for (int s = s0, it = 0; s < NSEG; s += 2, ++it) {
      v4u o[2];
      proj32<4>(L + (it & 1) * 36864 + rd, wt, o);
      st128(o[0], r, oo, s * NH * 16384);
      st128(o[1], r, oo + 4096, s * NH * 16384);
      if (it < 1 || s + 2 >= NSEG) drain();
      asm volatile("s_waitcnt vmcnt(2)" ::: "memory");
      bar();
    }
  }
}

DEVI void role_Y(const Params& p, rsrc_t r, LAS unsigned char* L, int h, int s0) {
  const int lane = threadIdx.x & 63, w = uni(threadIdx.x >> 6);
  constexpr int VB = 73728;
  if (w < 4) {
    v4u wt[2][18];
    load_w1_dense(p.wv, h * 128 + 32 * w, lane, wt);
    bar();
    const int rd = ximg_rd(lane);
    const int fo = (w == 0 && lane == 0) ? CTRL + (L_YP + s0 * 32 + h) * 128 : OOB;
    for (int s = s0, it = 0; s < NSEG; s += 2, ++it) {
      v4u o[2];
      proj32<4>(L + (it & 1) * 36864 + rd, wt, o);
      *(LAS v4u*)(L + VB + (it & 1) * 8192 + w * 1024 + lane * 16) = o[0];
      *(LAS v4u*)(L + VB + (it & 1) * 8192 + 4096 + w * 1024 + lane * 16) = o[1];
      bar();
      st32(s - 2, r, fo, 0);
    }
    bar();
    st32(NSEG - 2 + s0, r, fo, 0);
  } else {
    const int c = w - 4, row = lane >> 2;
    XStage xs; xs.init(p, 8 * (row >> 2) + (row & 3) + 4 * (c >> 1), c, lane);
    xs.req(s0);
    v8u wt[9][2];
    load_w2(p.wo, h, c, lane, wt);
    xs.put(L);
    xs.req(s0 + 2);
    __builtin_amdgcn_s_setprio(1);
    bar();
    xs.put(L + 36864);
    xs.req(s0 + 4);
    bar();
    const int oo = BZ_OFF + h * 1024 + lane * 16;
    const int t0o = tile_of(c, 0) * 32768, t8o = tile_of(c, 8) * 32768;
    for (int s = s0, it = 0; s < NSEG; s += 2, ++it) {
      v4u vf[2][4];
#pragma unroll
      for (int k = 0; k < 8; ++k) vf[k >> 2][k & 3] = *(LAS v4u*)(L + VB + (it & 1) * 8192 + k * 1024 + lane * 16);
#pragma unroll
      for (int i = 0; i < 9; ++i) {
        f32x4 a0 = {0, 0, 0, 0}, a1 = {0, 0, 0, 0};
#pragma unroll
        for (int k = 0; k < 2; ++k) {
          a0 = mfma(vf[0][2 * k], lo4(wt[i][k]), a0); a1 = mfma(vf[1][2 * k], lo4(wt[i][k]), a1);
          a0 = mfma(vf[0][2 * k + 1], hi4(wt[i][k]), a0); a1 = mfma(vf[1][2 * k + 1], hi4(wt[i][k]), a1);
        }
        st128(v4u{pack2(a0[0], a0[1]), pack2(a0[2], a0[3]), pack2(a1[0], a1[1]), pack2(a1[2], a1[3])}, r, oo,
              s * NTILE * 32768 + (i < 8 ? t0o + i * 32768 : t8o));
      }
      SB();
      xs.put(L + (it & 1) * 36864);
      SB();
      xs.req(s + 6);
      asm volatile("s_waitcnt vmcnt(9)" ::: "memory");
      bar();
    }
    __builtin_amdgcn_s_setprio(0);
  }
}

DEVI void role_T(const Params& p, rsrc_t r, int ti) {
  const int lane = threadIdx.x & 63, w = uni(threadIdx.x >> 6), n = lane & 15, g = lane >> 4;
  const rsrc_t r1 = mk_rsrc(p.w1, DM * FF * 2), r2 = mk_rsrc(p.w2, DM * FF * 2);
  for (int u = ti * 8 + w; u < 648 + 576; u += 28 * 8) {
    v4u raw[8];
    if (u < 648) {
      const int c = u / 18, ks = u - 18 * c;
#pragma unroll
      for (int q = 0; q < 8; ++q) raw[q] = pl128(r1, ((8 * g + q) * FF + 8 * n) * 2, (32 * ks * FF + 128 * c) * 2);
#pragma unroll
      for (int t = 0; t < 8; ++t) {
        v4u o;
#pragma unroll
        for (int q = 0; q < 4; ++q) o[q] = __builtin_amdgcn_perm(raw[2 * q + 1][t >> 1], raw[2 * q][t >> 1], (t & 1) ? 0x07060302u : 0x05040100u);
        st128(o, r, W1T_OFF + (((n >> 2) * 18) * 2 + (t & 1)) * 1024 + (g * 16 + (n & 3) * 4 + (t >> 1)) * 16, (c * 4 * 18 + ks) * 2048);
      }
    } else {
      const int k = (u - 648) >> 2, c4 = (u - 648) & 3;
      u32 r9[8];
#pragma unroll
      for (int q = 0; q < 8; ++q) raw[q] = pl128(r2, ((8 * g + q) * DM + 8 * n) * 2, (32 * k * DM + 144 * c4) * 2);
#pragma unroll
      for (int q = 0; q < 8; ++q) r9[q] = __builtin_amdgcn_raw_buffer_load_b16(r2, ((8 * g + q) * DM + 128 + n) * 2, (32 * k * DM + 144 * c4) * 2, 0);
#pragma unroll
      for (int t = 0; t < 9; ++t) {
        v4u o;
#pragma unroll
        for (int q = 0; q < 4; ++q)
          o[q] = t < 8 ? __builtin_amdgcn_perm(raw[2 * q + 1][(t & 7) >> 1], raw[2 * q][(t & 7) >> 1], (t & 1) ? 0x07060302u : 0x05040100u)
                       : __builtin_amdgcn_perm(r9[2 * q + 1], r9[2 * q], 0x05040100u);
        st128(o, r, W2T_OFF + t * 1024 + lane * 16, (k * 4 + c4) * 9216);
      }
    }
  }
  drain();
  bar();
  if (threadIdx.x == 0) st32(0u, r, CTRL + (L_TP + ti) * 128, 0);
}

DEVI float gelu_tanh(float x) {
  const float e = __builtin_amdgcn_exp2f(x * (-2.302208198f - 0.1029432397f * x * x));
  return x * __builtin_amdgcn_rcpf(1.f + e);
}

DEVI void role_F(const Params& p, rsrc_t r, LAS unsigned char* L, int tt, int sp, int c0, int c1) {
  const int lane = threadIdx.x & 63, w = uni(threadIdx.x >> 6), n = lane & 15, g = lane >> 4;
  constexpr int HB = 73728;
  const rsrc_t rx = mk_rsrc(p.x, 2048 * DM * 2);
  { int rounds = 0, seen = -1; wait_prog(r, L_TP, 28, 0, lane, seen, rounds); }
  bar();
  {
    const int go = (tt * 64 + (w >> 1) * 16 + (lane >> 2)) * (DM * 2) + (w & 1) * 9 * 64 + ximg_piece(lane);
    v4u st[9];
#pragma unroll
    for (int k = 0; k < 9; ++k) st[k] = pl128(rx, go + k * 64, 0);
#pragma unroll
    for (int k = 0; k < 9; ++k) *(LAS v4u*)(L + (w * 9 + k) * 1024 + lane * 16) = st[k];
  }
  if (w < 4) {
    const rsrc_t rb = mk_rsrc(p.b1, FF * 2);
    const int vo = W1T_OFF + w * 18 * 2048 + lane * 16;
    constexpr int WIN = 6;
    v4u wf[WIN][2];
    auto req = [&](int slot, int c, int ks) {
      wf[slot][0] = ld128(r, vo, (c * 72 + ks) * 2048);
      wf[slot][1] = ld128(r, vo + 1024, (c * 72 + ks) * 2048);
    };
#pragma unroll
    for (int k = 0; k < WIN - 1; ++k) req(k, c0, k);
    bar();
    const int rd = ximg_rd(lane);
    for (int c = c0; c < c1; ++c) {
      const int cn = c + 1 < c1 ? c + 1 : c;
      const v4u bias = pl128(rb, (32 * w + 8 * g) * 2, c * 256);
      f32x4 acc[2][4];
#pragma unroll
      for (int i = 0; i < 8; ++i) acc[i >> 2][i & 3] = zero4();
      v4u xf[2][4];
#pragma unroll
      for (int m = 0; m < 4; ++m) xf[0][m] = *(LAS v4u*)(L + (m * 18) * 1024 + rd);
#pragma unroll
      for (int ks = 0; ks < 18; ++ks) {
        const int kn = ks + WIN - 1;
        req(kn % WIN, kn < 18 ? c : cn, kn % 18);
        if (ks + 1 < 18) {
#pragma unroll
          for (int m = 0; m < 4; ++m) xf[(ks + 1) & 1][m] = *(LAS v4u*)(L + (m * 18 + ks + 1) * 1024 + rd);
        }
        SB();
#pragma unroll
        for (int m = 0; m < 4; ++m) { acc[0][m] = mfma(wf[ks % WIN][0], xf[ks & 1][m], acc[0][m]); acc[1][m] = mfma(wf[ks % WIN][1], xf[ks & 1][m], acc[1][m]); }
        SB();
      }
#pragma unroll
      for (int m = 0; m < 4; ++m) {
        v4u o;
#pragma unroll
        for (int q = 0; q < 4; ++q)
          o[q] = pack2(gelu_tanh(acc[0][m][q] + bf2f(bias[q] & 0xFFFFu)), gelu_tanh(acc[1][m][q] + __uint_as_float(bias[q] & 0xFFFF0000u)));
        *(LAS v4u*)(L + HB + ((c - c0) & 1) * 16384 + (m * 4 + w) * 1024 + lane * 16) = o;
      }
      bar();
    }
    if (sp == 0) {
      const rsrc_t rg = mk_rsrc(p.gw, DM * 2);
      const int o = lane < 36 ? lane * 32 : OOB;
      const v4u g0 = pl128(rg, o, 0), g1 = pl128(rg, o + 16, 0);
      float mine = 0.f;
      for (int t = 0; t < 16; ++t) {
        const v4u x0 = pl128(rx, o, (tt * 64 + w * 16 + t) * DM * 2), x1 = pl128(rx, o + 16, (tt * 64 + w * 16 + t) * DM * 2);
        float a = 0.f;
#pragma unroll
        for (int q = 0; q < 4; ++q) {
          a += bf2f(x0[q] & 0xFFFFu) * bf2f(g0[q] & 0xFFFFu) + __uint_as_float(x0[q] & 0xFFFF0000u) * __uint_as_float(g0[q] & 0xFFFF0000u);
          a += bf2f(x1[q] & 0xFFFFu) * bf2f(g1[q] & 0xFFFFu) + __uint_as_float(x1[q] & 0xFFFF0000u) * __uint_as_float(g1[q] & 0xFFFF0000u);
        }
        a = x32_sum(x16_sum(row16_sum(a)));
        mine = lane == t ? a : mine;
      }
      const float gv = __builtin_amdgcn_rcpf(1.f + __builtin_amdgcn_exp2f(-1.4426950408889634f * mine));
      st32(__float_as_uint(gv), r, lane < 16 ? G_OFF + (tt * 64 + w * 16 + lane) * 4 : OOB, 0);
    }
  } else {
    const int c4 = w - 4;
    const int vo = W2T_OFF + c4 * 9216 + lane * 16;
    v4u bw[2][9];
#pragma unroll
    for (int t = 0; t < 9; ++t) bw[0][t] = ld128(r, vo + t * 1024, 4 * c0 * 36864);
    f32x4 acc[9][4];
#pragma unroll
    for (int i = 0; i < 36; ++i) acc[i >> 2][i & 3] = zero4();
    bar();
    bar();
    for (int c = c0; c < c1; ++c) {
#pragma unroll
      for (int ks = 0; ks < 4; ++ks) {
        const int kn = 4 * c + ks + 1 < 4 * c1 ? 4 * c + ks + 1 : 4 * c + ks;
        v4u hf[4];
#pragma unroll
        for (int m = 0; m < 4; ++m) hf[m] = *(LAS v4u*)(L + HB + ((c - c0) & 1) * 16384 + (m * 4 + ks) * 1024 + lane * 16);
#pragma unroll
        for (int t = 0; t < 9; ++t) {
          bw[(ks + 1) & 1][t] = ld128(r, vo + t * 1024, kn * 36864);
          SB();
#pragma unroll
          for (int m = 0; m < 4; ++m) acc[t][m] = mfma(hf[m], bw[ks & 1][t], acc[t][m]);
          SB();
        }
      }
      if (c + 1 < c1) bar();
    }
    const int oo = FP_OFF + ((sp * 2048 + tt * 64 + 4 * g) * DM + 144 * c4) * 4;
#pragma unroll
    for (int m = 0; m < 4; ++m)
#pragma unroll
      for (int q = 0; q < 4; ++q) {
        const int ro = (16 * m + q) * DM * 4;
        st128(v4u{__float_as_uint(acc[0][m][q]), __float_as_uint(acc[1][m][q]), __float_as_uint(acc[2][m][q]), __float_as_uint(acc[3][m][q])}, r, oo + n * 32, ro);
        st128(v4u{__float_as_uint(acc[4][m][q]), __float_as_uint(acc[5][m][q]), __float_as_uint(acc[6][m][q]), __float_as_uint(acc[7][m][q])}, r, oo + n * 32 + 16, ro);
        st32(__float_as_uint(acc[8][m][q]), r, oo + 512 + n * 4, ro);
      }
  }
  drain();
  bar();
  if (threadIdx.x == 0) st32(0u, r, CTRL + (L_FP + tt * 4 + sp) * 128, 0);
}

DEVI void role_O(const Params& p, rsrc_t r, LAS unsigned char* L, int cg, int s0, int ds) {
  const int lane = threadIdx.x & 63, w = uni(threadIdx.x >> 6), n = lane & 15, g = lane >> 4;
  int rounds = 0, seenY0 = -1, seenY1 = -1, seenS = -1;
  const int fm = w & 1, ftp = (w >> 1) & 1, frh = w >> 2;
  const int col = 64 * cg + 32 * ftp + 2 * n;
  const u32 b2 = *(const GAS u32*)(p.b2 + col);
  const int svo = CTRL + ((lane & 4 ? L_VP : L_SP) + 4 * w + (lane & 3)) * 128;
  for (int s = s0; s < NSEG; s += ds) {
    wait_prog2(r, L_YP + 4 * w, 4, s, lane, seenY0, seenY1, rounds);
    v4u bz[4][4];
#pragma unroll
    for (int t = 0; t < 4; ++t)
#pragma unroll
      for (int q = 0; q < 4; ++q) bz[t][q] = ld128(r, (4 * w + q) * 1024 + lane * 16, BZ_OFF + (s * NTILE + 4 * cg + t) * 32768);
    const int tt = s >> 1;
    int seenF = -1;
    wait_prog(r, L_FP + tt * 4, 4, 0, lane, seenF, rounds);
    const int tok = s * 32 + 2 * (4 * g + 2 * frh) + fm;
    f32x2 fb[2];
    float gt[2];
#pragma unroll
    for (int q = 0; q < 2; ++q) {
      const int o = FP_OFF + ((tok + 2 * q) * DM + col) * 4;
      const v2u u0 = ld64(r, o, 0), u1 = ld64(r, o, 2048 * DM * 4), u2 = ld64(r, o, 2 * 2048 * DM * 4), u3 = ld64(r, o, 3 * 2048 * DM * 4);
      fb[q] = (__builtin_bit_cast(f32x2, u0) + __builtin_bit_cast(f32x2, u1)) + (__builtin_bit_cast(f32x2, u2) + __builtin_bit_cast(f32x2, u3));
      gt[q] = __uint_as_float(ld32(r, G_OFF + (tok + 2 * q) * 4, 0));
    }
    if (__builtin_amdgcn_ballot_w64(seenS < s) != 0) {
      POLL(seenS = (int)ld32(r, svo, 0); bad = __builtin_amdgcn_ballot_w64(seenS < s) != 0; if (bad) __builtin_amdgcn_s_sleep(4));
    }
    v4u px[2][4], pm[2], cz[4];
#pragma unroll
    for (int m = 0; m < 2; ++m) {
#pragma unroll
      for (int q = 0; q < 4; ++q) px[m][q] = ld128(r, ((4 * w + q) * 2 + m) * 1280 + lane * 16, PX_OFF + s * NH * 2560);
      pm[m] = ld128(r, ((4 * w + g) * 2 + m) * 1280 + 1024 + n * 16, PX_OFF + s * NH * 2560);
    }
#pragma unroll
    for (int t = 0; t < 4; ++t) cz[t] = ld128(r, w * 1024 + lane * 16, CZ_OFF + (s * NTILE + 4 * cg + t) * 8192);
#pragma unroll
    for (int t = 0; t < 4; ++t)
#pragma unroll
      for (int m = 0; m < 2; ++m) {
        f32x4 a = {0, 0, 0, 0};
#pragma unroll
        for (int q = 0; q < 4; ++q) a = mfma(px[m][q], bz[t][q], a);
        a = mfma(pm[m], cz[t], a);
        *(LAS f32x4*)(L + w * 8192 + (t * 2 + m) * 1024 + lane * 16) = a;
      }
    bar();
    f32x2 e = {0, 0}, o = {0, 0};
#pragma unroll
    for (int q = 0; q < 8; ++q) {
      e += *(LAS f32x2*)(L + q * 8192 + (4 * ftp + fm) * 1024 + lane * 16 + frh * 8);
      o += *(LAS f32x2*)(L + q * 8192 + (4 * ftp + 2 + fm) * 1024 + lane * 16 + frh * 8);
    }
#pragma unroll
    for (int q = 0; q < 2; ++q) {
      const float y0 = gt[q] * e[q] + (1.f - gt[q]) * (fb[q].x + bf2f(b2 & 0xFFFFu));
      const float y1 = gt[q] * o[q] + (1.f - gt[q]) * (fb[q].y + __uint_as_float(b2 & 0xFFFF0000u));
      *(GAS u32*)(p.out + (tok + 2 * q) * DM + col) = pack2(y0, y1);
    }
    bar();
  }
}

DEVI int get_role(rsrc_t r, const Params& p, LAS unsigned char* L) {
  if (threadIdx.x == 0) {
    const int xcc = __builtin_amdgcn_s_getreg(20 | (3 << 11)) & 7;
    GAS u32* ctl = (GAS u32*)(p.ws + CTRL);
    int t = (int)__hip_atomic_fetch_add(ctl + (L_TICKET + xcc) * 32, 1u, __ATOMIC_RELAXED, __HIP_MEMORY_SCOPE_AGENT) + 1;
    int role = xcc * 32 + t;
    if (t >= 32) {
      int o = (int)__hip_atomic_fetch_add(ctl + L_OVF * 32, 1u, __ATOMIC_RELAXED, __HIP_MEMORY_SCOPE_AGENT) + 1;
      role = -1;
      for (int spins = 0; spins < SPIN_CAP && role < 0; ++spins) {
        int tot = 0, k = o, cand = -1;
        for (int x = 0; x < 8; ++x) {
          const int c = (int)__hip_atomic_load(ctl + (L_TICKET + x) * 32, __ATOMIC_RELAXED, __HIP_MEMORY_SCOPE_AGENT) + 1;
          tot += c;
          const int fr = c < 32 ? 32 - c : 0;
          if (cand < 0 && k < fr) cand = x * 32 + c + k;
          k -= fr;
        }
        if (tot == 256) role = cand;
        else __builtin_amdgcn_s_sleep(8);
      }
    }
    *(LAS int*)(L + LDS_BYTES - 16) = role;
  }
  __syncthreads();
  return uni(*(LAS int*)(L + LDS_BYTES - 16));
}

__global__ void __launch_bounds__(512, 2) chain_kernel(Params p) {
  LAS unsigned char* L = (LAS unsigned char*)smem_g;
  rsrc_t r = __builtin_amdgcn_make_buffer_rsrc(p.ws, 0, WS_USED, 0x00020000);
  const int role = get_role(r, p, L);
  const int x = (role >> 5) & 3, t = role & 31;
  if (role >= 128) {
    if (t < 8) role_S(p, r, L, x * 8 + t);
    else if (t < 16) role_V(p, r, L, x * 8 + t - 8);
    else if (t < 25) role_R(p, r, L, x * 9 + t - 16);
    else role_T(p, r, x * 7 + t - 25);
  } else if (role >= 0) {
    if (t < 16) role_X(p, r, L, x * 8 + (t & 7), t >> 3);
    else role_Y(p, r, L, x * 8 + (t & 7), (t >> 3) & 1);
    role_F(p, r, L, t, x, x * 9, x * 9 + 9);
    if (role < 126) role_O(p, r, L, role % 9, role / 9, 14);
  }
}

extern "C" void kernel_launch(void* const* d_in, const int* in_sizes, int n_in, void* d_out, int out_size, void* d_ws, size_t ws_size, hipStream_t stream) {
  Params p;
  p.x = (const u16*)d_in[0]; p.mem0 = (const u16*)d_in[1]; p.wq = (const u16*)d_in[2]; p.wk = (const u16*)d_in[3]; p.wv = (const u16*)d_in[4]; p.wo = (const u16*)d_in[5];
  p.w1 = (const u16*)d_in[8]; p.b1 = (const u16*)d_in[9]; p.w2 = (const u16*)d_in[10]; p.b2 = (const u16*)d_in[11]; p.gw = (const u16*)d_in[12];
  p.out = (u16*)d_out;
  p.ws = (unsigned char*)(((uintptr_t)d_ws + RING_BYTES - 1) & ~(uintptr_t)(RING_BYTES - 1));
  hipFuncSetAttribute((const void*)chain_kernel, hipFuncAttributeMaxDynamicSharedMemorySize, LDS_BYTES);
  hipMemsetAsync(p.ws, 0xFF, INIT_BYTES, stream);
  chain_kernel<<<256, 512, LDS_BYTES, stream>>>(p);
}
```

```cpp
#include <hip/hip_runtime.h>
#include <hip/hip_bf16.h>
#include <stdint.h>
#include <string.h>

typedef unsigned u32;
typedef unsigned short u16;
typedef unsigned long long u64;
typedef u32 v2u __attribute__((ext_vector_type(2)));
typedef u32 v4u __attribute__((ext_vector_type(4)));
typedef u32 v8u __attribute__((ext_vector_type(8)));
typedef float f32x2 __attribute__((ext_vector_type(2)));
typedef float f32x4 __attribute__((ext_vector_type(4)));
typedef __bf16 bf16x2 __attribute__((ext_vector_type(2)));
typedef __bf16 bf16x8 __attribute__((ext_vector_type(8)));
typedef __bf16 bf16x16 __attribute__((ext_vector_type(16)));
typedef short s16x4 __attribute__((ext_vector_type(4)));
typedef short s16x8 __attribute__((ext_vector_type(8)));
typedef __amdgpu_buffer_rsrc_t rsrc_t;
#define LAS __attribute__((address_space(3)))
#define GAS __attribute__((address_space(1)))
#define DEVI __device__ __forceinline__

constexpr int DM = 576, NH = 32, NSEG = 64, FF = 4608, NTILE = 36;
constexpr u32 SENT = 0xFFFFFFFFu;
constexpr int SC1 = 16;
constexpr int CHUNK = 16384;
constexpr int SLOT = 42 * CHUNK;
constexpr int P_OFF = 36 * CHUNK, MEM_OFF = 40 * CHUNK;
constexpr int T_HS = 41 * CHUNK + 1024, T_HV = T_HS + SLOT;
constexpr int RING_BYTES = 2 << 20;
constexpr int CTRL = 3 * SLOT, INIT_BYTES = CTRL + 65536;
constexpr int L_TICKET = 0, L_OVF = 8, L_ABORT = 9, L_PAR = 10, L_SP = 16, L_VP = 48, L_XP = 128, L_YP = 192, L_FP = 256, L_OP = 320, L_TP = 352, NTB = 60;
#define HSEG 8
#define CUT 20
constexpr int KQ_OFF = RING_BYTES + 65536;
constexpr int BZ_OFF = KQ_OFF + NSEG * NH * 16384;
constexpr int CZ_OFF = BZ_OFF + NSEG * NTILE * NH * 1024;
constexpr int PX_OFF = CZ_OFF + NSEG * NTILE * NH * 256;
constexpr int FP_OFF = PX_OFF + NSEG * NH * 2560;
constexpr int W1T_OFF = FP_OFF + 2048 * DM * 4;
constexpr int W2T_OFF = W1T_OFF + DM * FF * 2;
constexpr int G_OFF = W2T_OFF + DM * FF * 2;
constexpr int JUNK = G_OFF + 8192;
constexpr int DBG_OFF = JUNK + SLOT + CHUNK;
constexpr int WS_USED = DBG_OFF + (4 << 20);
constexpr int SPIN_CAP = 1 << 18;
constexpr int LDS_BYTES = 148 * 1024;

struct Params {
  const u16 *x, *mem0, *wq, *wk, *wv, *wo, *w1, *b1, *w2, *b2, *gw;
  u16* out;
  unsigned char* ws;
};

DEVI float bf2f(u32 b) { return __uint_as_float(b << 16); }
DEVI u32 pack2(float lo, float hi) { f32x2 v = {lo, hi}; bf16x2 b = __builtin_convertvector(v, bf16x2); return __builtin_bit_cast(u32, b); }
DEVI u32 guard(u32 v) { return v < 0xFFFFFFFEu ? v : 0xFFFFFFFEu; }
DEVI void bar() { asm volatile("s_waitcnt lgkmcnt(0)\n\ts_barrier" ::: "memory"); }
DEVI void drain() { asm volatile("s_waitcnt vmcnt(0)" ::: "memory"); }
DEVI f32x4 smf64(v4u a, v8u b, f32x4 c, int idx) { return __builtin_amdgcn_smfmac_f32_16x16x64_bf16(__builtin_bit_cast(bf16x8, a), __builtin_bit_cast(bf16x16, b), c, idx, 0, 0); }
DEVI f32x4 smf32(v2u a, v4u b, f32x4 c, int idx) { return __builtin_amdgcn_smfmac_f32_16x16x32_bf16(__builtin_bit_cast(s16x4, a), __builtin_bit_cast(s16x8, b), c, idx, 0, 0); }
DEVI f32x4 mfma(v4u a, v4u b, f32x4 c) { return __builtin_amdgcn_mfma_f32_16x16x32_bf16(__builtin_bit_cast(bf16x8, a), __builtin_bit_cast(bf16x8, b), c, 0, 0, 0); }
DEVI v8u cat(v4u a, v4u b) { return __builtin_shufflevector(a, b, 0, 1, 2, 3, 4, 5, 6, 7); }
DEVI v4u lo4(v8u a) { return __builtin_shufflevector(a, a, 0, 1, 2, 3); }
DEVI v4u hi4(v8u a) { return __builtin_shufflevector(a, a, 4, 5, 6, 7); }
DEVI float swapadd(float a, float b) {
  auto r = __builtin_amdgcn_permlane32_swap(__float_as_uint(a), __float_as_uint(b), false, false);
  return __uint_as_float(r[0]) + __uint_as_float(r[1]);
}
template <int CTRL_> DEVI float dpp(float v) { return __uint_as_float(__builtin_amdgcn_update_dpp(0, __float_as_uint(v), CTRL_, 0xF, 0xF, true)); }
DEVI float row16_max(float v) { v = fmaxf(v, dpp<0xB1>(v)); v = fmaxf(v, dpp<0x4E>(v)); v = fmaxf(v, dpp<0x141>(v)); v = fmaxf(v, dpp<0x140>(v)); return v; }
DEVI float row16_sum(float v) { v += dpp<0xB1>(v); v += dpp<0x4E>(v); v += dpp<0x141>(v); v += dpp<0x140>(v); return v; }
DEVI float x16_max(float v) { auto r = __builtin_amdgcn_permlane16_swap(__float_as_uint(v), __float_as_uint(v), false, false); return fmaxf(__uint_as_float(r[0]), __uint_as_float(r[1])); }
DEVI float x32_max(float v) { auto r = __builtin_amdgcn_permlane32_swap(__float_as_uint(v), __float_as_uint(v), false, false); return fmaxf(__uint_as_float(r[0]), __uint_as_float(r[1])); }
DEVI float x16_sum(float v) { auto r = __builtin_amdgcn_permlane16_swap(__float_as_uint(v), __float_as_uint(v), false, false); return __uint_as_float(r[0]) + __uint_as_float(r[1]); }
DEVI float x32_sum(float v) { auto r = __builtin_amdgcn_permlane32_swap(__float_as_uint(v), __float_as_uint(v), false, false); return __uint_as_float(r[0]) + __uint_as_float(r[1]); }
DEVI v4u ld128(rsrc_t r, int vo, int so) { return __builtin_amdgcn_raw_buffer_load_b128(r, vo, so, SC1); }
DEVI v2u ld64(rsrc_t r, int vo, int so) { return __builtin_amdgcn_raw_buffer_load_b64(r, vo, so, SC1); }
DEVI u32 ld32(rsrc_t r, int vo, int so) { return __builtin_amdgcn_raw_buffer_load_b32(r, vo, so, SC1); }
DEVI void st128(v4u v, rsrc_t r, int vo, int so) { __builtin_amdgcn_raw_buffer_store_b128(v, r, vo + so, 0, SC1); }
DEVI void st64(v2u v, rsrc_t r, int vo, int so) { __builtin_amdgcn_raw_buffer_store_b64(v, r, vo, so, SC1); }
DEVI void st32(u32 v, rsrc_t r, int vo, int so) { __builtin_amdgcn_raw_buffer_store_b32(v, r, vo, so, SC1); }
DEVI rsrc_t mk_rsrc(const void* ptr, int bytes) { return __builtin_amdgcn_make_buffer_rsrc((void*)ptr, 0, bytes, 0x00020000); }
DEVI v4u pl128(rsrc_t r, int vo, int so) { return __builtin_amdgcn_raw_buffer_load_b128(r, vo, so, 0); }
DEVI u32 pl32(rsrc_t r, int vo, int so) { return __builtin_amdgcn_raw_buffer_load_b32(r, vo, so, 0); }
DEVI u32 umax(u32 a, u32 b) { return a > b ? a : b; }
DEVI u32 mx4(v4u v) { return umax(umax(v.x, v.y), umax(v.z, v.w)); }
DEVI int uni(int v) { return __builtin_amdgcn_readfirstlane(v); }
DEVI int opaque(int v) { asm volatile("" : "+v"(v)); return v; }
DEVI int sopaque(int v) { asm volatile("" : "+s"(v)); return v; }
DEVI void fence_c() { asm volatile("" ::: "memory"); }

#define XSLP 4
#define LATE 4
#define FSLP 9
#define PRIO0 1
#define EXT_UP 6
#define HPACE "1"
#define PSLP 12
#define RSLP 28
#define BSLP 0
#define XUN 4
#define TDLY 700
#define XFLIP 1
#define WARM 1
#define MAR_C 8
#define VBG 3
#define VPRIO 1
#define CFIRST 0
#define MAR_S 4
#define MAR_R 2
#define PEN 15
#define TRK_UP 8
#define TRK_SH 1
#define NOUNROLL _Pragma("clang loop unroll(disable)")
#define SB() __builtin_amdgcn_sched_barrier(0)
constexpr int OOB = 0x7FFFFFF0;
DEVI bool any_sent(u32 m) { return __builtin_amdgcn_ballot_w64(m == SENT) != 0; }
DEVI bool give_up(rsrc_t r, int& rounds) {
  if (++rounds > SPIN_CAP / 256) { st32(0u, r, CTRL + L_ABORT * 128, 0); return true; }
  if (uni(ld32(r, CTRL + L_ABORT * 128, 0)) != (int)SENT) { rounds = SPIN_CAP; return true; }
  return false;
}
#define POLL(BODY)                                                                                   \
  NOUNROLL for (int cnt_ = 1;; ++cnt_) {                                                             \
    bool bad;                                                                                        \
    fence_c();                                                                                       \
    BODY;                                                                                            \
    if (__builtin_expect(!bad, 1)) break;                                                            \
    if (((cnt_ & 255) == 0 || rounds >= SPIN_CAP / 256) && give_up(r, rounds)) break;                \
  }                                                                                                  \
  fence_c();
DEVI u32 rt7() {
  u64 t;
  asm volatile("s_memrealtime %0\n\ts_waitcnt lgkmcnt(0)" : "=s"(t)::"memory");
  return (u32)t << 7;
}
#define HC1 "v_and_b32 %1, 127, %0\n\tv_cmp_ne_u32 vcc, %6, %1\n\ts_cbranch_vccz 2f\n\t"
#define HC4 HC1 HC1 HC1 HC1
#define HCAD HC4 HC4 HC4 HC4
DEVI u32 hint_wait(rsrc_t r, int vo, int so, int step) {
  int cnt = step < 0 ? 0 : 1024;
  u32 hv, t;
  asm volatile(
      "v_mov_b32 %0, -1\n\ts_nop 3\n\t"
      "1:\n\t"
      "buffer_load_dword %0, %3, %4, %5 offen sc1\n\t"
      HCAD
      "s_sub_u32 %2, %2, 1\n\t"
      "s_cbranch_scc0 1b\n\t"
      "2:"
      : "=&v"(hv), "=&v"(t), "+s"(cnt)
      : "v"(vo), "s"(r), "s"(so), "s"(step)
      : "memory", "vcc", "scc");
  return hv;
}
template <int HI>
DEVI void time_wait(u32 tv) {
  int cnt = 64;
  u32 t;
  u64 now;
  asm volatile(
      "1:\n\t"
      "s_memrealtime s[96:97]\n\t"
      "s_sub_u32 %1, %1, 1\n\t"
      "s_cbranch_scc1 2f\n\t"
      "s_waitcnt lgkmcnt(0)\n\t"
      "s_lshl_b32 s96, s96, 7\n\t"
      "v_sub_u32 %0, s96, %3\n\t"
      "v_cmp_gt_i32 s[96:97], 0, %0\n\t"
      "s_cmp_lg_u32 s%c4, 0\n\t"
      "s_cbranch_scc1 1b\n\t"
      "2:\n\t"
      "s_waitcnt lgkmcnt(0)"
      : "=&v"(t), "+s"(cnt), "=&{s[96:97]}"(now)
      : "v"(tv), "i"(96 + HI)
      : "memory", "scc");
}
DEVI int row_max_i(int d) {
  d = max(d, __builtin_amdgcn_update_dpp(d, d, 0xB1, 0xF, 0xF, false)); d = max(d, __builtin_amdgcn_update_dpp(d, d, 0x4E, 0xF, 0xF, false));
  d = max(d, __builtin_amdgcn_update_dpp(d, d, 0x141, 0xF, 0xF, false));
  return d;
}
DEVI int track(int dur, int m) { return (m > 0 && m < (200 << 7)) ? dur + ((m - dur) >> 2) : dur; }
#define DCHK "v_max3_u32 %2, v240, v241, v242\n\tv_max3_u32 %3, v244, v245, v246\n\tv_max3_u32 %2, %2, v243, v247\n\tv_max_u32 %2, %2, %3\n\tv_cmp_eq_u32 vcc, -1, %2\n\t"
#define DC1 DCHK "s_cbranch_vccz 2f\n\t"
#define DC2 DC1 DC1
#define DCAD DC2 DC2 DC2
template <int TWO>
DEVI bool data_wait(rsrc_t r, int vo, int so, v4u& a, v4u& b) {
  int cnt = 4096;
  u32 t0, t1;
  asm volatile(
      "v_mov_b32 v240, -1\n\tv_mov_b32 v241, -1\n\tv_mov_b32 v242, -1\n\tv_mov_b32 v243, -1\n\t"
      "v_mov_b32 v244, %8\n\tv_mov_b32 v245, %8\n\tv_mov_b32 v246, %8\n\tv_mov_b32 v247, %8\n\t"
      "1:\n\t"
      "buffer_load_dwordx4 v[240:243], %5, %6, %7 offen sc1\n\t"
      ".if %8\n\t"
      "buffer_load_dwordx4 v[244:247], %5, %6, %7 offen offset:1024 sc1\n\t"
      ".endif\n\t"
      "s_sub_u32 %4, %4, 1\n\t"
      "s_cbranch_scc1 2f\n\t"
      DCAD
      DCHK "s_cbranch_vccnz 1b\n\t"
      "2:"
      : "=&{v[240:243]}"(a), "=&{v[244:247]}"(b), "=&v"(t0), "=&v"(t1), "+s"(cnt)
      : "v"(vo), "s"(r), "s"(so), "i"(-TWO)
      : "memory", "vcc", "scc");
  return cnt >= 0;
}
DEVI void wait_prog(rsrc_t r, int line0, int n, int want, int lane, int& seen, int& rounds) {
  if (__builtin_amdgcn_ballot_w64(seen < want) != 0) {
    const int vo = CTRL + (line0 + (lane < n ? lane : 0)) * 128;
    POLL(seen = (int)ld32(r, vo, 0); bad = __builtin_amdgcn_ballot_w64(seen < want) != 0; if (bad) __builtin_amdgcn_s_sleep(2));
  }
}

DEVI void wait_prog2(rsrc_t r, int line0, int n, int want, int lane, int& seen0, int& seen1, int& rounds) {
  const bool hp = (want & 1) && want < HSEG;
  int sn = hp ? seen1 : seen0;
  wait_prog(r, line0 + (hp ? 32 : 0), n, want, lane, sn, rounds);
  if (hp) seen1 = sn; else seen0 = sn;
}
extern __shared__ __attribute__((aligned(16))) unsigned char smem_g[];
#define PS(k) do {} while (0)
#define TS(v)
#define TS2(v)
#define PUT2(k, v) do {} while (0)
#define FAILCNT
#define CY(v)
#define NPOLL
#define PUT(k, v) do {} while (0)
#define DUMP(slot) do {} while (0)
#define SLP_S 16
#define SLP_BG 0
#define EXPER 0
#define DLY_S 2
#define DLY_R 8
#define HINTS 6
#define SLP_R 0

template <int WO>
struct WLoad {
  rsrc_t r1, r2; int va, vb;
  DEVI void init(const u16* W1, const u16* W2, int h) {
    r1 = mk_rsrc(W1, DM * 4096 * 2); r2 = mk_rsrc(W2, DM * 4096 * 2);
    const int tid = threadIdx.x;
    va = ((tid >> 4) * 4096 + h * 128) * 2 + (tid & 15) * 16; vb = h * 128 * DM * 2 + tid * 16;
  }
  template <int M, int RD> DEVI void req(v4u (&st)[9]) {
#pragma unroll
    for (int k = 0; k < 9; ++k) st[k] = (M && WO) ? pl128(r2, vb, (9 * RD + k) * 8192) : pl128(M ? r2 : r1, va, (9 * RD + k) * 32 * 8192);
    SB();
  }
  template <int M> DEVI void put(LAS unsigned char* L, const v4u (&st)[9]) {
    const int tid = threadIdx.x;
#pragma unroll
    for (int k = 0; k < 9; ++k) {
      const int sw = (M && WO) ? (((k * 512 + tid) / 72) >> 3) & 1 : (tid >> 7) & 1;
      *(LAS v4u*)(L + M * 73728 + k * 8192 + ((tid * 16) ^ (sw << 6))) = st[k];
    }
    SB();
  }
};
DEVI u32 lo2(u32 a, u32 b) { return __builtin_amdgcn_perm(b, a, 0x05040100u); }
DEVI u32 hi2(u32 a, u32 b) { return __builtin_amdgcn_perm(b, a, 0x07060302u); }
template <int RD>
DEVI void get_w1_sparse(LAS unsigned char* M, int wq, int lane, v8u (&wt)[2][9]) {
  const int n = lane & 15, gb = lane >> 4;
  LAS unsigned char* b = M + ((gb >> 1) + 8 * (gb & 1)) * 256 + ((wq * 64) ^ ((gb & 1) << 6)) + n * 4;
#pragma unroll
  for (int i = 0; i < 36; ++i) {
    const int hs = 9 * RD + (i >> 2), d = 32 * (i >> 2) + 16 * (i & 1) + 4 * ((i >> 1) & 1);
    const u32 x = *(LAS u32*)(b + d * 256), y = *(LAS u32*)(b + (d + 2) * 256);
    wt[0][hs >> 1][4 * (hs & 1) + (i & 3)] = lo2(x, y);
    wt[1][hs >> 1][4 * (hs & 1) + (i & 3)] = hi2(x, y);
  }
}
template <int RD>
DEVI void get_w1_dense(LAS unsigned char* M, int wq, int lane, v4u (&wt)[2][18]) {
  const int n = lane & 15, gb = lane >> 4;
  LAS unsigned char* b = M + 8 * gb * 256 + ((wq * 64) ^ ((gb & 1) << 6)) + n * 4;
#pragma unroll
  for (int i = 0; i < 36; ++i) {
    const int d = 32 * (i >> 2) + 2 * (i & 3);
    const u32 x = *(LAS u32*)(b + d * 256), y = *(LAS u32*)(b + (d + 1) * 256);
    wt[0][9 * RD + (i >> 2)][i & 3] = lo2(x, y);
    wt[1][9 * RD + (i >> 2)][i & 3] = hi2(x, y);
  }
}
DEVI int tile_of(int c, int i) { return i < 8 ? 9 * c + (c & 1) + i : (c & 1 ? 9 * c : 9 * c + 8); }
template <int RD>
DEVI void get_w2(LAS unsigned char* M, int c, int lane, v8u (&wt)[9][2]) {
  const int n = lane & 15, gb = lane >> 4;
  const u32 sel = (c & 1) ? 0x07060302u : 0x05040100u;
  LAS unsigned char* b = M + 8 * gb * 1152 + n * 4;
#pragma unroll
  for (int pr = 0; pr < 5; ++pr) {
    LAS unsigned char* bp = b + ((64 * (tile_of(c, 2 * pr) >> 1)) ^ ((gb & 1) << 6));
#pragma unroll
    for (int q = 0; q < 8; ++q) {
      const int e = 32 * (q >> 2) + 2 * (q & 3);
      const u32 x = *(LAS u32*)(bp + e * 1152), y = *(LAS u32*)(bp + (e + 1) * 1152);
      if (pr < 4) { wt[2 * pr][RD][q] = lo2(x, y); wt[2 * pr + 1][RD][q] = hi2(x, y); }
      else wt[8][RD][q] = __builtin_amdgcn_perm(y, x, sel);
    }
  }
}
#define LOAD_W(WO, W1, W2, GET, MINE, IDX)                                                       \
  {                                                                                              \
    PS(0);                                                                                       \
    WLoad<WO> wl; wl.init(W1, W2, h);                                                            \
    v4u sa[9], sb_[9], sc[9];                                                                    \
    wl.req<0, 0>(sa); wl.req<1, 0>(sb_);                                                         \
    wl.put<0>(L, sa); wl.req<0, 1>(sc); bar();                                                   \
    wl.put<1>(L, sb_); wl.req<1, 1>(sa);                                                         \
    if (MINE == 0) GET<0>(L, IDX, lane, wt);                                                     \
    bar();                                                                                       \
    wl.put<0>(L, sc);                                                                            \
    if (MINE == 1) GET<0>(L + 73728, IDX, lane, wt);                                             \
    bar();                                                                                       \
    wl.put<1>(L, sa);                                                                            \
    if (MINE == 0) GET<1>(L, IDX, lane, wt);                                                     \
    bar();                                                                                       \
    if (MINE == 1) GET<1>(L + 73728, IDX, lane, wt);                                             \
    bar();                                                                                       \
    PS(5);                                                                                       \
  }

constexpr int AIMG = 1088, QIMG = 2 * AIMG, KIMG = 2304, IMG = QIMG + KIMG;
constexpr int LM_IMG = 0, LM_MEM = 5120, LM_RED = 0, LM_T = 14336, LM_KQ = 16384, LM_BZ = 4096;

DEVI int wait_parity(rsrc_t r, int& rounds) {
  int v;
  POLL(v = uni(ld32(r, CTRL + L_PAR * 128, 0)); bad = v == (int)SENT; if (bad) __builtin_amdgcn_s_sleep(4));
  return (v & 1) * 8192;
}
DEVI int next_slot(int sb, int par) { return sb + SLOT >= par + 3 * SLOT ? par : sb + SLOT; }

DEVI f32x4 zero4() { f32x4 z = {0, 0, 0, 0}; asm volatile("" : "+v"(z)); return z; }
DEVI float addf(float a, float b) { float o; asm("v_add_f32 %0, %1, %2" : "=v"(o) : "v"(a), "v"(b)); return o; }
DEVI f32x2 fold(f32x4 a) { float x = a[0] + a[2]; asm("" : "+v"(x)); float y = a[1] + a[3]; asm("" : "+v"(y)); return f32x2{x, y}; }
template <int TWO>
struct Gather {
  v4u a, b;
  DEVI void run(rsrc_t r, LAS unsigned char* L, int l16, int c0, int sb, int& rounds) {
    NOUNROLL for (;;) {
      if (__builtin_expect(data_wait<TWO>(r, l16, sb + MEM_OFF + (TWO ? c0 * 1024 : CHUNK), a, b), 1)) break;
      rounds += 15;
      if (give_up(r, rounds)) break;
    }
    *(LAS v4u*)(L + LM_MEM + c0 * 1024 + l16) = a;
    if (TWO) *(LAS v4u*)(L + LM_MEM + c0 * 1024 + 1024 + l16) = b;
  }
  DEVI void release() { asm volatile("s_waitcnt vmcnt(0)" ::"{v[240:243]}"(a), "{v[244:247]}"(b) : "memory"); }
};
template <int RSTEP, class F>
DEVI void stage1(LAS unsigned char* L, const v8u (&wt)[2][9], f32x4 a0, f32x4 a1, int l16, int idx, LAS unsigned char* wp, F mid) {
  v4u af[9];
#pragma unroll
  for (int ks = 0; ks < 9; ++ks) af[ks] = *(LAS v4u*)(L + LM_MEM + ks * 1024 + l16);
  SB();
#pragma unroll
  for (int ks = 0; ks < 9; ++ks) {
    a0 = smf64(af[ks], wt[0][ks], a0, idx); a1 = smf64(af[ks], wt[1][ks], a1, idx);
    if (ks == 5) { SB(); mid(); SB(); }
  }
  const f32x2 p0 = fold(a0), p1 = fold(a1);
  *(LAS u32*)wp = pack2(p0.x, p1.x);
  *(LAS u32*)(wp + RSTEP) = pack2(p0.y, p1.y);
}
DEVI int aimg_w(int wq, int lane) { const int n = lane & 15, g = lane >> 4; return (wq >> 1) * AIMG + (2 * (wq & 1) + (n >> 3)) * 272 + g * 64 + (n & 1) * 32 + ((n & 7) >> 1) * 4; }
DEVI int aimg_r(int lane) { return (lane >> 4) * 272 + (lane & 15) * 16; }
DEVI int kimg_w(int wk, int lane) { const int n = lane & 15, g = lane >> 4; return (wk >> 1) * 1152 + (n >> 2) * 288 + g * 64 + (wk & 1) * 16 + (n & 3) * 4; }
DEVI int kimg_r(int k, int g, int key) { return (k >> 1) * 1152 + g * 288 + key * 32 + (k & 1) * 16; }
DEVI float row16_max_asm(float v) {
  asm volatile("s_nop 1\n\tv_max_f32_dpp %0, %0, %0 quad_perm:[1,0,3,2] row_mask:0xf bank_mask:0xf\n\t"
               "s_nop 1\n\tv_max_f32_dpp %0, %0, %0 quad_perm:[2,3,0,1] row_mask:0xf bank_mask:0xf\n\t"
               "s_nop 1\n\tv_max_f32_dpp %0, %0, %0 row_half_mirror row_mask:0xf bank_mask:0xf\n\t"
               "s_nop 1\n\tv_max_f32_dpp %0, %0, %0 row_mirror row_mask:0xf bank_mask:0xf"
               : "+v"(v));
  return v;
}

constexpr float SCL = 0.08838834764831845f * 1.4426950408889634f;

DEVI void role_S(const Params& p, rsrc_t r, LAS unsigned char* L, int h) {
  const int lane = threadIdx.x & 63, w = uni(threadIdx.x >> 6), n = lane & 15, g = lane >> 4;
  const int idx = (lane & 2) ? 0xEEEE : 0x4444, l16 = opaque(lane * 16);
  int rounds = 0;
  v8u wt[2][9];
  if (w < 4) LOAD_W(0, p.wq, p.wk, get_w1_sparse, 0, w)
  else LOAD_W(0, p.wq, p.wk, get_w1_sparse, 1, w - 4)
  const int par = wait_parity(r, rounds);
  int sb = par;
  if (w < 2) {
    LAS unsigned char* wa = L + LM_IMG + aimg_w(w, lane);
    const int pvo = P_OFF + (h >> 3) * CHUNK + (h & 7) * 1024 + w * 512;
    const int pxo = pvo + (n >> 3) * 128 + (g >> 1) * 64 + ((n >> 2) & 1) * 32 + (g & 1) * 16 + (n & 3) * 4;
    const int pmo = n < 4 ? pvo + 256 + (g >> 1) * 32 + (n >> 1) * 16 + (g & 1) * 8 + (n & 1) * 4 : OOB;
    const int cn = (n & 8) | ((n & 3) << 1) | ((n >> 2) & 1);
    const int kmc = ((n & 1) << 2) | (n & 2) | ((n >> 2) & 1);
    const float mk = n < 8 ? 1.f : 0.f;
    int sprev = par + 2 * SLOT;
    for (int s = sopaque(-1); s < NSEG; ++s) {
      const f32x4 z0 = zero4(), z1 = zero4();
      f32x4 sa = zero4(), sbb = zero4(), sm = zero4();
      LAS unsigned char* kq = L + LM_KQ + (s & 1) * 16384;
      v4u kf[2][4];
      const int so = s < 0 ? JUNK : sb;
      Gather<1> ga;
      ga.run(r, L, l16, 2 * w, sb, rounds);
      TS(t0);
      bar();
      stage1<16>(L, wt, z0, z1, l16, idx, wa, [&]() {
#pragma unroll
        for (int k = 0; k < 8; ++k) kf[k >> 2][k & 3] = *(LAS v4u*)(kq + k * 1024 + (g * 16 + cn) * 16);
      });
      ga.release();
      bar();
      TS(t1);
      v4u qa[2], km[4];
#pragma unroll
      for (int k = 0; k < 2; ++k) qa[k] = *(LAS v4u*)(L + LM_IMG + k * AIMG + aimg_r(lane ^ w));
#pragma unroll
      for (int k = 0; k < 4; ++k) km[k] = *(LAS v4u*)(L + LM_IMG + QIMG + kimg_r(k, g, kmc));
      SB();
      sa = smf64(qa[0], cat(kf[0][0], kf[0][1]), sa, idx);
      sbb = smf64(qa[0], cat(kf[1][0], kf[1][1]), sbb, idx);
      sa = smf64(qa[1], cat(kf[0][2], kf[0][3]), sa, idx);
      sm = smf64(qa[0], cat(km[0], km[1]), sm, idx);
      sbb = smf64(qa[1], cat(kf[1][2], kf[1][3]), sbb, idx);
      sm = smf64(qa[1], cat(km[2], km[3]), sm, idx);
      SB();
      const float va = sa[0] + sa[2], vb = sbb[0] + sbb[2], vm = sm[0] + sm[2];
      const float mx = row16_max_asm(fmaxf(fmaxf(va, vb), vm));
      const float ea = __builtin_amdgcn_exp2f((va - mx) * SCL), eb = __builtin_amdgcn_exp2f((vb - mx) * SCL), em = __builtin_amdgcn_exp2f((vm - mx) * SCL);
      const float em2 = dpp<0x104>(em);
      const float inv = __builtin_amdgcn_rcpf(row16_sum(__builtin_fmaf(em, mk, ea + eb)));
      st32(guard(pack2(ea * inv, eb * inv)), r, pxo, so);
      st32(guard(pack2(em * inv, em2 * inv)), r, pmo, so);
      SB();
      *(LAS u32*)(L + LM_T + w * 4) = rt7();
      TS(t2);
      PUT(w, t0); PUT(8 + w, t1); PUT(10 + w, t2);
      st32(SENT, r, pxo, sprev); st32(SENT, r, pmo, sprev);
      __builtin_amdgcn_s_sleep(PSLP - 8);
      sprev = s < 0 ? sprev : sb;
      sb = s < 0 ? sb : next_slot(sb, par);
    }
  } else if (w < 4) {
    LAS unsigned char* wa = L + LM_IMG + aimg_w(w, lane);
    const int m = w - 2, qi = 2 * n + m;
    const int po = (h * 2 + m) * 1280;
    for (int s = sopaque(-1); s < NSEG; ++s) {
      const f32x4 z0 = zero4(), z1 = zero4();
      bar();
      stage1<16>(L, wt, z0, z1, l16, idx, wa, []() {});
      drain();
      bar();
      __builtin_amdgcn_s_sleep(XSLP);
      LAS unsigned char* kq = L + LM_KQ + (s & 1) * 16384 + l16;
      f32x4 xa = {0, 0, 0, 0}, xb = {0, 0, 0, 0}, xm = {0, 0, 0, 0};
#pragma unroll
      for (int k = 0; k < 4; ++k) {
        const v4u qf = *(LAS v4u*)(kq + 8192 + (m * 4 + k) * 1024);
        xa = mfma(*(LAS v4u*)(kq + k * 1024), qf, xa);
        xb = mfma(*(LAS v4u*)(kq + (4 + k) * 1024), qf, xb);
        xm = mfma(*(LAS v4u*)(L + LM_IMG + QIMG + kimg_r(k, g, n & 7)), qf, xm);
      }
      float mx = fmaxf(fmaxf(xm[0], xm[1]), fmaxf(xm[2], xm[3]));
#pragma unroll
      for (int k = 0; k < 4; ++k) {
        xa[k] = (8 * g + 2 * k <= qi) ? xa[k] : -INFINITY;
        xb[k] = (8 * g + 2 * k + 1 <= qi) ? xb[k] : -INFINITY;
        mx = fmaxf(mx, fmaxf(xa[k], xb[k]));
      }
      mx = x32_max(x16_max(mx));
      float sum = 0.f, summ = 0.f;
#pragma unroll
      for (int k = 0; k < 4; ++k) {
        xa[k] = __builtin_amdgcn_exp2f((xa[k] - mx) * SCL); xb[k] = __builtin_amdgcn_exp2f((xb[k] - mx) * SCL); xm[k] = __builtin_amdgcn_exp2f((xm[k] - mx) * SCL);
        sum += xa[k] + xb[k]; summ += xm[k];
      }
      sum += g < 2 ? summ : 0.f;
      const float inv = __builtin_amdgcn_rcpf(x32_sum(x16_sum(sum)));
      v4u px; v2u pm2;
#pragma unroll
      for (int k = 0; k < 4; ++k) px[k] = pack2(xa[k] * inv, xb[k] * inv);
      pm2.x = pack2(xm[0] * inv, xm[1] * inv); pm2.y = pack2(xm[2] * inv, xm[3] * inv);
      const int so = s < 0 ? JUNK : PX_OFF + s * NH * 2560;
      st128(px, r, po + l16, so);
      st64(pm2, r, g < 2 ? po + 1024 + n * 16 + g * 8 : OOB, so);
    }
    drain();
  } else if (w < 6) {
    LAS unsigned char* wa = L + LM_IMG + QIMG + kimg_w(w - 4, lane);
    for (int s = sopaque(-1); s < NSEG; ++s) {
      const f32x4 z0 = zero4(), z1 = zero4();
      Gather<1> ga;
      ga.run(r, L, l16, 2 * w - 4, sb, rounds);
      TS(t0);
      bar();
      stage1<32>(L, wt, z0, z1, l16, idx, wa, []() {});
      bar();
      ga.release();
      PUT(w, t0);
      __builtin_amdgcn_s_sleep(PSLP);
      sb = s < 0 ? sb : next_slot(sb, par);
    }
  } else if (w == 6) {
    LAS unsigned char* wa = L + LM_IMG + QIMG + kimg_w(2, lane);
    for (int s = sopaque(-1); s < NSEG; ++s) {
      const f32x4 z0 = zero4(), z1 = zero4();
      Gather<0> ga;
      ga.run(r, L, l16, 8, sb, rounds);
      TS(t0);
      bar();
      stage1<32>(L, wt, z0, z1, l16, idx, wa, []() {});
      bar();
      ga.release();
      PUT(w, t0);
      __builtin_amdgcn_s_sleep(PSLP);
      if (s > 0 && lane == 0) st32(s - 1, r, CTRL + (L_SP + h) * 128, 0);
      sb = s < 0 ? sb : next_slot(sb, par);
    }
  } else {
    LAS unsigned char* wa = L + LM_IMG + QIMG + kimg_w(3, lane);
    int seen0 = -1, dur = 60 << 7;
    auto fetch = [&](int s) {
      wait_prog(r, L_XP + h, 1, s, lane, seen0, rounds);
      v4u t[16];
#pragma unroll
      for (int k = 0; k < 16; ++k) t[k] = ld128(r, l16 + (k & 3) * 1024, KQ_OFF + (s * NH + h) * 16384 + (k >> 2) * 4096);
#pragma unroll
      for (int k = 0; k < 16; ++k) *(LAS v4u*)(L + LM_KQ + (s & 1) * 16384 + l16 + k * 1024) = t[k];
    };
    const int ho = lane == 0 ? T_HS + h * 128 : OOB;
    for (int s = sopaque(-1); s < NSEG; ++s) {
      const f32x4 z0 = zero4(), z1 = zero4();
      bar();
      const u32 tb = rt7();
      st32(((tb + dur) & ~127u) | (s & 127), r, ho, par);
      stage1<32>(L, wt, z0, z1, l16, idx, wa, []() {});
      bar();
      if (s + 1 < NSEG) fetch(s + 1);
      if (s >= 0) dur = track(dur, max((int)(uni(*(LAS u32*)(L + LM_T)) - tb), (int)(uni(*(LAS u32*)(L + LM_T + 4)) - tb)));
    }
  }
  bar();
  if (w == 0 && lane == 0) st32(NSEG - 1, r, CTRL + (L_SP + h) * 128, 0);
  DUMP(h);
}

template <int RD>
DEVI void get_v1(LAS unsigned char* M, int w, int lane, v8u (&w1)[9]) {
  const int n = lane & 15, gb = lane >> 4;
  const u32 sel = (w & 1) ? 0x07060302u : 0x05040100u;
  LAS unsigned char* b = M + ((gb >> 1) + 8 * (gb & 1)) * 256 + (((w >> 1) * 64) ^ ((gb & 1) << 6)) + n * 4;
#pragma unroll
  for (int i = 0; i < 36; ++i) {
    const int hs = 9 * RD + (i >> 2), d = 32 * (i >> 2) + 16 * (i & 1) + 4 * ((i >> 1) & 1);
    w1[hs >> 1][4 * (hs & 1) + (i & 3)] = __builtin_amdgcn_perm(*(LAS u32*)(b + (d + 2) * 256), *(LAS u32*)(b + d * 256), sel);
  }
}
template <int RD>
DEVI void get_v2(LAS unsigned char* M, int w, int lane, v8u (&w2)[5][2]) {
  const int n = lane & 15, gb = lane >> 4;
  const u32 sel = (w & 1) ? 0x07060302u : 0x05040100u;
  LAS unsigned char* b = M + 8 * gb * 1152 + n * 4;
#pragma unroll
  for (int pr = 0; pr < 3; ++pr) {
    LAS unsigned char* bp = b + ((64 * (pr < 2 ? 2 * w + pr : 16 + ((w >> 1) & 1))) ^ ((gb & 1) << 6));
#pragma unroll
    for (int q = 0; q < 8; ++q) {
      const int e = 32 * (q >> 2) + 2 * (q & 3);
      const u32 x = *(LAS u32*)(bp + e * 1152), y = *(LAS u32*)(bp + (e + 1) * 1152);
      if (pr < 2) { w2[2 * pr][RD][q] = lo2(x, y); w2[2 * pr + 1][RD][q] = hi2(x, y); }
      else w2[4][RD][q] = __builtin_amdgcn_perm(y, x, sel);
    }
  }
}
template <int GATHER, int FIVE, int HINT>
DEVI void v_loop(const Params& p, rsrc_t r, LAS unsigned char* L, int h, int w, const v8u (&w1)[9], const v8u (&w2)[5][2]) {
  const int lane = threadIdx.x & 63, n = lane & 15, g = lane >> 4;
  const int idx = (lane & 2) ? 0xEEEE : 0x4444, l16 = opaque(lane * 16);
  int rounds = 0;
  const int par = wait_parity(r, rounds);
  LAS unsigned char* wa = L + LM_IMG + aimg_w(w >> 1, lane) + (w & 1) * 2;
  LAS unsigned char* img = L + LM_IMG + aimg_r(lane);
  const int lr0 = (4 * w + g) * CHUNK + h * 256 + n * 16, lr1 = g == 0 ? (28 + w) * CHUNK + h * 256 + n * 16 : OOB;
  const int lz0 = (4 * w + g) * 8192 + h * 256 + n * 16, lz1 = g == 0 ? (28 + w) * 8192 + h * 256 + n * 16 : OOB;
  const int ho = (w == 7 && lane == 0) ? T_HV + h * 128 : OOB;
  int sb = par, sprev = par + 2 * SLOT, dur = 50 << 7;
  u32 tb = 0;
  for (int s = sopaque(-1); s < NSEG; ++s) {
    f32x4 a1 = zero4(), a[5];
#pragma unroll
    for (int i = 0; i < 4 + FIVE; ++i) a[i] = zero4();
    const int so0 = opaque(lr0 + (s < 0 ? JUNK : sb)), so1 = opaque(lr1 + (s < 0 ? JUNK : sb));
    Gather<GATHER == 2> ga;
    if (GATHER) ga.run(r, L, l16, GATHER == 2 ? 2 * w : 8, sb, rounds);
    else drain();
    if (HINT && s > 0) {
      const int m = row_max_i((int)(*(LAS u32*)(L + LM_T + (lane & 7) * 4) - tb));
      dur = track(dur, uni(m));
    }
    bar();
    if (HINT) tb = rt7();
    v4u af[9];
#pragma unroll
    for (int ks = 0; ks < 9; ++ks) af[ks] = *(LAS v4u*)(L + LM_MEM + ks * 1024 + l16);
    SB();
    if (HINT) { st32(((tb + dur) & ~127u) | (s & 127), r, ho, par); SB(); }
#pragma unroll
    for (int ks = 0; ks < 9; ++ks) a1 = smf64(af[ks], w1[ks], a1, idx);
    const f32x2 f1 = fold(a1);
    const u32 v2 = pack2(f1.x, f1.y);
    *(LAS u16*)wa = (u16)v2;
    *(LAS u16*)(wa + 16) = (u16)(v2 >> 16);
    if (GATHER) ga.release();
    TS(t0);
    bar();
    v4u va[2];
#pragma unroll
    for (int k = 0; k < 2; ++k) va[k] = *(LAS v4u*)(img + k * AIMG);
    auto fin = [&](int i) { const f32x2 f = fold(a[i]); return guard(pack2(f.x, f.y)); };
    auto xch = [&](u32 u0, u32 u1, u32 u2, u32 u3) {
      const auto e0 = __builtin_amdgcn_permlane32_swap(u0, u2, false, false);
      const auto e1 = __builtin_amdgcn_permlane32_swap(u1, u3, false, false);
      const auto f0 = __builtin_amdgcn_permlane16_swap(e0[0], e1[0], false, false);
      const auto f1 = __builtin_amdgcn_permlane16_swap(e0[1], e1[1], false, false);
      return v4u{f0[0], f0[1], f1[0], f1[1]};
    };
    v4u o1;
    if (FIVE) a[4] = smf64(va[0], w2[4][0], a[4], idx);
    a[0] = smf64(va[0], w2[0][0], a[0], idx);
    if (FIVE) a[4] = smf64(va[1], w2[4][1], a[4], idx);
    else a[2] = smf64(va[0], w2[2][0], a[2], idx);
    a[0] = smf64(va[1], w2[0][1], a[0], idx);
    if (FIVE) a[2] = smf64(va[0], w2[2][0], a[2], idx);
    a[1] = smf64(va[0], w2[1][0], a[1], idx);
    SB();
    u32 u4;
    if (FIVE) u4 = fin(4);
    a[2] = smf64(va[1], w2[2][1], a[2], idx);
    SB();
    if (FIVE) { o1 = xch(u4, u4, u4, u4); st128(o1, r, so1, 0); }
    a[3] = smf64(va[0], w2[3][0], a[3], idx);
    a[1] = smf64(va[1], w2[1][1], a[1], idx);
    SB();
    const u32 u0 = fin(0);
    a[3] = smf64(va[1], w2[3][1], a[3], idx);
    SB();
    const u32 u2 = fin(2);
    const auto e0 = __builtin_amdgcn_permlane32_swap(u0, u2, false, false);
    SB();
    const auto e1 = __builtin_amdgcn_permlane32_swap(fin(1), fin(3), false, false);
    const auto f0 = __builtin_amdgcn_permlane16_swap(e0[0], e1[0], false, false);
    const auto f1_ = __builtin_amdgcn_permlane16_swap(e0[1], e1[1], false, false);
    const v4u o0 = v4u{f0[0], f0[1], f1_[0], f1_[1]};
    st128(o0, r, so0, 0);
    SB();
    *(LAS u32*)(L + LM_T + w * 4) = rt7();
    TS(t2);
    PUT(w, t0); PUT(10 + (w & 1), t2);
    __builtin_amdgcn_s_sleep(VBG);
    const int zo = s < 0 ? JUNK : CZ_OFF + s * NTILE * 8192;
    st128(o0, r, lz0, zo);
    st128(v4u{SENT, SENT, SENT, SENT}, r, lr0, sprev);
    if (FIVE) { st128(o1, r, lz1, zo); st128(v4u{SENT, SENT, SENT, SENT}, r, lr1, sprev); }
    if (w == 6 && s > 0 && lane == 0) st32(s - 1, r, CTRL + (L_VP + h) * 128, 0);
    sprev = s < 0 ? sprev : sb;
    sb = s < 0 ? sb : next_slot(sb, par);
  }
  drain();
}

DEVI void role_V(const Params& p, rsrc_t r, LAS unsigned char* L, int h) {
  const int lane = threadIdx.x & 63, w = uni(threadIdx.x >> 6);
  v8u w1[9], w2[5][2];
  {
    PS(0);
    WLoad<1> wl; wl.init(p.wv, p.wo, h);
    v4u sa[9], sb_[9], sc[9];
    wl.req<0, 0>(sa); wl.req<1, 0>(sb_);
    wl.put<0>(L, sa); wl.req<0, 1>(sc); bar();
    wl.put<1>(L, sb_); wl.req<1, 1>(sa); get_v1<0>(L, w, lane, w1); bar();
    wl.put<0>(L, sc); get_v2<0>(L + 73728, w, lane, w2); bar();
    wl.put<1>(L, sa); get_v1<1>(L, w, lane, w1); bar();
    get_v2<1>(L + 73728, w, lane, w2); bar();
    PS(5);
  }
  if (w < 4) { __builtin_amdgcn_s_setprio(VPRIO); v_loop<2, 0, 0>(p, r, L, h, w, w1, w2); }
  else if (w == 4) v_loop<1, 1, 0>(p, r, L, h, w, w1, w2);
  else if (w < 7) v_loop<0, 1, 0>(p, r, L, h, w, w1, w2);
  else v_loop<0, 1, 1>(p, r, L, h, w, w1, w2);
  bar();
  if (w == 0 && lane == 0) st32(NSEG - 1, r, CTRL + (L_VP + h) * 128, 0);
  DUMP(32 + h);
}

DEVI u64 cyc() { u64 t; asm volatile("s_memtime %0\n\ts_waitcnt lgkmcnt(0)" : "=s"(t)::"memory"); return t; }

DEVI void role_R(const Params& p, rsrc_t r, LAS unsigned char* L, int j) {
  const int lane = threadIdx.x & 63, w = uni(threadIdx.x >> 6), n = lane & 15, g = lane >> 4;
  int rounds = 0;
  if (j == 0 && w == 4) {
    u32 best[2] = {~0u, ~0u};
    if (lane == 0) {
      for (int it = 0; it < 3; ++it)
        for (int q = 0; q < 2; ++q) {
          GAS u32* a = (GAS u32*)(p.ws + MEM_OFF + CHUNK + q * 8192 + 7168);
          const u64 t0 = cyc();
          u32 v = __hip_atomic_fetch_add(a, 1u, __ATOMIC_RELAXED, __HIP_MEMORY_SCOPE_AGENT);
          asm volatile("s_waitcnt vmcnt(0)" ::"v"(v) : "memory");
          const u32 d = (u32)(cyc() - t0);
          best[q] = d < best[q] ? d : best[q];
        }
      st32(best[1] < best[0] ? 1u : 0u, r, CTRL + L_PAR * 128, 0);
    }
  }
  const int par = wait_parity(r, rounds);
  int sb = par;
  if (w < 4) {
    const int idx = (lane & 2) ? 0xEEEE : 0x4444;
    const int pxo = opaque(P_OFF + w * CHUNK + (g >> 1) * 1024 + (n >> 3) * 512 + (g & 1) * 128 + (n & 7) * 16);
    const int pmo = P_OFF + w * CHUNK + g * 1024 + (n >> 3) * 512 + 256 + (n & 7) * 8;
    const int co = j * CHUNK + w * 2048 + lane * 16;
    const int redw = LM_RED + w * 512 + (g & 1) * 256 + (n >> 3) * 64 + (g >> 1) * 32 + (n & 7) * 4;
    const int hio = (lane < 32 ? T_HS : T_HV) + (8 * w + ((lane >> 2) & 7)) * 128;
    for (int s = sopaque(-2); s < NSEG - 1; ++s) {
      v4u bz[8];
#pragma unroll
      for (int q = 0; q < 8; ++q) bz[q] = *(LAS v4u*)(L + LM_BZ + (s & 1) * 32768 + (8 * w + q) * 1024 + lane * 16);
      if (s > 0) __builtin_amdgcn_s_sleep(RSLP);
      const u32 hv = hint_wait(r, hio, par, s);
      TS(tb);
      const u32 tv = s < 0 ? rt7() : hv + (lane < 32 ? MAR_R : MAR_C) * 128;
      auto attempt = [&](bool first) {
        v4u px[4]; v2u pm[2];
        if (first) time_wait<0>(tv);
#pragma unroll
        for (int q = 0; q < 4; ++q) px[q] = ld128(r, pxo + q * 2048, sb);
#pragma unroll
        for (int q = 0; q < 2; ++q) pm[q] = ld64(r, pmo + q * 4096, sb);
        if (first) time_wait<1>(tv);
        const v4u c0 = ld128(r, co, sb), c1 = ld128(r, co + 1024, sb);
        f32x4 a = zero4(), b = zero4();
        SB();
        a = smf64(px[0], cat(bz[0], bz[1]), a, idx); u32 mp = mx4(px[0]); SB();
        b = smf64(px[1], cat(bz[2], bz[3]), b, idx); mp = umax(mp, mx4(px[1])); SB();
        a = smf64(px[2], cat(bz[4], bz[5]), a, idx); mp = umax(mp, mx4(px[2])); SB();
        b = smf64(px[3], cat(bz[6], bz[7]), b, idx); mp = umax(mp, mx4(px[3]));
        mp = umax(mp, umax(umax(pm[0].x, pm[0].y), umax(pm[1].x, pm[1].y)));
        asm volatile("" : "+v"(mp));
        SB();
        a = smf32(pm[0], c0, a, idx); mp = umax(mp, mx4(c0)); SB();
        b = smf32(pm[1], c1, b, idx); mp = umax(mp, mx4(c1));
        const f32x2 fa = fold(a), fb = fold(b);
        *(LAS float*)(L + (s & 1) * 2048 + redw) = fa.x + fb.x;
        *(LAS float*)(L + (s & 1) * 2048 + redw + 128) = fa.y + fb.y;
        return any_sent(mp);
      };
      if (__builtin_expect(attempt(true), 0) && s >= 0) {
        POLL(FAILCNT bad = attempt(false));
      }
      asm volatile("" ::"v"(hv));
      TS(t0);
      bar();
      PUT(w, t0); PUT(4 + w, tb); if (w == 1) PUT(12, tb);
      __builtin_amdgcn_s_sleep(4);
      sb = s < 0 ? sb : next_slot(sb, par);
    }
  } else {
    const int mo = MEM_OFF + (j >> 5) * CHUNK + (j & 31) * 256 + lane * 4, l16 = opaque(lane * 16);
    int snext = par + SLOT, seen0 = -1, seen1 = -1;
    auto fetch = [&](int s) {
      wait_prog2(r, L_YP + 8 * (w - 4), 8, s, lane, seen0, seen1, rounds);
      v4u t[8];
#pragma unroll
      for (int q = 0; q < 8; ++q) t[q] = ld128(r, l16 + (q & 3) * 1024, BZ_OFF + (s * NTILE + j) * 32768 + (w - 4) * 8192 + (q >> 2) * 4096);
#pragma unroll
      for (int q = 0; q < 8; ++q) *(LAS v4u*)(L + LM_BZ + (s & 1) * 32768 + (8 * (w - 4) + q) * 1024 + l16) = t[q];
    };
    if (w == 4) {
      const u16* src = p.mem0 + ((lane >> 4) * 2 + ((lane >> 2) & 1)) * DM + 32 * (j >> 1) + (j & 1) + 16 * ((lane >> 3) & 1) + 4 * (lane & 3);
      st32(guard((u32)src[0] | ((u32)src[2] << 16)), r, mo, sb);
      __builtin_amdgcn_s_setprio(2);
      for (int s = sopaque(-2); s < NSEG - 1; ++s) {
        LAS unsigned char* red = L + LM_RED + (s & 1) * 2048 + lane * 8;
        const int so = s < 0 ? JUNK : snext;
        if (s + 1 >= 0) fetch(s + 1);
        drain();
        bar();
        f32x2 t[4];
#pragma unroll
        for (int q = 0; q < 4; ++q) t[q] = *(LAS f32x2*)(red + q * 512);
        const f32x2 u = (t[0] + t[1]) + (t[2] + t[3]);
        st32(guard(pack2(u.x, u.y)), r, mo, so);
        SB();
        TS2(t2);
        if (s >= 0) {
          st32(SENT, r, mo, sb);
          sb = snext;
          snext = next_slot(snext, par);
        }
        PUT2(10, t2);
      }
    } else {
      for (int s = sopaque(-2); s < NSEG - 1; ++s) {
        if (s + 1 >= 0) fetch(s + 1);
        bar();
        __builtin_amdgcn_s_sleep(4);
      }
    }
  }
  DUMP(64 + j);
}

DEVI int ximg_rd(int lane) { const int n = lane & 15, g = lane >> 4; return n * 64 + (g ^ ((n >> 3) << 1)) * 16; }
DEVI int ximg_piece(int lane) { return ((lane & 3) ^ ((lane >> 5) << 1)) * 16; }
template <int DEPTH>
DEVI void proj32(LAS unsigned char* xb, const v4u (&wt)[2][18], v4u (&o)[2]) {
  f32x4 acc[2][2];
#pragma unroll
  for (int i = 0; i < 4; ++i) acc[i >> 1][i & 1] = zero4();
  v4u xf[DEPTH][2];
#pragma unroll
  for (int ks = 0; ks < DEPTH - 1; ++ks) { xf[ks][0] = *(LAS v4u*)(xb + ks * 1024); xf[ks][1] = *(LAS v4u*)(xb + (18 + ks) * 1024); }
  SB();
#pragma unroll
  for (int ks = 0; ks < 18; ++ks) {
    const int kn = ks + DEPTH - 1;
    if (kn < 18) { xf[kn % DEPTH][0] = *(LAS v4u*)(xb + kn * 1024); xf[kn % DEPTH][1] = *(LAS v4u*)(xb + (18 + kn) * 1024); }
    SB();
#pragma unroll
    for (int m = 0; m < 2; ++m) {
      acc[0][m] = mfma(wt[0][ks], xf[ks % DEPTH][m], acc[0][m]);
      acc[1][m] = mfma(wt[1][ks], xf[ks % DEPTH][m], acc[1][m]);
    }
    SB();
  }
#pragma unroll
  for (int m = 0; m < 2; ++m)
#pragma unroll
    for (int q = 0; q < 4; ++q) o[m][q] = pack2(acc[0][m][q], acc[1][m][q]);
}

struct XStage {
  rsrc_t rx; int go, lo; v4u st[9];
  DEVI void init(const Params& p, int tok, int q, int lane) {
    rx = mk_rsrc(p.x, 2048 * DM * 2);
    go = tok * (DM * 2) + (q & 1) * 9 * 64 + ximg_piece(lane);
    lo = q * 9 * 1024 + lane * 16;
  }
  DEVI void req(int s) {
#pragma unroll
    for (int k = 0; k < 9; ++k) st[k] = pl128(rx, go + k * 64, (s < NSEG ? s : NSEG - 1) * 32 * DM * 2);
  }
  DEVI void put(LAS unsigned char* L) {
#pragma unroll
    for (int k = 0; k < 9; ++k) *(LAS v4u*)(L + lo + k * 1024) = st[k];
  }
};

DEVI int seg_of(int hp, int i) { return i < HSEG / 2 ? 2 * i + hp : i + HSEG / 2; }

DEVI void role_X(const Params& p, rsrc_t r, LAS unsigned char* L, int h) {
  const int lane = threadIdx.x & 63, w = uni(threadIdx.x >> 6);
  const int rd = ximg_rd(lane), oo = KQ_OFF + h * 16384 + (w >> 2) * 8192 + (w & 3) * 1024 + lane * 16;
  constexpr int cnt = NSEG;
  v4u wt[2][18];
  if (w < 4) {
    XStage xs; xs.init(p, 2 * (lane >> 2) + (w >> 1), w, lane);
    xs.req(1);
    LOAD_W(0, p.wk, p.wq, get_w1_dense, 0, w)
    __builtin_amdgcn_s_setprio(1);
    bar();
    const int fo = (w == 0 && lane == 0) ? CTRL + (L_XP + h) * 128 : OOB;
    for (int it = sopaque(0); it < cnt; ++it) {
      const int s = it;
      v4u o[2];
      proj32<3>(L + (it & 1) * 36864 + rd, wt, o);
      st128(o[0], r, oo, s * NH * 16384);
      st128(o[1], r, oo + 4096, s * NH * 16384);
      SB();
      xs.put(L + ((it + 1) & 1) * 36864);
      SB();
      xs.req(it + 2);
      asm volatile("s_waitcnt vmcnt(9)" ::: "memory");
      bar();
      st32(it < XUN || it + 1 == cnt ? s : it - 1, r, fo, 0);
      if (it < 3) PS(6 + it);
    }
    __builtin_amdgcn_s_setprio(0);
  } else {
    {
      XStage xs; xs.init(p, 2 * (lane >> 2) + ((w & 3) >> 1), w & 3, lane);
      xs.req(0);
      LOAD_W(0, p.wk, p.wq, get_w1_dense, 1, w & 3)
      xs.put(L);
    }
    bar();
    for (int it = sopaque(0); it < cnt; ++it) {
      const int s = it;
      v4u o[2];
      proj32<4>(L + (it & 1) * 36864 + rd, wt, o);
      st128(o[0], r, oo, s * NH * 16384);
      st128(o[1], r, oo + 4096, s * NH * 16384);
      if (it < XUN || it + 1 == cnt) drain();
      asm volatile("s_waitcnt vmcnt(2)" ::: "memory");
      bar();
    }
  }
}

DEVI void role_Y(const Params& p, rsrc_t r, LAS unsigned char* L, int h, int hp) {
  const int lane = threadIdx.x & 63, w = uni(threadIdx.x >> 6);
  const int cnt = hp ? HSEG / 2 : NSEG - HSEG / 2;
  constexpr int VB = 73728;
  if (w < 4) {
    v4u wt[2][18];
    {
      const int row = lane >> 2;
      XStage xs; xs.init(p, 8 * (row >> 2) + (row & 3) + 4 * (w >> 1), w, lane);
      xs.req(seg_of(hp, 0));
      LOAD_W(1, p.wv, p.wo, get_w1_dense, 0, w)
      xs.put(L);
    }
    bar();
    const int rd = ximg_rd(lane);
    const int fo = (w == 0 && lane == 0) ? CTRL + (L_YP + hp * 32 + h) * 128 : OOB;
    for (int it = sopaque(0); it < cnt; ++it) {
      v4u o[2];
      proj32<4>(L + (it & 1) * 36864 + rd, wt, o);
      *(LAS v4u*)(L + VB + (it & 1) * 8192 + w * 1024 + lane * 16) = o[0];
      *(LAS v4u*)(L + VB + (it & 1) * 8192 + 4096 + w * 1024 + lane * 16) = o[1];
      bar();
      if (it > 0) st32(seg_of(hp, it - 1), r, fo, 0);
      if (it < 3) PS(6 + it);
    }
    bar();
    st32(seg_of(hp, cnt - 1), r, fo, 0);
  } else {
    const int c = w - 4, row = lane >> 2;
    XStage xs; xs.init(p, 8 * (row >> 2) + (row & 3) + 4 * (c >> 1), c, lane);
    xs.req(seg_of(hp, 1));
    v8u wt[9][2];
    LOAD_W(1, p.wv, p.wo, get_w2, 1, c)
    __builtin_amdgcn_s_setprio(1);
    bar();
    xs.put(L + 36864);
    xs.req(seg_of(hp, 2));
    bar();
    const int oo = BZ_OFF + h * 1024 + lane * 16;
    const int t0o = tile_of(c, 0) * 32768, t8o = tile_of(c, 8) * 32768;
    for (int it = sopaque(0); it < cnt; ++it) {
      const int s = seg_of(hp, it);
      v4u vf[2][4];
#pragma unroll
      for (int k = 0; k < 8; ++k) vf[k >> 2][k & 3] = *(LAS v4u*)(L + VB + (it & 1) * 8192 + k * 1024 + lane * 16);
#pragma unroll
      for (int i = 0; i < 9; ++i) {
        f32x4 a0 = {0, 0, 0, 0}, a1 = {0, 0, 0, 0};
#pragma unroll
        for (int k = 0; k < 2; ++k) {
          a0 = mfma(vf[0][2 * k], lo4(wt[i][k]), a0); a1 = mfma(vf[1][2 * k], lo4(wt[i][k]), a1);
          a0 = mfma(vf[0][2 * k + 1], hi4(wt[i][k]), a0); a1 = mfma(vf[1][2 * k + 1], hi4(wt[i][k]), a1);
        }
        st128(v4u{pack2(a0[0], a0[1]), pack2(a0[2], a0[3]), pack2(a1[0], a1[1]), pack2(a1[2], a1[3])}, r, oo,
              s * NTILE * 32768 + (i < 8 ? t0o + i * 32768 : t8o));
      }
      SB();
      xs.put(L + (it & 1) * 36864);
      SB();
      xs.req(seg_of(hp, it + 3));
      asm volatile("s_waitcnt vmcnt(9)" ::: "memory");
      bar();
    }
    __builtin_amdgcn_s_setprio(0);
  }
}

DEVI void role_T(const Params& p, rsrc_t r, int ti) {
  const int lane = threadIdx.x & 63, w = uni(threadIdx.x >> 6), n = lane & 15, g = lane >> 4;
  const rsrc_t r1 = mk_rsrc(p.w1, DM * FF * 2), r2 = mk_rsrc(p.w2, DM * FF * 2);
  { const u64 t0 = __builtin_amdgcn_s_memrealtime(); while (__builtin_amdgcn_s_memrealtime() - t0 < TDLY) __builtin_amdgcn_s_sleep(16); }
  {
    const rsrc_t rg = mk_rsrc(p.gw, DM * 2), rx = mk_rsrc(p.x, 2048 * DM * 2);
    const int o = lane < 36 ? lane * 32 : OOB, t0 = (ti * 8 + w) * 5;
    const v4u g0 = pl128(rg, o, 0), g1 = pl128(rg, o + 16, 0);
    float mine = 0.f;
#pragma unroll
    for (int t = 0; t < 5; ++t) {
      const int tok = t0 + t < 2048 ? t0 + t : 2047;
      const v4u x0 = pl128(rx, o, tok * DM * 2), x1 = pl128(rx, o + 16, tok * DM * 2);
      float a = 0.f;
#pragma unroll
      for (int q = 0; q < 4; ++q) {
        a += bf2f(x0[q] & 0xFFFFu) * bf2f(g0[q] & 0xFFFFu) + __uint_as_float(x0[q] & 0xFFFF0000u) * __uint_as_float(g0[q] & 0xFFFF0000u);
        a += bf2f(x1[q] & 0xFFFFu) * bf2f(g1[q] & 0xFFFFu) + __uint_as_float(x1[q] & 0xFFFF0000u) * __uint_as_float(g1[q] & 0xFFFF0000u);
      }
      a = x32_sum(x16_sum(row16_sum(a)));
      mine = lane == t ? a : mine;
    }
    const float gv = __builtin_amdgcn_rcpf(1.f + __builtin_amdgcn_exp2f(-1.4426950408889634f * mine));
    st32(__float_as_uint(gv), r, (lane < 5 && t0 + lane < 2048) ? G_OFF + (t0 + lane) * 4 : OOB, 0);
  }
  const int u0 = ti * 8 + w;
  v4u raw[2][8];
#pragma unroll
  for (int i = 0; i < 2; ++i) {
    const int u = u0 + i * 480 < 648 ? u0 + i * 480 : u0, c = u / 18, ks = u - 18 * c;
#pragma unroll
    for (int q = 0; q < 8; ++q) raw[i][q] = pl128(r1, (8 * g * FF + 8 * n) * 2, ((32 * ks + q) * FF + 128 * c) * 2);
  }
#pragma unroll
  for (int i = 0; i < 2; ++i) {
    const int u = u0 + i * 480 < 648 ? u0 + i * 480 : u0, c = u / 18, ks = u - 18 * c;
#pragma unroll
    for (int t = 0; t < 8; ++t) {
      v4u o;
#pragma unroll
      for (int q = 0; q < 4; ++q) o[q] = __builtin_amdgcn_perm(raw[i][2 * q + 1][t >> 1], raw[i][2 * q][t >> 1], (t & 1) ? 0x07060302u : 0x05040100u);
      st128(o, r, W1T_OFF + lane * 16, ((c * 4 + (t >> 1)) * 18 + ks) * 2048 + (t & 1) * 1024);
    }
  }
  u32 r9[2][8];
#pragma unroll
  for (int i = 0; i < 2; ++i) {
    const int u = u0 + i * 480 < 576 ? u0 + i * 480 : u0, k = u >> 2, c4 = u & 3;
    const int so = ((128 * (k >> 2) + 2 * (k & 3)) * DM + 144 * c4) * 2;
#pragma unroll
    for (int q = 0; q < 8; ++q) raw[i][q] = pl128(r2, (32 * g * DM + 8 * n) * 2, so + (8 * (q >> 1) + (q & 1)) * DM * 2);
#pragma unroll
    for (int q = 0; q < 8; ++q) r9[i][q] = __builtin_amdgcn_raw_buffer_load_b16(r2, (32 * g * DM + 128 + n) * 2, so + (8 * (q >> 1) + (q & 1)) * DM * 2, 0);
  }
#pragma unroll
  for (int i = 0; i < 2; ++i) {
    const int u = u0 + i * 480 < 576 ? u0 + i * 480 : u0;
#pragma unroll
    for (int t = 0; t < 9; ++t) {
      v4u o;
#pragma unroll
      for (int q = 0; q < 4; ++q)
        o[q] = t < 8 ? __builtin_amdgcn_perm(raw[i][2 * q + 1][(t & 7) >> 1], raw[i][2 * q][(t & 7) >> 1], (t & 1) ? 0x07060302u : 0x05040100u)
                     : __builtin_amdgcn_perm(r9[i][2 * q + 1], r9[i][2 * q], 0x05040100u);
      st128(o, r, W2T_OFF + lane * 16, u * 9216 + t * 1024);
    }
  }
  drain();
  bar();
  if (threadIdx.x == 0) st32(0u, r, CTRL + (L_TP + ti) * 128, 0);
}

DEVI float gelu_tanh(float x) {
  const float e = __builtin_amdgcn_exp2f(x * (-2.302208198f - 0.1029432397f * x * x));
  return x * __builtin_amdgcn_rcpf(1.f + e);
}

DEVI void role_F(const Params& p, rsrc_t r, LAS unsigned char* L, int tt, int sp, int c0, int c1) {
  const int lane = threadIdx.x & 63, w = uni(threadIdx.x >> 6), n = lane & 15, g = lane >> 4;
  constexpr int HB = 73728;
  const rsrc_t rx = mk_rsrc(p.x, 2048 * DM * 2);
  { int rounds = 0, seen = -1; wait_prog(r, L_TP, NTB, 0, lane, seen, rounds); }
  bar();
  {
    const int go = (tt * 64 + (w >> 1) * 16 + (lane >> 2)) * (DM * 2) + (w & 1) * 9 * 64 + ximg_piece(lane);
    v4u st[9];
#pragma unroll
    for (int k = 0; k < 9; ++k) st[k] = pl128(rx, go + k * 64, 0);
#pragma unroll
    for (int k = 0; k < 9; ++k) *(LAS v4u*)(L + (w * 9 + k) * 1024 + lane * 16) = st[k];
  }
  if (w < 4) {
    const rsrc_t rb = mk_rsrc(p.b1, FF * 2);
    const int vo = W1T_OFF + w * 18 * 2048 + lane * 16;
    constexpr int WIN = 6;
    v4u wf[WIN][2];
    auto req = [&](int slot, int c, int ks) {
      wf[slot][0] = ld128(r, vo, (c * 72 + ks) * 2048);
      wf[slot][1] = ld128(r, vo + 1024, (c * 72 + ks) * 2048);
    };
#pragma unroll
    for (int k = 0; k < WIN - 1; ++k) req(k, c0, k);
    bar();
    const int rd = ximg_rd(lane);
    for (int c = c0; c < c1; ++c) {
      const int cn = c + 1 < c1 ? c + 1 : c;
      v4u bias;
#pragma unroll
      for (int q = 0; q < 4; ++q) bias[q] = pl32(rb, (32 * g + 2 * w) * 2 + q * 16, c * 256);
      f32x4 acc[2][4];
#pragma unroll
      for (int i = 0; i < 8; ++i) acc[i >> 2][i & 3] = zero4();
      v4u xf[2][4];
#pragma unroll
      for (int m = 0; m < 4; ++m) xf[0][m] = *(LAS v4u*)(L + (m * 18) * 1024 + rd);
#pragma unroll
      for (int ks = 0; ks < 18; ++ks) {
        const int kn = ks + WIN - 1;
        req(kn % WIN, kn < 18 ? c : cn, kn % 18);
        if (ks + 1 < 18) {
#pragma unroll
          for (int m = 0; m < 4; ++m) xf[(ks + 1) & 1][m] = *(LAS v4u*)(L + (m * 18 + ks + 1) * 1024 + rd);
        }
        SB();
#pragma unroll
        for (int m = 0; m < 4; ++m) { acc[0][m] = mfma(wf[ks % WIN][0], xf[ks & 1][m], acc[0][m]); acc[1][m] = mfma(wf[ks % WIN][1], xf[ks & 1][m], acc[1][m]); }
        SB();
      }
#pragma unroll
      for (int m = 0; m < 4; ++m) {
        v4u o;
#pragma unroll
        for (int q = 0; q < 4; ++q)
          o[q] = pack2(gelu_tanh(acc[0][m][q] + bf2f(bias[q] & 0xFFFFu)), gelu_tanh(acc[1][m][q] + __uint_as_float(bias[q] & 0xFFFF0000u)));
        *(LAS v4u*)(L + HB + ((c - c0) & 1) * 16384 + (m * 4 + w) * 1024 + lane * 16) = o;
      }
      bar();
    }
  } else {
    const int c4 = w - 4;
    const int vo = W2T_OFF + c4 * 9216 + lane * 16;
    v4u bw[2][9];
#pragma unroll
    for (int t = 0; t < 9; ++t) bw[0][t] = ld128(r, vo + t * 1024, 4 * c0 * 36864);
    f32x4 acc[9][4];
#pragma unroll
    for (int i = 0; i < 36; ++i) acc[i >> 2][i & 3] = zero4();
    bar();
    bar();
    for (int c = c0; c < c1; ++c) {
#pragma unroll
      for (int ks = 0; ks < 4; ++ks) {
        const int kn = 4 * c + ks + 1 < 4 * c1 ? 4 * c + ks + 1 : 4 * c + ks;
        v4u hf[4];
#pragma unroll
        for (int m = 0; m < 4; ++m) hf[m] = *(LAS v4u*)(L + HB + ((c - c0) & 1) * 16384 + (m * 4 + ks) * 1024 + lane * 16);
#pragma unroll
        for (int t = 0; t < 9; ++t) {
          bw[(ks + 1) & 1][t] = ld128(r, vo + t * 1024, kn * 36864);
          SB();
#pragma unroll
          for (int m = 0; m < 4; ++m) acc[t][m] = mfma(hf[m], bw[ks & 1][t], acc[t][m]);
          SB();
        }
      }
      if (c + 1 < c1) bar();
    }
    const int xo = FP_OFF + (tt * 4 + c4) * 36864 + lane * 16;
    if (sp == 1) {
#pragma unroll
      for (int i = 0; i < 36; ++i) st128(__builtin_bit_cast(v4u, acc[i >> 2][i & 3]), r, xo, i * 1024);
    } else {
      int rounds = 0, seen = -1;
      wait_prog(r, L_FP + 32 + tt, 1, 0, lane, seen, rounds);
#pragma unroll
      for (int j = 0; j < 3; ++j) {
        v4u t[12];
#pragma unroll
        for (int i = 0; i < 12; ++i) t[i] = ld128(r, xo, (12 * j + i) * 1024);
#pragma unroll
        for (int i = 0; i < 12; ++i) { acc[3 * j + (i >> 2)][i & 3] += __builtin_bit_cast(f32x4, t[i]); asm volatile("" : "+v"(acc[3 * j + (i >> 2)][i & 3])); }
        SB();
      }
      const rsrc_t ro = mk_rsrc(p.out, 2048 * DM * 2), rb = mk_rsrc(p.b2, DM * 2);
      const v4u b8 = pl128(rb, (144 * c4 + 8 * n) * 2, 0);
      const float b9 = bf2f(__builtin_amdgcn_raw_buffer_load_b16(rb, (144 * c4 + 128 + n) * 2, 0, 0));
      if (tt < CUT) {
        const int sg = 2 * tt + (lane >= 9), want = lane < 18 ? sg : -1;
        const int vo2 = CTRL + (L_OP + (sg % 3) * 9 + lane % 9) * 128;
        POLL(const int v = (int)ld32(r, vo2, 0); bad = __builtin_amdgcn_ballot_w64(v < want) != 0; if (bad) __builtin_amdgcn_s_sleep(32));
      }
      const int o8 = ((tt * 64 + 4 * g) * DM + 144 * c4 + 8 * n) * 2, o9 = ((tt * 64 + 4 * g) * DM + 144 * c4 + 128 + n) * 2;
#pragma unroll
      for (int m = 0; m < 4; ++m) {
        const v4u gq = ld128(r, G_OFF + (tt * 64 + 16 * m + 4 * g) * 4, 0);
        v4u a8[4]; u32 a9[4];
#pragma unroll
        for (int q = 0; q < 4; ++q) {
          a8[q] = ld128(ro, tt < CUT ? o8 : OOB, (16 * m + q) * DM * 2);
          a9[q] = __builtin_amdgcn_raw_buffer_load_b16(ro, tt < CUT ? o9 : OOB, (16 * m + q) * DM * 2, SC1);
        }
#pragma unroll
        for (int q = 0; q < 4; ++q) {
          const float k = 1.f - __uint_as_float(gq[q]);
          v4u o;
#pragma unroll
          for (int t = 0; t < 4; ++t)
            o[t] = pack2(k * (acc[2 * t][m][q] + bf2f(b8[t] & 0xFFFFu)) + bf2f(a8[q][t] & 0xFFFFu),
                         k * (acc[2 * t + 1][m][q] + __uint_as_float(b8[t] & 0xFFFF0000u)) + __uint_as_float(a8[q][t] & 0xFFFF0000u));
          st128(o, ro, o8, (16 * m + q) * DM * 2);
          __builtin_amdgcn_raw_buffer_store_b16((u16)pack2(k * (acc[8][m][q] + b9) + bf2f(a9[q]), 0.f), ro, o9, (16 * m + q) * DM * 2, SC1);
        }
        SB();
      }
    }
  }
  drain();
  bar();
  if (threadIdx.x == 0) st32(0u, r, CTRL + (L_FP + sp * 32 + tt) * 128, 0);
}

DEVI void role_O(const Params& p, rsrc_t r, LAS unsigned char* L, int cg, int s0, int ds) {
  const int lane = threadIdx.x & 63, w = uni(threadIdx.x >> 6), n = lane & 15, g = lane >> 4;
  int rounds = 0, seenY0 = -1, seenY1 = -1, seenS = -1;
  const int fm = w & 1, ftp = (w >> 1) & 1, frh = w >> 2;
  const int col = 64 * cg + 32 * ftp + 2 * n;
  const rsrc_t ro = mk_rsrc(p.out, 2048 * DM * 2);
  { int seen = -1; wait_prog(r, L_TP, NTB, 0, lane, seen, rounds); }
  const int svo = CTRL + ((lane & 4 ? L_VP : L_SP) + 4 * w + (lane & 3)) * 128;
  for (int s = s0; s < NSEG; s += ds) {
    wait_prog2(r, L_YP + 4 * w, 4, s, lane, seenY0, seenY1, rounds);
    v4u bz[4][4];
#pragma unroll
    for (int t = 0; t < 4; ++t)
#pragma unroll
      for (int q = 0; q < 4; ++q) bz[t][q] = ld128(r, (4 * w + q) * 1024 + lane * 16, BZ_OFF + (s * NTILE + 4 * cg + t) * 32768);
    const int tt = s >> 1;
    if (tt >= CUT) { int seenF = -1; wait_prog(r, L_FP + tt, 1, 0, lane, seenF, rounds); }
    const int tok = s * 32 + 2 * (4 * g + 2 * frh) + fm;
    u32 fb[2];
    float gt[2];
#pragma unroll
    for (int q = 0; q < 2; ++q) {
      fb[q] = ld32(ro, tt >= CUT ? ((tok + 2 * q) * DM + col) * 2 : OOB, 0);
      gt[q] = __uint_as_float(ld32(r, G_OFF + (tok + 2 * q) * 4, 0));
    }
    if (__builtin_amdgcn_ballot_w64(seenS < s) != 0) {
      POLL(seenS = (int)ld32(r, svo, 0); bad = __builtin_amdgcn_ballot_w64(seenS < s) != 0; if (bad) __builtin_amdgcn_s_sleep(4));
    }
    v4u px[2][4], pm[2], cz[4];
#pragma unroll
    for (int m = 0; m < 2; ++m) {
#pragma unroll
      for (int q = 0; q < 4; ++q) px[m][q] = ld128(r, ((4 * w + q) * 2 + m) * 1280 + lane * 16, PX_OFF + s * NH * 2560);
      pm[m] = ld128(r, ((4 * w + g) * 2 + m) * 1280 + 1024 + n * 16, PX_OFF + s * NH * 2560);
    }
#pragma unroll
    for (int t = 0; t < 4; ++t) cz[t] = ld128(r, w * 1024 + lane * 16, CZ_OFF + (s * NTILE + 4 * cg + t) * 8192);
#pragma unroll
    for (int t = 0; t < 4; ++t)
#pragma unroll
      for (int m = 0; m < 2; ++m) {
        f32x4 a = {0, 0, 0, 0};
#pragma unroll
        for (int q = 0; q < 4; ++q) a = mfma(px[m][q], bz[t][q], a);
        a = mfma(pm[m], cz[t], a);
        *(LAS f32x4*)(L + w * 8192 + (t * 2 + m) * 1024 + lane * 16) = a;
      }
    bar();
    f32x2 e = {0, 0}, o = {0, 0};
#pragma unroll
    for (int q = 0; q < 8; ++q) {
      e += *(LAS f32x2*)(L + q * 8192 + (4 * ftp + fm) * 1024 + lane * 16 + frh * 8);
      o += *(LAS f32x2*)(L + q * 8192 + (4 * ftp + 2 + fm) * 1024 + lane * 16 + frh * 8);
    }
#pragma unroll
    for (int q = 0; q < 2; ++q) {
      st32(pack2(gt[q] * e[q] + bf2f(fb[q] & 0xFFFFu), gt[q] * o[q] + __uint_as_float(fb[q] & 0xFFFF0000u)), ro, ((tok + 2 * q) * DM + col) * 2, 0);
    }
    drain();
    bar();
    if (threadIdx.x == 0) st32(s, r, CTRL + (L_OP + s0 * 9 + cg) * 128, 0);
  }
}

DEVI int get_role(rsrc_t r, const Params& p, LAS unsigned char* L) {
  if (threadIdx.x == 0) {
    const int xcc = __builtin_amdgcn_s_getreg(20 | (3 << 11)) & 7;
    GAS u32* ctl = (GAS u32*)(p.ws + CTRL);
    int t = (int)__hip_atomic_fetch_add(ctl + (L_TICKET + xcc) * 32, 1u, __ATOMIC_RELAXED, __HIP_MEMORY_SCOPE_AGENT) + 1;
    int role = xcc * 32 + t;
    if (t >= 32) {
      int o = (int)__hip_atomic_fetch_add(ctl + L_OVF * 32, 1u, __ATOMIC_RELAXED, __HIP_MEMORY_SCOPE_AGENT) + 1;
      role = -1;
      for (int spins = 0; spins < SPIN_CAP && role < 0; ++spins) {
        int tot = 0, k = o, cand = -1;
        for (int x = 0; x < 8; ++x) {
          const int c = (int)__hip_atomic_load(ctl + (L_TICKET + x) * 32, __ATOMIC_RELAXED, __HIP_MEMORY_SCOPE_AGENT) + 1;
          tot += c;
          const int fr = c < 32 ? 32 - c : 0;
          if (cand < 0 && k < fr) cand = x * 32 + c + k;
          k -= fr;
        }
        if (tot == 256) role = cand;
        else __builtin_amdgcn_s_sleep(8);
      }
    }
    *(LAS int*)(L + LDS_BYTES - 16) = role;
  }
  __syncthreads();
  return uni(*(LAS int*)(L + LDS_BYTES - 16));
}

__global__ void __launch_bounds__(512, 2) chain_kernel(Params p) {
  LAS unsigned char* L = (LAS unsigned char*)smem_g;
  rsrc_t r = __builtin_amdgcn_make_buffer_rsrc(p.ws, 0, WS_USED, 0x00020000);
  const int role = get_role(r, p, L);
  const int x = (role >> 5) & 3, t = role & 31;
  const bool near = role >= 128;
  if (role < 0) return;
  if (near && t < 8) role_S(p, r, L, x * 8 + t);
  else if (near && t < 16) role_V(p, r, L, x * 8 + t - 8);
  else if (near && t < 25) role_R(p, r, L, x * 9 + t - 16);
  else if (near || t == 7) role_X(p, r, L, x * 8 + (near ? t - 25 : 7));
  else {
    if (t & 8) role_Y(p, r, L, x * 8 + (t & 7), t >> 4);
    else { role_T(p, r, t < 7 ? x * 7 + t : 28 + x * 8 + t - 16); }
    if (t >= 16) role_F(p, r, L, (x & 1) * 16 + t - 16, x >> 1, (x >> 1) * 18, (x >> 1) * 18 + 18);
    else if (t < 7 && x * 7 + t < 27) role_O(p, r, L, (x * 7 + t) % 9, (x * 7 + t) / 9, 3);
  }
}

extern "C" void kernel_launch(void* const* d_in, const int* in_sizes, int n_in, void* d_out, int out_size, void* d_ws, size_t ws_size, hipStream_t stream) {
  Params p;
  p.x = (const u16*)d_in[0]; p.mem0 = (const u16*)d_in[1]; p.wq = (const u16*)d_in[2]; p.wk = (const u16*)d_in[3]; p.wv = (const u16*)d_in[4]; p.wo = (const u16*)d_in[5];
  p.w1 = (const u16*)d_in[8]; p.b1 = (const u16*)d_in[9]; p.w2 = (const u16*)d_in[10]; p.b2 = (const u16*)d_in[11]; p.gw = (const u16*)d_in[12];
  p.out = (u16*)d_out;
  p.ws = (unsigned char*)(((uintptr_t)d_ws + RING_BYTES - 1) & ~(uintptr_t)(RING_BYTES - 1));
  hipFuncSetAttribute((const void*)chain_kernel, hipFuncAttributeMaxDynamicSharedMemorySize, LDS_BYTES);
  hipMemsetAsync(p.ws, 0xFF, INIT_BYTES, stream);
  chain_kernel<<<256, 512, LDS_BYTES, stream>>>(p);
}
```

```cpp
#include <hip/hip_runtime.h>
#include <hip/hip_bf16.h>
#include <stdint.h>
#include <string.h>
#pragma clang diagnostic ignored "-Winline-asm"

typedef unsigned u32;
typedef unsigned short u16;
typedef unsigned long long u64;
typedef u32 v2u __attribute__((ext_vector_type(2)));
typedef u32 v4u __attribute__((ext_vector_type(4)));
typedef u32 v8u __attribute__((ext_vector_type(8)));
typedef float f32x2 __attribute__((ext_vector_type(2)));
typedef float f32x4 __attribute__((ext_vector_type(4)));
typedef __bf16 bf16x2 __attribute__((ext_vector_type(2)));
typedef __bf16 bf16x8 __attribute__((ext_vector_type(8)));
typedef __bf16 bf16x16 __attribute__((ext_vector_type(16)));
typedef short s16x4 __attribute__((ext_vector_type(4)));
typedef short s16x8 __attribute__((ext_vector_type(8)));
typedef __amdgpu_buffer_rsrc_t rsrc_t;
#define LAS __attribute__((address_space(3)))
#define GAS __attribute__((address_space(1)))
#define DEVI __device__ __forceinline__

constexpr int DM = 576, NH = 32, NSEG = 64, FF = 4608, NTILE = 36;
constexpr u32 SENT = 0xFFFFFFFFu;
constexpr int SC1 = 16;
constexpr int CHUNK = 16384;
constexpr int SLOT = 42 * CHUNK;
constexpr int P_OFF = 36 * CHUNK, MEM_OFF = 40 * CHUNK;
constexpr int T_HS = 41 * CHUNK + 1024, T_HV = T_HS + SLOT;
constexpr int RING_BYTES = 2 << 20;
constexpr int CTRL = 3 * SLOT;
constexpr int L_EPOCH = 0, L_ABORT = 1, L_PAR = 2, L_RP = 80, L_CLAIM = 416, L_SP = 16, L_VP = 48, L_XP = 128, L_YP = 192, L_FP = 256, L_OP = 320, L_TP = 352, NTB = 60;
#define HSEG 8
#define CUT 20
constexpr int KQ_OFF = RING_BYTES + 65536;
constexpr int BZ_OFF = KQ_OFF + NSEG * NH * 16384;
constexpr int CZ_OFF = BZ_OFF + NSEG * NTILE * NH * 1024;
constexpr int PX_OFF = CZ_OFF + NSEG * NTILE * NH * 256;
constexpr int FP_OFF = PX_OFF + NSEG * NH * 2560;
constexpr int W1T_OFF = FP_OFF + 2048 * DM * 4;
constexpr int W2T_OFF = W1T_OFF + DM * FF * 2;
constexpr int G_OFF = W2T_OFF + DM * FF * 2;
constexpr int JUNK = G_OFF + 8192;
constexpr int DBG_OFF = JUNK + SLOT + CHUNK;
constexpr int WS_USED = DBG_OFF + (4 << 20);
constexpr int SPIN_CAP = 1 << 18;
constexpr int LDS_BYTES = 148 * 1024;

struct Params {
  const u16 *x, *mem0, *wq, *wk, *wv, *wo, *w1, *b1, *w2, *b2, *gw;
  u16* out;
  unsigned char* ws;
};

struct WS {
  rsrc_t rs; u32 klo, khi;
  DEVI operator rsrc_t() const { return rs; }
};
DEVI float bf2f(u32 b) { return __uint_as_float(b << 16); }
DEVI u32 pack2(float lo, float hi) { f32x2 v = {lo, hi}; bf16x2 b = __builtin_convertvector(v, bf16x2); return __builtin_bit_cast(u32, b); }
DEVI u32 guard(u32 v) { return v < 0xFFFFFFFEu ? v : 0xFFFFFFFEu; }
DEVI void bar() { asm volatile("s_waitcnt lgkmcnt(0)\n\ts_barrier" ::: "memory"); }
DEVI void drain() { asm volatile("s_waitcnt vmcnt(0)" ::: "memory"); }
DEVI f32x4 smf64(v4u a, v8u b, f32x4 c, int idx) { return __builtin_amdgcn_smfmac_f32_16x16x64_bf16(__builtin_bit_cast(bf16x8, a), __builtin_bit_cast(bf16x16, b), c, idx, 0, 0); }
DEVI f32x4 smf32(v2u a, v4u b, f32x4 c, int idx) { return __builtin_amdgcn_smfmac_f32_16x16x32_bf16(__builtin_bit_cast(s16x4, a), __builtin_bit_cast(s16x8, b), c, idx, 0, 0); }
DEVI f32x4 mfma(v4u a, v4u b, f32x4 c) { return __builtin_amdgcn_mfma_f32_16x16x32_bf16(__builtin_bit_cast(bf16x8, a), __builtin_bit_cast(bf16x8, b), c, 0, 0, 0); }
DEVI v8u cat(v4u a, v4u b) { return __builtin_shufflevector(a, b, 0, 1, 2, 3, 4, 5, 6, 7); }
DEVI v4u lo4(v8u a) { return __builtin_shufflevector(a, a, 0, 1, 2, 3); }
DEVI v4u hi4(v8u a) { return __builtin_shufflevector(a, a, 4, 5, 6, 7); }
DEVI float swapadd(float a, float b) {
  auto r = __builtin_amdgcn_permlane32_swap(__float_as_uint(a), __float_as_uint(b), false, false);
  return __uint_as_float(r[0]) + __uint_as_float(r[1]);
}
template <int CTRL_> DEVI float dpp(float v) { return __uint_as_float(__builtin_amdgcn_update_dpp(0, __float_as_uint(v), CTRL_, 0xF, 0xF, true)); }
DEVI float row16_max(float v) { v = fmaxf(v, dpp<0xB1>(v)); v = fmaxf(v, dpp<0x4E>(v)); v = fmaxf(v, dpp<0x141>(v)); v = fmaxf(v, dpp<0x140>(v)); return v; }
DEVI float row16_sum(float v) { v += dpp<0xB1>(v); v += dpp<0x4E>(v); v += dpp<0x141>(v); v += dpp<0x140>(v); return v; }
DEVI float x16_max(float v) { auto r = __builtin_amdgcn_permlane16_swap(__float_as_uint(v), __float_as_uint(v), false, false); return fmaxf(__uint_as_float(r[0]), __uint_as_float(r[1])); }
DEVI float x32_max(float v) { auto r = __builtin_amdgcn_permlane32_swap(__float_as_uint(v), __float_as_uint(v), false, false); return fmaxf(__uint_as_float(r[0]), __uint_as_float(r[1])); }
DEVI float x16_sum(float v) { auto r = __builtin_amdgcn_permlane16_swap(__float_as_uint(v), __float_as_uint(v), false, false); return __uint_as_float(r[0]) + __uint_as_float(r[1]); }
DEVI float x32_sum(float v) { auto r = __builtin_amdgcn_permlane32_swap(__float_as_uint(v), __float_as_uint(v), false, false); return __uint_as_float(r[0]) + __uint_as_float(r[1]); }
DEVI v4u ld128(rsrc_t r, int vo, int so) { return __builtin_amdgcn_raw_buffer_load_b128(r, vo, so, SC1); }
DEVI v2u ld64(rsrc_t r, int vo, int so) { return __builtin_amdgcn_raw_buffer_load_b64(r, vo, so, SC1); }
DEVI u32 ld32(rsrc_t r, int vo, int so) { return __builtin_amdgcn_raw_buffer_load_b32(r, vo, so, SC1); }
DEVI void st128(v4u v, rsrc_t r, int vo, int so) { __builtin_amdgcn_raw_buffer_store_b128(v, r, vo + so, 0, SC1); }
DEVI void st64(v2u v, rsrc_t r, int vo, int so) { __builtin_amdgcn_raw_buffer_store_b64(v, r, vo, so, SC1); }
DEVI void st32(u32 v, rsrc_t r, int vo, int so) { __builtin_amdgcn_raw_buffer_store_b32(v, r, vo, so, SC1); }
DEVI rsrc_t mk_rsrc(const void* ptr, int bytes) { return __builtin_amdgcn_make_buffer_rsrc((void*)ptr, 0, bytes, 0x00020000); }
DEVI v4u pl128(rsrc_t r, int vo, int so) { return __builtin_amdgcn_raw_buffer_load_b128(r, vo, so, 0); }
DEVI u32 pl32(rsrc_t r, int vo, int so) { return __builtin_amdgcn_raw_buffer_load_b32(r, vo, so, 0); }
DEVI u32 umax(u32 a, u32 b) { return a > b ? a : b; }
DEVI u32 mx4(v4u v) { return umax(umax(v.x, v.y), umax(v.z, v.w)); }
DEVI int uni(int v) { return __builtin_amdgcn_readfirstlane(v); }
DEVI int opaque(int v) { asm volatile("" : "+v"(v)); return v; }
DEVI int sopaque(int v) { asm volatile("" : "+s"(v)); return v; }
DEVI void fence_c() { asm volatile("" ::: "memory"); }

#define XSLP 4
#define PSLP 23
#define RSLP 26
#define XUN 4
#define TDLY 700
#define MAR_C 8
#define VBG 6
#define VPRIO 1
#define FWIN 9
#define FPRIO 1
#define FPLAIN 0
#define VSLP 8
#define PLACE 2
#define WAUX 0
#define TAUX 2
#define INTL 0
#define NEARH 1
#define MAR_R 8
#define NOUNROLL _Pragma("clang loop unroll(disable)")
#define SB() __builtin_amdgcn_sched_barrier(0)
constexpr int OOB = 0x7FFFFFF0;
DEVI bool any_sent(u32 m) { return __builtin_amdgcn_ballot_w64(m == SENT) != 0; }
DEVI void set_flag(const WS& r, int vo, int v) { st64(v2u{r.klo + (u32)(v + 2), r.khi}, r, vo, 0); }
DEVI int get_flag(const WS& r, int vo) {
  const v2u v = ld64(r, vo, 0);
  return (v.y == r.khi && v.x - r.klo < 256u) ? (int)(v.x - r.klo) - 2 : -2;
}
DEVI bool give_up(const WS& r, int& rounds) {
  if (++rounds > SPIN_CAP / 256) { set_flag(r, CTRL + L_ABORT * 128, 0); return true; }
  if (uni(get_flag(r, CTRL + L_ABORT * 128)) >= 0) { rounds = SPIN_CAP; return true; }
  return false;
}
#define POLL(BODY)                                                                                   \
  NOUNROLL for (int cnt_ = 1;; ++cnt_) {                                                             \
    bool bad;                                                                                        \
    fence_c();                                                                                       \
    BODY;                                                                                            \
    if (__builtin_expect(!bad, 1)) break;                                                            \
    if (((cnt_ & 255) == 0 || rounds >= SPIN_CAP / 256) && give_up(r, rounds)) break;                \
  }                                                                                                  \
  fence_c();
DEVI u32 rt7() {
  u64 t;
  asm volatile("s_memrealtime %0\n\ts_waitcnt lgkmcnt(0)" : "=s"(t)::"memory");
  return (u32)t << 7;
}
#define HC1 "v_and_b32 %0, 127, v248\n\tv_cmp_ne_u32 vcc, %5, %0\n\ts_cbranch_vccz 2f\n\t"
#define HC4 HC1 HC1 HC1 HC1
#define HCAD HC4 HC4 HC4 HC4
DEVI u32 hint_wait(rsrc_t r, int vo, int so, int step) {
  int cnt = step < 0 ? 0 : 1024;
  u32 hv;
  asm volatile(
      "v_mov_b32 v248, -1\n\ts_nop 3\n\t"
      "1:\n\t"
      "buffer_load_dword v248, %2, %3, %4 offen sc1\n\t"
      HCAD
      "s_sub_u32 %1, %1, 1\n\t"
      "s_cbranch_scc0 1b\n\t"
      "2:\n\t"
      "v_mov_b32 %0, v248"
      : "=&v"(hv), "+s"(cnt)
      : "v"(vo), "s"(r), "s"(so), "s"(step)
      : "memory", "vcc", "scc", "v248");
  return hv;
}
template <int HI>
DEVI void time_wait(u32 tv) {
  int cnt = 64;
  u32 t;
  u64 now;
  asm volatile(
      "1:\n\t"
      "s_memrealtime s[96:97]\n\t"
      "s_sub_u32 %1, %1, 1\n\t"
      "s_cbranch_scc1 2f\n\t"
      "s_waitcnt lgkmcnt(0)\n\t"
      "s_lshl_b32 s96, s96, 7\n\t"
      "v_sub_u32 %0, s96, %3\n\t"
      "v_cmp_gt_i32 s[96:97], 0, %0\n\t"
      "s_cmp_lg_u32 s%c4, 0\n\t"
      "s_cbranch_scc1 1b\n\t"
      "2:\n\t"
      "s_waitcnt lgkmcnt(0)"
      : "=&v"(t), "+s"(cnt), "=&{s[96:97]}"(now)
      : "v"(tv), "i"(96 + HI)
      : "memory", "scc");
}
DEVI int row_max_i(int d) {
  d = max(d, __builtin_amdgcn_update_dpp(d, d, 0xB1, 0xF, 0xF, false)); d = max(d, __builtin_amdgcn_update_dpp(d, d, 0x4E, 0xF, 0xF, false));
  d = max(d, __builtin_amdgcn_update_dpp(d, d, 0x141, 0xF, 0xF, false));
  return d;
}
DEVI int track(int dur, int m) { return (m > 0 && m < (200 << 7)) ? dur + ((m - dur) >> 2) : dur; }
#define DCHK "v_max3_u32 %0, v248, v249, v250\n\tv_max3_u32 %1, v252, v253, v254\n\tv_max3_u32 %0, %0, v251, v255\n\tv_max_u32 %0, %0, %1\n\tv_cmp_eq_u32 vcc, -1, %0\n\t"
#define DC1 DCHK "s_cbranch_vccz 2f\n\t"
#define DC2 DC1 DC1
#define DCAD DC2 DC2 DC2
template <int TWO>
DEVI bool data_wait(rsrc_t r, int vo, int so, int la) {
  int cnt = 4096;
  u32 t0, t1;
  asm volatile(
      "s_waitcnt vmcnt(0)\n\t"
      "v_mov_b32 v248, -1\n\tv_mov_b32 v249, -1\n\tv_mov_b32 v250, -1\n\tv_mov_b32 v251, -1\n\t"
      "v_mov_b32 v252, %6\n\tv_mov_b32 v253, %6\n\tv_mov_b32 v254, %6\n\tv_mov_b32 v255, %6\n\t"
      "1:\n\t"
      "buffer_load_dwordx4 v[248:251], %3, %4, %5 offen sc1\n\t"
      ".if %6\n\t"
      "buffer_load_dwordx4 v[252:255], %3, %4, %5 offen offset:1024 sc1\n\t"
      ".endif\n\t"
      "s_sub_u32 %2, %2, 1\n\t"
      "s_cbranch_scc1 2f\n\t"
      DCAD
      DCHK "s_cbranch_vccnz 1b\n\t"
      "2:\n\t"
      "ds_write_b128 %7, v[248:251]\n\t"
      ".if %6\n\t"
      "ds_write_b128 %7, v[252:255] offset:1024\n\t"
      ".endif"
      : "=&v"(t0), "=&v"(t1), "+s"(cnt)
      : "v"(vo), "s"(r), "s"(so), "i"(-TWO), "v"(la)
      : "memory", "vcc", "scc", "v248", "v249", "v250", "v251", "v252", "v253", "v254", "v255");
  return cnt >= 0;
}
DEVI void wait_prog(const WS& r, int line0, int n, int want, int lane, int& seen, int& rounds) {
  if (__builtin_amdgcn_ballot_w64(seen < want) != 0) {
    const int vo = CTRL + (line0 + (lane < n ? lane : 0)) * 128;
    POLL(seen = get_flag(r, vo); bad = __builtin_amdgcn_ballot_w64(seen < want) != 0; if (bad) __builtin_amdgcn_s_sleep(2));
  }
}

DEVI void wait_prog2(const WS& r, int line0, int n, int want, int lane, int& seen0, int& seen1, int& rounds) {
  const bool hp = (want & 1) && want < HSEG;
  int sn = hp ? seen1 : seen0;
  wait_prog(r, line0 + (hp ? 32 : 0), n, want, lane, sn, rounds);
  if (hp) seen1 = sn; else seen0 = sn;
}
extern __shared__ __attribute__((aligned(16))) unsigned char smem_g[];

template <int WO>
struct WLoad {
  rsrc_t r1, r2; int va, vb;
  DEVI void init(const u16* W1, const u16* W2, int h) {
    r1 = mk_rsrc(W1, DM * 4096 * 2); r2 = mk_rsrc(W2, DM * 4096 * 2);
    const int tid = threadIdx.x;
    va = ((tid >> 4) * 4096 + h * 128) * 2 + (tid & 15) * 16; vb = h * 128 * DM * 2 + tid * 16;
  }
  template <int M, int RD> DEVI void req(v4u (&st)[9]) {
#pragma unroll
    for (int k = 0; k < 9; ++k) st[k] = __builtin_amdgcn_raw_buffer_load_b128(M ? r2 : r1, (M && WO) ? vb : va, (9 * RD + k) * ((M && WO) ? 8192 : 32 * 8192), WAUX);
    SB();
  }
  template <int M> DEVI void put(LAS unsigned char* L, const v4u (&st)[9]) {
    const int tid = threadIdx.x;
#pragma unroll
    for (int k = 0; k < 9; ++k) {
      const int sw = (M && WO) ? (((k * 512 + tid) / 72) >> 3) & 1 : (tid >> 7) & 1;
      *(LAS v4u*)(L + M * 73728 + k * 8192 + ((tid * 16) ^ (sw << 6))) = st[k];
    }
    SB();
  }
};
DEVI u32 lo2(u32 a, u32 b) { return __builtin_amdgcn_perm(b, a, 0x05040100u); }
DEVI u32 hi2(u32 a, u32 b) { return __builtin_amdgcn_perm(b, a, 0x07060302u); }
template <int RD>
DEVI void get_w1_sparse(LAS unsigned char* M, int wq, int lane, v8u (&wt)[2][9]) {
  const int n = lane & 15, gb = lane >> 4;
  LAS unsigned char* b = M + ((gb >> 1) + 8 * (gb & 1)) * 256 + ((wq * 64) ^ ((gb & 1) << 6)) + n * 4;
#pragma unroll
  for (int i = 0; i < 36; ++i) {
    const int hs = 9 * RD + (i >> 2), d = 32 * (i >> 2) + 16 * (i & 1) + 4 * ((i >> 1) & 1);
    const u32 x = *(LAS u32*)(b + d * 256), y = *(LAS u32*)(b + (d + 2) * 256);
    wt[0][hs >> 1][4 * (hs & 1) + (i & 3)] = lo2(x, y);
    wt[1][hs >> 1][4 * (hs & 1) + (i & 3)] = hi2(x, y);
  }
}
template <int RD>
DEVI void get_w1_dense(LAS unsigned char* M, int wq, int lane, v4u (&wt)[2][18]) {
  const int n = lane & 15, gb = lane >> 4;
  LAS unsigned char* b = M + 8 * gb * 256 + ((wq * 64) ^ ((gb & 1) << 6)) + n * 4;
#pragma unroll
  for (int i = 0; i < 36; ++i) {
    const int d = 32 * (i >> 2) + 2 * (i & 3);
    const u32 x = *(LAS u32*)(b + d * 256), y = *(LAS u32*)(b + (d + 1) * 256);
    wt[0][9 * RD + (i >> 2)][i & 3] = lo2(x, y);
    wt[1][9 * RD + (i >> 2)][i & 3] = hi2(x, y);
  }
}
DEVI int tile_of(int c, int i) { return i < 8 ? 9 * c + (c & 1) + i : (c & 1 ? 9 * c : 9 * c + 8); }
template <int RD>
DEVI void get_w2(LAS unsigned char* M, int c, int lane, v8u (&wt)[9][2]) {
  const int n = lane & 15, gb = lane >> 4;
  const u32 sel = (c & 1) ? 0x07060302u : 0x05040100u;
  LAS unsigned char* b = M + 8 * gb * 1152 + n * 4;
#pragma unroll
  for (int pr = 0; pr < 5; ++pr) {
    LAS unsigned char* bp = b + ((64 * (tile_of(c, 2 * pr) >> 1)) ^ ((gb & 1) << 6));
#pragma unroll
    for (int q = 0; q < 8; ++q) {
      const int e = 32 * (q >> 2) + 2 * (q & 3);
      const u32 x = *(LAS u32*)(bp + e * 1152), y = *(LAS u32*)(bp + (e + 1) * 1152);
      if (pr < 4) { wt[2 * pr][RD][q] = lo2(x, y); wt[2 * pr + 1][RD][q] = hi2(x, y); }
      else wt[8][RD][q] = __builtin_amdgcn_perm(y, x, sel);
    }
  }
}
#define LOAD_W(WO, W1, W2, GET, MINE, IDX, LAST)                                                       \
  {                                                                                              \
    WLoad<WO> wl; wl.init(W1, W2, h);                                                            \
    v4u sa[9], sb_[9], sc[9];                                                                    \
    wl.req<0, 0>(sa); wl.req<1, 0>(sb_);                                                         \
    wl.put<0>(L, sa); wl.req<0, 1>(sc); bar();                                                   \
    wl.put<1>(L, sb_); wl.req<1, 1>(sa);                                                         \
    if (MINE == 0) GET<0>(L, IDX, lane, wt);                                                     \
    bar();                                                                                       \
    wl.put<0>(L, sc);                                                                            \
    if (MINE == 1) GET<0>(L + 73728, IDX, lane, wt);                                             \
    bar();                                                                                       \
    wl.put<1>(L, sa); drain();                                                                   \
    if (MINE == 0) GET<1>(L, IDX, lane, wt);                                                     \
    bar();                                                                                       \
    if (MINE == 1) GET<1>(L + 73728, IDX, lane, wt);                                             \
    LAST;                                                                                        \
    bar();                                                                                       \
  }

constexpr int AIMG = 1088, QIMG = 2 * AIMG, KIMG = 2304, IMG = QIMG + KIMG;
constexpr int LM_IMG = 0, LM_MEM = 5120, LM_RED = 0, LM_T = 14336, LM_KQ = 16384, LM_BZ = 4096;

DEVI int wait_parity(const WS& r, int& rounds) {
  int v;
  POLL(v = uni(get_flag(r, CTRL + L_PAR * 128)); bad = v < 0; if (bad) __builtin_amdgcn_s_sleep(4));
  return (v & 1) * 8192;
}
DEVI int wait_mem0(const WS& r, int lane, int& rounds) {
  int seen = -2;
  wait_prog(r, L_RP, NTILE, 0, lane, seen, rounds);
  return (uni(seen) & 1) * 8192;
}
DEVI int next_slot(int sb, int par) { return sb + SLOT >= par + 3 * SLOT ? par : sb + SLOT; }

DEVI f32x4 zero4() { f32x4 z = {0, 0, 0, 0}; asm volatile("" : "+v"(z)); return z; }
constexpr float SCL = 0.08838834764831845f * 1.4426950408889634f;
DEVI float mulf(float a, float b) { float o = a * b; asm("" : "+v"(o)); return o; }
DEVI f32x2 fold(f32x4 a) { float x = a[0] + a[2]; asm("" : "+v"(x)); float y = a[1] + a[3]; asm("" : "+v"(y)); return f32x2{x, y}; }
template <int TWO>
struct Gather {
  DEVI void run(const WS& r, LAS unsigned char* L, int l16, int c0, int sb, int& rounds) {
    const int so = sb + MEM_OFF + (TWO ? c0 * 1024 : CHUNK), la = (int)(size_t)(L + LM_MEM + c0 * 1024 + l16);
    if (__builtin_expect(!data_wait<TWO>(r, l16, so, la), 0)) {
      NOUNROLL for (;;) {
        rounds += 15;
        if (give_up(r, rounds) || data_wait<TWO>(r, l16, so, la)) break;
      }
    }
  }
};
template <int RSTEP, int SCALE, class F>
DEVI void stage1(LAS unsigned char* L, const v8u (&wt)[2][9], f32x4 a0, f32x4 a1, int l16, int idx, LAS unsigned char* wp, F mid) {
  v4u af[9];
#pragma unroll
  for (int ks = 0; ks < 9; ++ks) af[ks] = *(LAS v4u*)(L + LM_MEM + ks * 1024 + l16);
  SB();
#pragma unroll
  for (int ks = 0; ks < 9; ++ks) {
    a0 = smf64(af[ks], wt[0][ks], a0, idx); a1 = smf64(af[ks], wt[1][ks], a1, idx);
    if (ks == 5) { SB(); mid(); SB(); }
  }
  f32x2 p0 = fold(a0), p1 = fold(a1);
  if (SCALE) { p0.x = mulf(p0.x, SCL); p0.y = mulf(p0.y, SCL); p1.x = mulf(p1.x, SCL); p1.y = mulf(p1.y, SCL); }
  *(LAS u32*)wp = pack2(p0.x, p1.x);
  *(LAS u32*)(wp + RSTEP) = pack2(p0.y, p1.y);
}
DEVI int aimg_w(int wq, int lane) { const int n = lane & 15, g = lane >> 4; return (wq >> 1) * AIMG + (2 * (wq & 1) + (n >> 3)) * 272 + g * 64 + (n & 1) * 32 + ((n & 7) >> 1) * 4; }
DEVI int aimg_r(int lane) { return (lane >> 4) * 272 + (lane & 15) * 16; }
DEVI int kimg_w(int wk, int lane) { const int n = lane & 15, g = lane >> 4; return (wk >> 1) * 1152 + (n >> 2) * 288 + g * 64 + (wk & 1) * 16 + (n & 3) * 4; }
DEVI int kimg_r(int k, int g, int key) { return (k >> 1) * 1152 + g * 288 + key * 32 + (k & 1) * 16; }
DEVI float row16_max_asm(float v) {
  asm volatile("s_nop 1\n\tv_max_f32_dpp %0, %0, %0 quad_perm:[1,0,3,2] row_mask:0xf bank_mask:0xf\n\t"
               "s_nop 1\n\tv_max_f32_dpp %0, %0, %0 quad_perm:[2,3,0,1] row_mask:0xf bank_mask:0xf\n\t"
               "s_nop 1\n\tv_max_f32_dpp %0, %0, %0 row_half_mirror row_mask:0xf bank_mask:0xf\n\t"
               "s_nop 1\n\tv_max_f32_dpp %0, %0, %0 row_mirror row_mask:0xf bank_mask:0xf"
               : "+v"(v));
  return v;
}

DEVI void role_S(const Params& p, const WS& r, LAS unsigned char* L, int h) {
  const int lane = threadIdx.x & 63, w = uni(threadIdx.x >> 6), n = lane & 15, g = lane >> 4;
  const int idx = (lane & 2) ? 0xEEEE : 0x4444, l16 = opaque(lane * 16);
  int rounds = 0;
  if (w < 6) st128(v4u{SENT, SENT, SENT, SENT}, r, P_OFF + (h >> 3) * CHUNK + (h & 7) * 1024 + l16, (w & 1) * 8192 + (w >> 1) * SLOT);
  else st32(SENT, r, lane == 0 ? T_HS + h * 128 : OOB, (w & 1) * 8192);
  v8u wt[2][9];
  if (w < 4) LOAD_W(0, p.wq, p.wk, get_w1_sparse, 0, w, )
  else LOAD_W(0, p.wq, p.wk, get_w1_sparse, 1, w - 4, )
  if (w == 6 && lane == 0) set_flag(r, CTRL + (L_SP + h) * 128, -1);
  const int par = wait_mem0(r, lane, rounds);
  int sb = par;
  if (w < 2) {
    LAS unsigned char* wa = L + LM_IMG + aimg_w(w, lane);
    const int pvo = P_OFF + (h >> 3) * CHUNK + (h & 7) * 1024 + w * 512;
    const int pxo = pvo + (n >> 3) * 128 + (g >> 1) * 64 + ((n >> 2) & 1) * 32 + (g & 1) * 16 + (n & 3) * 4;
    const int pmo = n < 4 ? pvo + 256 + (g >> 1) * 32 + (n >> 1) * 16 + (g & 1) * 8 + (n & 1) * 4 : OOB;
    const int cn = (n & 8) | ((n & 3) << 1) | ((n >> 2) & 1);
    const int kmc = ((n & 1) << 2) | (n & 2) | ((n >> 2) & 1);
    const float mk = n < 8 ? 1.f : 0.f;
    int sprev = par + 2 * SLOT;
    for (int s = sopaque(-1); s < NSEG; ++s) {
      const f32x4 z0 = zero4(), z1 = zero4();
      f32x4 sa = zero4(), sbb = zero4(), sm = zero4();
      LAS unsigned char* kq = L + LM_KQ + (s & 1) * 16384;
      v4u kf[2][4];
      const int so = s < 0 ? JUNK : sb;
      Gather<1> ga;
      ga.run(r, L, l16, 2 * w, sb, rounds);
      bar();
      stage1<16, 1>(L, wt, z0, z1, l16, idx, wa, [&]() {
#pragma unroll
        for (int k = 0; k < 8; ++k) kf[k >> 2][k & 3] = *(LAS v4u*)(kq + k * 1024 + (g * 16 + cn) * 16);
      });
      bar();
      v4u qa[2], km[4];
#pragma unroll
      for (int k = 0; k < 2; ++k) qa[k] = *(LAS v4u*)(L + LM_IMG + k * AIMG + aimg_r(lane ^ w));
#pragma unroll
      for (int k = 0; k < 4; ++k) km[k] = *(LAS v4u*)(L + LM_IMG + QIMG + kimg_r(k, g, kmc));
      SB();
      sa = smf64(qa[0], cat(kf[0][0], kf[0][1]), sa, idx);
      sbb = smf64(qa[0], cat(kf[1][0], kf[1][1]), sbb, idx);
      sa = smf64(qa[1], cat(kf[0][2], kf[0][3]), sa, idx);
      sm = smf64(qa[0], cat(km[0], km[1]), sm, idx);
      sbb = smf64(qa[1], cat(kf[1][2], kf[1][3]), sbb, idx);
      sm = smf64(qa[1], cat(km[2], km[3]), sm, idx);
      SB();
      const float va = sa[0] + sa[2], vb = sbb[0] + sbb[2], vm = sm[0] + sm[2];
      const float mx = row16_max_asm(fmaxf(fmaxf(va, vb), vm));
      const float ea = __builtin_amdgcn_exp2f(va - mx), eb = __builtin_amdgcn_exp2f(vb - mx), em = __builtin_amdgcn_exp2f(vm - mx);
      const float em2 = dpp<0x104>(em);
      const float inv = __builtin_amdgcn_rcpf(row16_sum(__builtin_fmaf(em, mk, ea + eb)));
      st32(guard(pack2(ea * inv, eb * inv)), r, pxo, so);
      st32(guard(pack2(em * inv, em2 * inv)), r, pmo, so);
      SB();
      *(LAS u32*)(L + LM_T + w * 4) = rt7();

      st32(SENT, r, pxo, sprev); st32(SENT, r, pmo, sprev);
      if (s >= 0) __builtin_amdgcn_s_sleep(PSLP - 8);
      sprev = s < 0 ? sprev : sb;
      sb = s < 0 ? sb : next_slot(sb, par);
    }
  } else if (w < 4) {
    LAS unsigned char* wa = L + LM_IMG + aimg_w(w, lane);
    const int m = w - 2, qi = 2 * n + m;
    const int po = (h * 2 + m) * 1280;
    for (int s = sopaque(-1); s < NSEG; ++s) {
      const f32x4 z0 = zero4(), z1 = zero4();
      bar();
      stage1<16, 1>(L, wt, z0, z1, l16, idx, wa, []() {});
      drain();
      bar();
      if (s < NSEG - 1) __builtin_amdgcn_s_sleep(XSLP);
      LAS unsigned char* kq = L + LM_KQ + (s & 1) * 16384 + l16;
      f32x4 xa = {0, 0, 0, 0}, xb = {0, 0, 0, 0}, xm = {0, 0, 0, 0};
#pragma unroll
      for (int k = 0; k < 4; ++k) {
        const v4u qf = *(LAS v4u*)(kq + 8192 + (m * 4 + k) * 1024);
        xa = mfma(*(LAS v4u*)(kq + k * 1024), qf, xa);
        xb = mfma(*(LAS v4u*)(kq + (4 + k) * 1024), qf, xb);
        xm = mfma(*(LAS v4u*)(L + LM_IMG + QIMG + kimg_r(k, g, n & 7)), qf, xm);
      }
      float mx = fmaxf(fmaxf(xm[0], xm[1]), fmaxf(xm[2], xm[3]));
#pragma unroll
      for (int k = 0; k < 4; ++k) {
        xa[k] = (8 * g + 2 * k <= qi) ? xa[k] : -INFINITY;
        xb[k] = (8 * g + 2 * k + 1 <= qi) ? xb[k] : -INFINITY;
        mx = fmaxf(mx, fmaxf(xa[k], xb[k]));
      }
      mx = x32_max(x16_max(mx));
      float sum = 0.f, summ = 0.f;
#pragma unroll
      for (int k = 0; k < 4; ++k) {
        xa[k] = __builtin_amdgcn_exp2f((xa[k] - mx) * SCL); xb[k] = __builtin_amdgcn_exp2f((xb[k] - mx) * SCL); xm[k] = __builtin_amdgcn_exp2f((xm[k] - mx) * SCL);
        sum += xa[k] + xb[k]; summ += xm[k];
      }
      sum += g < 2 ? summ : 0.f;
      const float inv = __builtin_amdgcn_rcpf(x32_sum(x16_sum(sum)));
      v4u px; v2u pm2;
#pragma unroll
      for (int k = 0; k < 4; ++k) px[k] = pack2(xa[k] * inv, xb[k] * inv);
      pm2.x = pack2(xm[0] * inv, xm[1] * inv); pm2.y = pack2(xm[2] * inv, xm[3] * inv);
      const int so = s < 0 ? JUNK : PX_OFF + s * NH * 2560;
      st128(px, r, po + l16, so);
      st64(pm2, r, g < 2 ? po + 1024 + n * 16 + g * 8 : OOB, so);
    }
    drain();
  } else if (w < 6) {
    LAS unsigned char* wa = L + LM_IMG + QIMG + kimg_w(w - 4, lane);
    for (int s = sopaque(-1); s < NSEG; ++s) {
      const f32x4 z0 = zero4(), z1 = zero4();
      Gather<1> ga;
      ga.run(r, L, l16, 2 * w - 4, sb, rounds);
      bar();
      stage1<32, 0>(L, wt, z0, z1, l16, idx, wa, []() {});
      bar();
      if (s >= 0) __builtin_amdgcn_s_sleep(PSLP);
      sb = s < 0 ? sb : next_slot(sb, par);
    }
  } else if (w == 6) {
    LAS unsigned char* wa = L + LM_IMG + QIMG + kimg_w(2, lane);
    for (int s = sopaque(-1); s < NSEG; ++s) {
      const f32x4 z0 = zero4(), z1 = zero4();
      Gather<0> ga;
      ga.run(r, L, l16, 8, sb, rounds);
      bar();
      stage1<32, 0>(L, wt, z0, z1, l16, idx, wa, []() {});
      bar();
      if (s >= 0) __builtin_amdgcn_s_sleep(PSLP);
      if (s > 0 && lane == 0) set_flag(r, CTRL + (L_SP + h) * 128, s - 1);
      sb = s < 0 ? sb : next_slot(sb, par);
    }
  } else {
    LAS unsigned char* wa = L + LM_IMG + QIMG + kimg_w(3, lane);
    int seen0 = -2, dur = 60 << 7;
    auto fetch = [&](int s) {
      wait_prog(r, L_XP + h, 1, s, lane, seen0, rounds);
      v4u t[16];
#pragma unroll
      for (int k = 0; k < 16; ++k) t[k] = ld128(r, l16 + (k & 3) * 1024, KQ_OFF + (s * NH + h) * 16384 + (k >> 2) * 4096);
#pragma unroll
      for (int k = 0; k < 16; ++k) *(LAS v4u*)(L + LM_KQ + (s & 1) * 16384 + l16 + k * 1024) = t[k];
    };
    const int ho = lane == 0 ? T_HS + h * 128 : OOB;
    { const u32 t = ld32(r, KQ_OFF + (3 * NH + h) * 16384 + l16, 0); asm volatile("" ::"v"(t)); }
    for (int s = sopaque(-1); s < NSEG; ++s) {
      const f32x4 z0 = zero4(), z1 = zero4();
      bar();
      const u32 tb = rt7();
      st32(((tb + dur) & ~127u) | (s & 127), r, ho, par);
      stage1<32, 0>(L, wt, z0, z1, l16, idx, wa, []() {});
      bar();
      if (s + 1 < NSEG) fetch(s + 1);
      if (s >= 0) dur = track(dur, max((int)(uni(*(LAS u32*)(L + LM_T)) - tb), (int)(uni(*(LAS u32*)(L + LM_T + 4)) - tb)));
    }
  }
  bar();
  if (w == 6 && lane == 0) set_flag(r, CTRL + (L_SP + h) * 128, NSEG - 1);
}

template <int RD>
DEVI void get_v1(LAS unsigned char* M, int w, int lane, v8u (&w1)[9]) {
  const int n = lane & 15, gb = lane >> 4;
  const u32 sel = (w & 1) ? 0x07060302u : 0x05040100u;
  LAS unsigned char* b = M + ((gb >> 1) + 8 * (gb & 1)) * 256 + (((w >> 1) * 64) ^ ((gb & 1) << 6)) + n * 4;
#pragma unroll
  for (int i = 0; i < 36; ++i) {
    const int hs = 9 * RD + (i >> 2), d = 32 * (i >> 2) + 16 * (i & 1) + 4 * ((i >> 1) & 1);
    w1[hs >> 1][4 * (hs & 1) + (i & 3)] = __builtin_amdgcn_perm(*(LAS u32*)(b + (d + 2) * 256), *(LAS u32*)(b + d * 256), sel);
  }
}
template <int RD>
DEVI void get_v2(LAS unsigned char* M, int w, int lane, v8u (&w2)[5][2]) {
  const int n = lane & 15, gb = lane >> 4;
  const u32 sel = (w & 1) ? 0x07060302u : 0x05040100u;
  LAS unsigned char* b = M + 8 * gb * 1152 + n * 4;
#pragma unroll
  for (int pr = 0; pr < 3; ++pr) {
    LAS unsigned char* bp = b + ((64 * (pr < 2 ? 2 * w + pr : 16 + ((w >> 1) & 1))) ^ ((gb & 1) << 6));
#pragma unroll
    for (int q = 0; q < 8; ++q) {
      const int e = 32 * (q >> 2) + 2 * (q & 3);
      const u32 x = *(LAS u32*)(bp + e * 1152), y = *(LAS u32*)(bp + (e + 1) * 1152);
      if (pr < 2) { w2[2 * pr][RD][q] = lo2(x, y); w2[2 * pr + 1][RD][q] = hi2(x, y); }
      else w2[4][RD][q] = __builtin_amdgcn_perm(y, x, sel);
    }
  }
}
template <int GATHER, int FIVE, int HINT>
DEVI void v_loop(const Params& p, const WS& r, LAS unsigned char* L, int h, int w, const v8u (&w1)[9], const v8u (&w2)[5][2]) {
  const int lane = threadIdx.x & 63, n = lane & 15, g = lane >> 4;
  const int idx = (lane & 2) ? 0xEEEE : 0x4444, l16 = opaque(lane * 16);
  int rounds = 0;
  const int par = wait_mem0(r, lane, rounds);
  LAS unsigned char* wa = L + LM_IMG + aimg_w(w >> 1, lane) + (w & 1) * 2;
  LAS unsigned char* img = L + LM_IMG + aimg_r(lane);
  const int lr0 = (4 * w + g) * CHUNK + h * 256 + n * 16, lr1 = g == 0 ? (28 + w) * CHUNK + h * 256 + n * 16 : OOB;
  const int lz0 = (4 * w + g) * 8192 + h * 256 + n * 16, lz1 = g == 0 ? (28 + w) * 8192 + h * 256 + n * 16 : OOB;
  const int ho = (w == 7 && lane == 0) ? T_HV + h * 128 : OOB;
  int sb = par, sprev = par + 2 * SLOT, dur = 50 << 7;
  u32 tb = 0;
  for (int s = sopaque(-1); s < NSEG; ++s) {
    f32x4 a1 = zero4(), a[5];
#pragma unroll
    for (int i = 0; i < 4 + FIVE; ++i) a[i] = zero4();
    const int so0 = opaque(lr0 + (s < 0 ? JUNK : sb)), so1 = opaque(lr1 + (s < 0 ? JUNK : sb));
    Gather<GATHER == 2> ga;
    if (GATHER) ga.run(r, L, l16, GATHER == 2 ? 2 * w : 8, sb, rounds);
    else drain();
    if (HINT && s > 0) {
      const int m = row_max_i((int)(*(LAS u32*)(L + LM_T + (lane & 7) * 4) - tb));
      dur = track(dur, uni(m));
    }
    bar();
    if (HINT) tb = rt7();
    v4u af[9];
#pragma unroll
    for (int ks = 0; ks < 9; ++ks) af[ks] = *(LAS v4u*)(L + LM_MEM + ks * 1024 + l16);
    SB();
    if (HINT) { st32(((tb + dur) & ~127u) | (s & 127), r, ho, par); SB(); }
#pragma unroll
    for (int ks = 0; ks < 9; ++ks) a1 = smf64(af[ks], w1[ks], a1, idx);
    const f32x2 f1 = fold(a1);
    const u32 v2 = pack2(f1.x, f1.y);
    *(LAS u16*)wa = (u16)v2;
    *(LAS u16*)(wa + 16) = (u16)(v2 >> 16);
    bar();
    v4u va[2];
#pragma unroll
    for (int k = 0; k < 2; ++k) va[k] = *(LAS v4u*)(img + k * AIMG);
    auto fin = [&](int i) { const f32x2 f = fold(a[i]); return guard(pack2(f.x, f.y)); };
    auto xch = [&](u32 u0, u32 u1, u32 u2, u32 u3) {
      const auto e0 = __builtin_amdgcn_permlane32_swap(u0, u2, false, false);
      const auto e1 = __builtin_amdgcn_permlane32_swap(u1, u3, false, false);
      const auto f0 = __builtin_amdgcn_permlane16_swap(e0[0], e1[0], false, false);
      const auto f1 = __builtin_amdgcn_permlane16_swap(e0[1], e1[1], false, false);
      return v4u{f0[0], f0[1], f1[0], f1[1]};
    };
    v4u o1;
    if (FIVE) a[4] = smf64(va[0], w2[4][0], a[4], idx);
    a[0] = smf64(va[0], w2[0][0], a[0], idx);
    if (FIVE) a[4] = smf64(va[1], w2[4][1], a[4], idx);
    else a[2] = smf64(va[0], w2[2][0], a[2], idx);
    a[0] = smf64(va[1], w2[0][1], a[0], idx);
    if (FIVE) a[2] = smf64(va[0], w2[2][0], a[2], idx);
    a[1] = smf64(va[0], w2[1][0], a[1], idx);
    SB();
    u32 u4;
    if (FIVE) u4 = fin(4);
    a[2] = smf64(va[1], w2[2][1], a[2], idx);
    SB();
    if (FIVE) { o1 = xch(u4, u4, u4, u4); st128(o1, r, so1, 0); }
    a[3] = smf64(va[0], w2[3][0], a[3], idx);
    a[1] = smf64(va[1], w2[1][1], a[1], idx);
    SB();
    const u32 u0 = fin(0);
    a[3] = smf64(va[1], w2[3][1], a[3], idx);
    SB();
    const u32 u2 = fin(2);
    const auto e0 = __builtin_amdgcn_permlane32_swap(u0, u2, false, false);
    SB();
    const auto e1 = __builtin_amdgcn_permlane32_swap(fin(1), fin(3), false, false);
    const auto f0 = __builtin_amdgcn_permlane16_swap(e0[0], e1[0], false, false);
    const auto f1_ = __builtin_amdgcn_permlane16_swap(e0[1], e1[1], false, false);
    const v4u o0 = v4u{f0[0], f0[1], f1_[0], f1_[1]};
    st128(o0, r, so0, 0);
    SB();
    *(LAS u32*)(L + LM_T + w * 4) = rt7();

    if (s >= 0) __builtin_amdgcn_s_sleep(VBG);
    const int zo = s < 0 ? JUNK : CZ_OFF + s * NTILE * 8192;
    st128(o0, r, lz0, zo);
    st128(v4u{SENT, SENT, SENT, SENT}, r, lr0, sprev);
    if (FIVE) { st128(o1, r, lz1, zo); st128(v4u{SENT, SENT, SENT, SENT}, r, lr1, sprev); }
    if (w == 6 && s > 0 && lane == 0) set_flag(r, CTRL + (L_VP + h) * 128, s - 1);
    if (GATHER && s >= 0) __builtin_amdgcn_s_sleep(VSLP);
    sprev = s < 0 ? sprev : sb;
    sb = s < 0 ? sb : next_slot(sb, par);
  }
  drain();
}

DEVI void role_V(const Params& p, const WS& r, LAS unsigned char* L, int h) {
  const int lane = threadIdx.x & 63, w = uni(threadIdx.x >> 6);
  v8u w1[9], w2[5][2];
  {
    const int o = h * 256 + (lane & 15) * 16;
#pragma unroll
    for (int q = 0; q < 6; ++q) st128(v4u{SENT, SENT, SENT, SENT}, r, (4 * w + (lane >> 4)) * CHUNK + o, (q & 1) * 8192 + (q >> 1) * SLOT);
    if (w < 6) st128(v4u{SENT, SENT, SENT, SENT}, r, (32 + (lane >> 4)) * CHUNK + o, (w & 1) * 8192 + (w >> 1) * SLOT);
    else st32(SENT, r, lane == 0 ? T_HV + h * 128 : OOB, (w & 1) * 8192);
  }
  {
    WLoad<1> wl; wl.init(p.wv, p.wo, h);
    v4u sa[9], sb_[9], sc[9];
    wl.req<0, 0>(sa); wl.req<1, 0>(sb_);
    wl.put<0>(L, sa); wl.req<0, 1>(sc); bar();
    wl.put<1>(L, sb_); wl.req<1, 1>(sa); get_v1<0>(L, w, lane, w1); bar();
    wl.put<0>(L, sc); get_v2<0>(L + 73728, w, lane, w2); bar();
    wl.put<1>(L, sa); drain(); get_v1<1>(L, w, lane, w1); bar();
    get_v2<1>(L + 73728, w, lane, w2); bar();
  }
  if (w == 6 && lane == 0) set_flag(r, CTRL + (L_VP + h) * 128, -1);
  if (w < 4) { __builtin_amdgcn_s_setprio(VPRIO); v_loop<2, 0, 0>(p, r, L, h, w, w1, w2); }
  else if (w == 4) v_loop<1, 1, 0>(p, r, L, h, w, w1, w2);
  else if (w < 7) v_loop<0, 1, 0>(p, r, L, h, w, w1, w2);
  else v_loop<0, 1, 1>(p, r, L, h, w, w1, w2);
  bar();
  if (w == 6 && lane == 0) set_flag(r, CTRL + (L_VP + h) * 128, NSEG - 1);
}

DEVI u64 cyc() { u64 t; asm volatile("s_memtime %0\n\ts_waitcnt lgkmcnt(0)" : "=s"(t)::"memory"); return t; }

DEVI void role_R(const Params& p, const WS& r, LAS unsigned char* L, int j) {
  const int lane = threadIdx.x & 63, w = uni(threadIdx.x >> 6), n = lane & 15, g = lane >> 4;
  int rounds = 0;
  if (j == 0 && w == 4) {
    u32 best[2] = {~0u, ~0u};
    if (lane == 0) {
      for (int it = 0; it < 3; ++it)
        for (int q = 0; q < 2; ++q) {
          GAS u32* a = (GAS u32*)(p.ws + MEM_OFF + CHUNK + q * 8192 + 7168);
          const u64 t0 = cyc();
          u32 v = __hip_atomic_fetch_add(a, 1u, __ATOMIC_RELAXED, __HIP_MEMORY_SCOPE_AGENT);
          asm volatile("s_waitcnt vmcnt(0)" ::"v"(v) : "memory");
          const u32 d = (u32)(cyc() - t0);
          best[q] = d < best[q] ? d : best[q];
        }
      set_flag(r, CTRL + L_PAR * 128, best[1] < best[0] ? 1 : 0);
    }
  }
  const int par = wait_parity(r, rounds);
  int sb = par;
  if (w < 4) {
    const int idx = (lane & 2) ? 0xEEEE : 0x4444;
    const int pxo = opaque(P_OFF + w * CHUNK + (g >> 1) * 1024 + (n >> 3) * 512 + (g & 1) * 128 + (n & 7) * 16);
    const int pmo = P_OFF + w * CHUNK + g * 1024 + (n >> 3) * 512 + 256 + (n & 7) * 8;
    const int co = opaque(j * CHUNK + w * 2048 + lane * 16);
    const int redw = LM_RED + w * 512 + (g & 1) * 256 + (n >> 3) * 64 + (g >> 1) * 32 + (n & 7) * 4;
    const int hio = (lane < 32 ? T_HS : T_HV) + (8 * w + ((lane >> 2) & 7)) * 128;
    for (int s = sopaque(-2); s < NSEG - 1; ++s) {
      v4u bz[8];
#pragma unroll
      for (int q = 0; q < 8; ++q) bz[q] = *(LAS v4u*)(L + LM_BZ + (s & 1) * 32768 + (8 * w + q) * 1024 + lane * 16);
      if (s > 0) __builtin_amdgcn_s_sleep(RSLP);
      if (s == -1) { int seen = -2; wait_prog(r, L_SP, 64, -1, lane, seen, rounds); }
      const u32 hv = hint_wait(r, hio, par, s);
      const u32 tv = s < 0 ? rt7() : hv + (lane < 32 ? MAR_R : MAR_C) * 128;
      LAS unsigned char* rw = (LAS unsigned char*)(size_t)opaque((int)(size_t)(L + (s & 1) * 2048 + redw));
      auto attempt = [&](bool first) {
        v4u px[4]; v2u pm[2];
        if (first) time_wait<0>(tv);
#pragma unroll
        for (int q = 0; q < 4; ++q) { px[q] = ld128(r, pxo + (q & 1) * 2048, sb + (q >> 1) * 4096); SB(); }
#pragma unroll
        for (int q = 0; q < 2; ++q) { pm[q] = ld64(r, pmo, sb + q * 4096); SB(); }
        if (first) time_wait<1>(tv);
        const v4u c0 = ld128(r, co, sb);
        SB();
        const v4u c1 = ld128(r, co + 1024, sb);
        f32x4 a = zero4();
        u32 mp = 0;
        SB();
#pragma unroll
        for (int q = 0; q < 4; ++q) {
          a = smf64(px[q], cat(bz[2 * q], bz[2 * q + 1]), a, idx);
          SB();
          mp = umax(mp, mx4(px[q]));
          asm volatile("" : "+v"(mp));
          SB();
        }
        mp = umax(mp, umax(umax(pm[0].x, pm[0].y), umax(pm[1].x, pm[1].y)));
        asm volatile("" : "+v"(mp));
        SB();
        a = smf32(pm[0], c0, a, idx);
        SB();
        a = smf32(pm[1], c1, a, idx);
        SB();
        mp = umax(mp, umax(mx4(c0), mx4(c1)));
        const f32x2 fa = fold(a);
        *(LAS float*)rw = fa.x;
        *(LAS float*)(rw + 128) = fa.y;
        return any_sent(mp);
      };
      if (__builtin_expect(attempt(true), 0) && s >= 0) {
        POLL(bad = attempt(false));
      }
      bar();
      __builtin_amdgcn_s_sleep(4);
      sb = s < 0 ? sb : next_slot(sb, par);
    }
  } else {
    const int mo = MEM_OFF + (j >> 5) * CHUNK + (j & 31) * 256 + lane * 4, l16 = opaque(lane * 16);
    int snext = par + SLOT, seen0 = -2, seen1 = -2;
    auto fetch = [&](int s) {
      if (s >= 0) wait_prog2(r, L_YP + 8 * (w - 4), 8, s, lane, seen0, seen1, rounds);
      else s = 1;
      v4u t[8];
#pragma unroll
      for (int q = 0; q < 8; ++q) t[q] = ld128(r, l16 + (q & 3) * 1024, BZ_OFF + (s * NTILE + j) * 32768 + (w - 4) * 8192 + (q >> 2) * 4096);
#pragma unroll
      for (int q = 0; q < 8; ++q) *(LAS v4u*)(L + LM_BZ + (s & 1) * 32768 + (8 * (w - 4) + q) * 1024 + l16) = t[q];
    };
    if (w == 4) {
      const u16* src = p.mem0 + ((lane >> 4) * 2 + ((lane >> 2) & 1)) * DM + 32 * (j >> 1) + (j & 1) + 16 * ((lane >> 3) & 1) + 4 * (lane & 3);
      st32(guard((u32)src[0] | ((u32)src[2] << 16)), r, mo, sb);
      st32(SENT, r, mo, par + SLOT);
      st32(SENT, r, mo, par + 2 * SLOT);
      drain();
      if (lane == 0) set_flag(r, CTRL + (L_RP + j) * 128, par >> 13);
      __builtin_amdgcn_s_setprio(2);
      for (int s = sopaque(-2); s < NSEG - 1; ++s) {
        LAS unsigned char* red = L + LM_RED + (s & 1) * 2048 + lane * 8;
        const int so = s < 0 ? JUNK : snext;
        fetch(s + 1);
        drain();
        bar();
        f32x2 t[4];
#pragma unroll
        for (int q = 0; q < 4; ++q) t[q] = *(LAS f32x2*)(red + q * 512);
        const f32x2 u = (t[0] + t[1]) + (t[2] + t[3]);
        st32(guard(pack2(u.x, u.y)), r, mo, so);
        SB();
        if (s >= 0) {
          st32(SENT, r, mo, sb);
          sb = snext;
          snext = next_slot(snext, par);
        }
      }
    } else {
      for (int s = sopaque(-2); s < NSEG - 1; ++s) {
        fetch(s + 1);
        bar();
        __builtin_amdgcn_s_sleep(4);
      }
    }
  }
}

DEVI int ximg_rd(int lane) { const int n = lane & 15, g = lane >> 4; return n * 64 + (g ^ ((n >> 3) << 1)) * 16; }
DEVI int ximg_piece(int lane) { return ((lane & 3) ^ ((lane >> 5) << 1)) * 16; }
template <int DEPTH>
DEVI void proj32(LAS unsigned char* xb, const v4u (&wt)[2][18], v4u (&o)[2]) {
  f32x4 acc[2][2];
#pragma unroll
  for (int i = 0; i < 4; ++i) acc[i >> 1][i & 1] = zero4();
  v4u xf[DEPTH][2];
#pragma unroll
  for (int ks = 0; ks < DEPTH - 1; ++ks) { xf[ks][0] = *(LAS v4u*)(xb + ks * 1024); xf[ks][1] = *(LAS v4u*)(xb + (18 + ks) * 1024); }
  SB();
#pragma unroll
  for (int ks = 0; ks < 18; ++ks) {
    const int kn = ks + DEPTH - 1;
    if (kn < 18) { xf[kn % DEPTH][0] = *(LAS v4u*)(xb + kn * 1024); xf[kn % DEPTH][1] = *(LAS v4u*)(xb + (18 + kn) * 1024); }
    SB();
#pragma unroll
    for (int m = 0; m < 2; ++m) {
      acc[0][m] = mfma(wt[0][ks], xf[ks % DEPTH][m], acc[0][m]);
      acc[1][m] = mfma(wt[1][ks], xf[ks % DEPTH][m], acc[1][m]);
    }
    SB();
  }
#pragma unroll
  for (int m = 0; m < 2; ++m)
#pragma unroll
    for (int q = 0; q < 4; ++q) o[m][q] = pack2(acc[0][m][q], acc[1][m][q]);
}

struct XStage {
  rsrc_t rx; int go, lo; v4u st[9];
  DEVI void init(const Params& p, int tok, int q, int lane) {
    rx = mk_rsrc(p.x, 2048 * DM * 2);
    go = tok * (DM * 2) + (q & 1) * 9 * 64 + ximg_piece(lane);
    lo = q * 9 * 1024 + lane * 16;
  }
  DEVI void req(int s) {
#pragma unroll
    for (int k = 0; k < 9; ++k) st[k] = pl128(rx, go + k * 64, (s < NSEG ? s : NSEG - 1) * 32 * DM * 2);
  }
  DEVI void put(LAS unsigned char* L) {
#pragma unroll
    for (int k = 0; k < 9; ++k) *(LAS v4u*)(L + lo + k * 1024) = st[k];
  }
};

DEVI int seg_of(int hp, int i) { return i < HSEG / 2 ? 2 * i + hp : i + HSEG / 2; }

DEVI void role_X(const Params& p, const WS& r, LAS unsigned char* L, int h) {
  const int lane = threadIdx.x & 63, w = uni(threadIdx.x >> 6);
  const int rd = ximg_rd(lane), oo = KQ_OFF + h * 16384 + (w >> 2) * 8192 + (w & 3) * 1024 + lane * 16;
  constexpr int cnt = NSEG;
  v4u wt[2][18];
  if (w < 4) {
    XStage xs; xs.init(p, 2 * (lane >> 2) + (w >> 1), w, lane);
    xs.req(0);
    LOAD_W(0, p.wk, p.wq, get_w1_dense, 0, w, xs.put(L); xs.req(1))
    __builtin_amdgcn_s_setprio(1);
    const int fo = (w == 0 && lane == 0) ? CTRL + (L_XP + h) * 128 : OOB;
    for (int it = sopaque(0); it < cnt; ++it) {
      const int s = it;
      v4u o[2];
      proj32<3>(L + (it & 1) * 36864 + rd, wt, o);
      st128(o[0], r, oo, s * NH * 16384);
      st128(o[1], r, oo + 4096, s * NH * 16384);
      SB();
      xs.put(L + ((it + 1) & 1) * 36864);
      SB();
      xs.req(it + 2);
      asm volatile("s_waitcnt vmcnt(9)" ::: "memory");
      bar();
      set_flag(r, fo, it < XUN || it + 1 == cnt ? s : it - 1);
    }
    __builtin_amdgcn_s_setprio(0);
  } else {
    LOAD_W(0, p.wk, p.wq, get_w1_dense, 1, w & 3, )
    for (int it = sopaque(0); it < cnt; ++it) {
      const int s = it;
      v4u o[2];
      proj32<4>(L + (it & 1) * 36864 + rd, wt, o);
      st128(o[0], r, oo, s * NH * 16384);
      st128(o[1], r, oo + 4096, s * NH * 16384);
      if (it < XUN || it + 1 == cnt) drain();
      asm volatile("s_waitcnt vmcnt(2)" ::: "memory");
      bar();
    }
  }
}

DEVI void role_Y(const Params& p, const WS& r, LAS unsigned char* L, int h, int hp) {
  const int lane = threadIdx.x & 63, w = uni(threadIdx.x >> 6);
  const int cnt = hp ? HSEG / 2 : NSEG - HSEG / 2;
  constexpr int VB = 73728;
  if (w < 4) {
    v4u wt[2][18];
    {
      const int row = lane >> 2;
      XStage xs; xs.init(p, 8 * (row >> 2) + (row & 3) + 4 * (w >> 1), w, lane);
      xs.req(seg_of(hp, 0));
      LOAD_W(1, p.wv, p.wo, get_w1_dense, 0, w, xs.put(L))
    }
    const int rd = ximg_rd(lane);
    const int fo = (w == 0 && lane == 0) ? CTRL + (L_YP + hp * 32 + h) * 128 : OOB;
    for (int it = sopaque(0); it < cnt; ++it) {
      v4u o[2];
      proj32<4>(L + (it & 1) * 36864 + rd, wt, o);
      *(LAS v4u*)(L + VB + (it & 1) * 8192 + w * 1024 + lane * 16) = o[0];
      *(LAS v4u*)(L + VB + (it & 1) * 8192 + 4096 + w * 1024 + lane * 16) = o[1];
      bar();
      if (it == 0) { bar(); set_flag(r, fo, seg_of(hp, 0)); }
      if (it > 1) set_flag(r, fo, seg_of(hp, it - 1));
    }
    bar();
    set_flag(r, fo, seg_of(hp, cnt - 1));
  } else {
    const int c = w - 4, row = lane >> 2;
    XStage xs; xs.init(p, 8 * (row >> 2) + (row & 3) + 4 * (c >> 1), c, lane);
    xs.req(seg_of(hp, 1));
    v8u wt[9][2];
    LOAD_W(1, p.wv, p.wo, get_w2, 1, c, )
    __builtin_amdgcn_s_setprio(1);
    xs.put(L + 36864);
    xs.req(seg_of(hp, 2));
    bar();
    const int oo = BZ_OFF + h * 1024 + lane * 16;
    const int t0o = tile_of(c, 0) * 32768, t8o = tile_of(c, 8) * 32768;
    for (int it = sopaque(0); it < cnt; ++it) {
      const int s = seg_of(hp, it);
      v4u vf[2][4];
#pragma unroll
      for (int k = 0; k < 8; ++k) vf[k >> 2][k & 3] = *(LAS v4u*)(L + VB + (it & 1) * 8192 + k * 1024 + lane * 16);
#pragma unroll
      for (int i = 0; i < 9; ++i) {
        f32x4 a0 = {0, 0, 0, 0}, a1 = {0, 0, 0, 0};
#pragma unroll
        for (int k = 0; k < 2; ++k) {
          a0 = mfma(vf[0][2 * k], lo4(wt[i][k]), a0); a1 = mfma(vf[1][2 * k], lo4(wt[i][k]), a1);
          a0 = mfma(vf[0][2 * k + 1], hi4(wt[i][k]), a0); a1 = mfma(vf[1][2 * k + 1], hi4(wt[i][k]), a1);
        }
        st128(v4u{pack2(a0[0], a0[1]), pack2(a0[2], a0[3]), pack2(a1[0], a1[1]), pack2(a1[2], a1[3])}, r, oo,
              s * NTILE * 32768 + (i < 8 ? t0o + i * 32768 : t8o));
      }
      SB();
      xs.put(L + (it & 1) * 36864);
      SB();
      xs.req(seg_of(hp, it + 3));
      asm volatile("s_waitcnt vmcnt(9)" ::: "memory");
      bar();
      if (it == 0) bar();
    }
    __builtin_amdgcn_s_setprio(0);
  }
}

DEVI void role_T(const Params& p, const WS& r, int ti) {
  const int lane = threadIdx.x & 63, w = uni(threadIdx.x >> 6), n = lane & 15, g = lane >> 4;
  const rsrc_t r1 = mk_rsrc(p.w1, DM * FF * 2), r2 = mk_rsrc(p.w2, DM * FF * 2);
  { const u64 t0 = __builtin_amdgcn_s_memrealtime(); while (__builtin_amdgcn_s_memrealtime() - t0 < TDLY) __builtin_amdgcn_s_sleep(16); }
  {
    const rsrc_t rg = mk_rsrc(p.gw, DM * 2), rx = mk_rsrc(p.x, 2048 * DM * 2);
    const int o = lane < 36 ? lane * 32 : OOB, t0 = (ti * 8 + w) * 5;
    const v4u g0 = pl128(rg, o, 0), g1 = pl128(rg, o + 16, 0);
    float mine = 0.f;
#pragma unroll
    for (int t = 0; t < 5; ++t) {
      const int tok = t0 + t < 2048 ? t0 + t : 2047;
      const v4u x0 = pl128(rx, o, tok * DM * 2), x1 = pl128(rx, o + 16, tok * DM * 2);
      float a = 0.f;
#pragma unroll
      for (int q = 0; q < 4; ++q) {
        a += bf2f(x0[q] & 0xFFFFu) * bf2f(g0[q] & 0xFFFFu) + __uint_as_float(x0[q] & 0xFFFF0000u) * __uint_as_float(g0[q] & 0xFFFF0000u);
        a += bf2f(x1[q] & 0xFFFFu) * bf2f(g1[q] & 0xFFFFu) + __uint_as_float(x1[q] & 0xFFFF0000u) * __uint_as_float(g1[q] & 0xFFFF0000u);
      }
      a = x32_sum(x16_sum(row16_sum(a)));
      mine = lane == t ? a : mine;
    }
    const float gv = __builtin_amdgcn_rcpf(1.f + __builtin_amdgcn_exp2f(-1.4426950408889634f * mine));
    st32(__float_as_uint(gv), r, (lane < 5 && t0 + lane < 2048) ? G_OFF + (t0 + lane) * 4 : OOB, 0);
  }
  const int u0 = ti * 8 + w;
  v4u raw[2][8];
#pragma unroll
  for (int i = 0; i < 2; ++i) {
    const int u = u0 + i * 480 < 648 ? u0 + i * 480 : u0, c = u / 18, ks = u - 18 * c;
#pragma unroll
    for (int q = 0; q < 8; ++q) raw[i][q] = __builtin_amdgcn_raw_buffer_load_b128(r1, (8 * g * FF + 8 * n) * 2, ((32 * ks + q) * FF + 128 * c) * 2, TAUX);
  }
#pragma unroll
  for (int i = 0; i < 2; ++i) {
    const int u = u0 + i * 480 < 648 ? u0 + i * 480 : u0, c = u / 18, ks = u - 18 * c;
#pragma unroll
    for (int t = 0; t < 8; ++t) {
      v4u o;
#pragma unroll
      for (int q = 0; q < 4; ++q) o[q] = __builtin_amdgcn_perm(raw[i][2 * q + 1][t >> 1], raw[i][2 * q][t >> 1], (t & 1) ? 0x07060302u : 0x05040100u);
      st128(o, r, W1T_OFF + lane * 16, ((c * 4 + (t >> 1)) * 18 + ks) * 2048 + (t & 1) * 1024);
    }
  }
  u32 r9[2][8];
#pragma unroll
  for (int i = 0; i < 2; ++i) {
    const int u = u0 + i * 480 < 576 ? u0 + i * 480 : u0, k = u >> 2, c4 = u & 3;
    const int so = ((128 * (k >> 2) + 2 * (k & 3)) * DM + 144 * c4) * 2;
#pragma unroll
    for (int q = 0; q < 8; ++q) raw[i][q] = __builtin_amdgcn_raw_buffer_load_b128(r2, (32 * g * DM + 8 * n) * 2, so + (8 * (q >> 1) + (q & 1)) * DM * 2, TAUX);
#pragma unroll
    for (int q = 0; q < 8; ++q) r9[i][q] = __builtin_amdgcn_raw_buffer_load_b16(r2, (32 * g * DM + 128 + n) * 2, so + (8 * (q >> 1) + (q & 1)) * DM * 2, 0);
  }
#pragma unroll
  for (int i = 0; i < 2; ++i) {
    const int u = u0 + i * 480 < 576 ? u0 + i * 480 : u0;
#pragma unroll
    for (int t = 0; t < 9; ++t) {
      v4u o;
#pragma unroll
      for (int q = 0; q < 4; ++q)
        o[q] = t < 8 ? __builtin_amdgcn_perm(raw[i][2 * q + 1][(t & 7) >> 1], raw[i][2 * q][(t & 7) >> 1], (t & 1) ? 0x07060302u : 0x05040100u)
                     : __builtin_amdgcn_perm(r9[i][2 * q + 1], r9[i][2 * q], 0x05040100u);
      st128(o, r, W2T_OFF + lane * 16, u * 9216 + t * 1024);
    }
  }
  drain();
  bar();
  if (threadIdx.x == 0) set_flag(r, CTRL + (L_TP + ti) * 128, 0);
}

DEVI float gelu_tanh(float x) {
  const float e = __builtin_amdgcn_exp2f(x * (-2.302208198f - 0.1029432397f * x * x));
  return x * __builtin_amdgcn_rcpf(1.f + e);
}

#define FLD ld128
DEVI void role_F(const Params& p, const WS& r, LAS unsigned char* L, int tt, int sp, int c0, int c1) {
  const int lane = threadIdx.x & 63, w = uni(threadIdx.x >> 6), n = lane & 15, g = lane >> 4;
  constexpr int HB = 73728;
  const rsrc_t rx = mk_rsrc(p.x, 2048 * DM * 2);
  { int rounds = 0, seen = -2; wait_prog(r, L_TP, NTB, 0, lane, seen, rounds); }
  bar();
  {
    const int go = (tt * 64 + (w >> 1) * 16 + (lane >> 2)) * (DM * 2) + (w & 1) * 9 * 64 + ximg_piece(lane);
    v4u st[9];
#pragma unroll
    for (int k = 0; k < 9; ++k) st[k] = pl128(rx, go + k * 64, 0);
#pragma unroll
    for (int k = 0; k < 9; ++k) *(LAS v4u*)(L + (w * 9 + k) * 1024 + lane * 16) = st[k];
  }
  if (w < 4) {
    const rsrc_t rb = mk_rsrc(p.b1, FF * 2);
    const int vo = W1T_OFF + w * 18 * 2048 + lane * 16;
    constexpr int WIN = FWIN;
    __builtin_amdgcn_s_setprio(FPRIO);
    v4u wf[WIN][2];
    auto req = [&](int slot, int c, int ks) {
      wf[slot][0] = FLD(r, vo, (c * 72 + ks) * 2048);
      wf[slot][1] = FLD(r, vo + 1024, (c * 72 + ks) * 2048);
    };
#pragma unroll
    for (int k = 0; k < WIN - 1; ++k) req(k, c0, k);
    bar();
    const int rd = ximg_rd(lane);
    for (int c = c0; c < c1; ++c) {
      const int cn = c + 1 < c1 ? c + 1 : c;
      v4u bias;
#pragma unroll
      for (int q = 0; q < 4; ++q) bias[q] = pl32(rb, (32 * g + 2 * w) * 2 + q * 16, c * 256);
      f32x4 acc[2][4];
#pragma unroll
      for (int i = 0; i < 8; ++i) acc[i >> 2][i & 3] = zero4();
      v4u xf[2][4];
#pragma unroll
      for (int m = 0; m < 4; ++m) xf[0][m] = *(LAS v4u*)(L + (m * 18) * 1024 + rd);
#pragma unroll
      for (int ks = 0; ks < 18; ++ks) {
        const int kn = ks + WIN - 1;
        req(kn % WIN, kn < 18 ? c : cn, kn % 18);
        if (ks + 1 < 18) {
#pragma unroll
          for (int m = 0; m < 4; ++m) xf[(ks + 1) & 1][m] = *(LAS v4u*)(L + (m * 18 + ks + 1) * 1024 + rd);
        }
        SB();
#pragma unroll
        for (int m = 0; m < 4; ++m) { acc[0][m] = mfma(wf[ks % WIN][0], xf[ks & 1][m], acc[0][m]); acc[1][m] = mfma(wf[ks % WIN][1], xf[ks & 1][m], acc[1][m]); }
        SB();
      }
#pragma unroll
      for (int m = 0; m < 4; ++m) {
        v4u o;
#pragma unroll
        for (int q = 0; q < 4; ++q)
          o[q] = pack2(gelu_tanh(acc[0][m][q] + bf2f(bias[q] & 0xFFFFu)), gelu_tanh(acc[1][m][q] + __uint_as_float(bias[q] & 0xFFFF0000u)));
        *(LAS v4u*)(L + HB + ((c - c0) & 1) * 16384 + (m * 4 + w) * 1024 + lane * 16) = o;
      }
      bar();
    }
  } else {
    const int c4 = w - 4;
    const int vo = W2T_OFF + c4 * 9216 + lane * 16;
    v4u bw[2][9];
#pragma unroll
    for (int t = 0; t < 9; ++t) bw[0][t] = FLD(r, vo + t * 1024, 4 * c0 * 36864);
    f32x4 acc[9][4];
#pragma unroll
    for (int i = 0; i < 36; ++i) acc[i >> 2][i & 3] = zero4();
    bar();
    bar();
    for (int c = c0; c < c1; ++c) {
#pragma unroll
      for (int ks = 0; ks < 4; ++ks) {
        const int kn = 4 * c + ks + 1 < 4 * c1 ? 4 * c + ks + 1 : 4 * c + ks;
        v4u hf[4];
#pragma unroll
        for (int m = 0; m < 4; ++m) hf[m] = *(LAS v4u*)(L + HB + ((c - c0) & 1) * 16384 + (m * 4 + ks) * 1024 + lane * 16);
#pragma unroll
        for (int t = 0; t < 9; ++t) {
          bw[(ks + 1) & 1][t] = FLD(r, vo + t * 1024, kn * 36864);
          SB();
#pragma unroll
          for (int m = 0; m < 4; ++m) acc[t][m] = mfma(hf[m], bw[ks & 1][t], acc[t][m]);
          SB();
        }
      }
      if (c + 1 < c1) bar();
    }
    const int xo = FP_OFF + (tt * 4 + c4) * 36864 + lane * 16;
    if (sp == 1) {
#pragma unroll
      for (int i = 0; i < 36; ++i) st128(__builtin_bit_cast(v4u, acc[i >> 2][i & 3]), r, xo, i * 1024);
    } else {
      int rounds = 0, seen = -2;
      wait_prog(r, L_FP + 32 + tt, 1, 0, lane, seen, rounds);
#pragma unroll
      for (int j = 0; j < 3; ++j) {
        v4u t[12];
#pragma unroll
        for (int i = 0; i < 12; ++i) t[i] = ld128(r, xo, (12 * j + i) * 1024);
#pragma unroll
        for (int i = 0; i < 12; ++i) { acc[3 * j + (i >> 2)][i & 3] += __builtin_bit_cast(f32x4, t[i]); asm volatile("" : "+v"(acc[3 * j + (i >> 2)][i & 3])); }
        SB();
      }
      const rsrc_t ro = mk_rsrc(p.out, 2048 * DM * 2), rb = mk_rsrc(p.b2, DM * 2);
      const v4u b8 = pl128(rb, (144 * c4 + 8 * n) * 2, 0);
      const float b9 = bf2f(__builtin_amdgcn_raw_buffer_load_b16(rb, (144 * c4 + 128 + n) * 2, 0, 0));
      if (tt < CUT) {
        const int sg = 2 * tt + (lane >= 9), want = lane < 18 ? sg : -2;
        const int vo2 = CTRL + (L_OP + (sg % 3) * 9 + lane % 9) * 128;
        POLL(const int v = get_flag(r, vo2); bad = __builtin_amdgcn_ballot_w64(v < want) != 0; if (bad) __builtin_amdgcn_s_sleep(32));
      }
      const int o8 = ((tt * 64 + 4 * g) * DM + 144 * c4 + 8 * n) * 2, o9 = ((tt * 64 + 4 * g) * DM + 144 * c4 + 128 + n) * 2;
#pragma unroll
      for (int m = 0; m < 4; ++m) {
        const v4u gq = ld128(r, G_OFF + (tt * 64 + 16 * m + 4 * g) * 4, 0);
        v4u a8[4]; u32 a9[4];
#pragma unroll
        for (int q = 0; q < 4; ++q) {
          a8[q] = ld128(ro, tt < CUT ? o8 : OOB, (16 * m + q) * DM * 2);
          a9[q] = __builtin_amdgcn_raw_buffer_load_b16(ro, tt < CUT ? o9 : OOB, (16 * m + q) * DM * 2, SC1);
        }
#pragma unroll
        for (int q = 0; q < 4; ++q) {
          const float k = 1.f - __uint_as_float(gq[q]);
          v4u o;
#pragma unroll
          for (int t = 0; t < 4; ++t)
            o[t] = pack2(k * (acc[2 * t][m][q] + bf2f(b8[t] & 0xFFFFu)) + bf2f(a8[q][t] & 0xFFFFu),
                         k * (acc[2 * t + 1][m][q] + __uint_as_float(b8[t] & 0xFFFF0000u)) + __uint_as_float(a8[q][t] & 0xFFFF0000u));
          st128(o, ro, o8, (16 * m + q) * DM * 2);
          __builtin_amdgcn_raw_buffer_store_b16((u16)pack2(k * (acc[8][m][q] + b9) + bf2f(a9[q]), 0.f), ro, o9, (16 * m + q) * DM * 2, SC1);
        }
        SB();
      }
    }
  }
  drain();
  bar();
  if (threadIdx.x == 0) set_flag(r, CTRL + (L_FP + sp * 32 + tt) * 128, 0);
}

DEVI void role_O(const Params& p, const WS& r, LAS unsigned char* L, int cg, int s0, int ds) {
  const int lane = threadIdx.x & 63, w = uni(threadIdx.x >> 6), n = lane & 15, g = lane >> 4;
  int rounds = 0, seenY0 = -2, seenY1 = -2, seenS = -2;
  const int fm = w & 1, ftp = (w >> 1) & 1, frh = w >> 2;
  const int col = 64 * cg + 32 * ftp + 2 * n;
  const rsrc_t ro = mk_rsrc(p.out, 2048 * DM * 2);
  { int seen = -2; wait_prog(r, L_TP, NTB, 0, lane, seen, rounds); }
  const int svo = CTRL + ((lane & 4 ? L_VP : L_SP) + 4 * w + (lane & 3)) * 128;
  for (int s = s0; s < NSEG - 4; s += ds) {
    wait_prog2(r, L_YP + 4 * w, 4, s, lane, seenY0, seenY1, rounds);
    v4u bz[4][4];
#pragma unroll
    for (int t = 0; t < 4; ++t)
#pragma unroll
      for (int q = 0; q < 4; ++q) bz[t][q] = ld128(r, (4 * w + q) * 1024 + lane * 16, BZ_OFF + (s * NTILE + 4 * cg + t) * 32768);
    const int tt = s >> 1;
    if (tt >= CUT) { int seenF = -2; wait_prog(r, L_FP + tt, 1, 0, lane, seenF, rounds); }
    const int tok = s * 32 + 2 * (4 * g + 2 * frh) + fm;
    u32 fb[2];
    float gt[2];
#pragma unroll
    for (int q = 0; q < 2; ++q) {
      fb[q] = ld32(ro, tt >= CUT ? ((tok + 2 * q) * DM + col) * 2 : OOB, 0);
      gt[q] = __uint_as_float(ld32(r, G_OFF + (tok + 2 * q) * 4, 0));
    }
    if (__builtin_amdgcn_ballot_w64(seenS < s) != 0) {
      POLL(seenS = get_flag(r, svo); bad = __builtin_amdgcn_ballot_w64(seenS < s) != 0; if (bad) __builtin_amdgcn_s_sleep(4));
    }
    v4u px[2][4], pm[2], cz[4];
#pragma unroll
    for (int m = 0; m < 2; ++m) {
#pragma unroll
      for (int q = 0; q < 4; ++q) px[m][q] = ld128(r, ((4 * w + q) * 2 + m) * 1280 + lane * 16, PX_OFF + s * NH * 2560);
      pm[m] = ld128(r, ((4 * w + g) * 2 + m) * 1280 + 1024 + n * 16, PX_OFF + s * NH * 2560);
    }
#pragma unroll
    for (int t = 0; t < 4; ++t) cz[t] = ld128(r, w * 1024 + lane * 16, CZ_OFF + (s * NTILE + 4 * cg + t) * 8192);
#pragma unroll
    for (int t = 0; t < 4; ++t)
#pragma unroll
      for (int m = 0; m < 2; ++m) {
        f32x4 a = {0, 0, 0, 0};
#pragma unroll
        for (int q = 0; q < 4; ++q) a = mfma(px[m][q], bz[t][q], a);
        a = mfma(pm[m], cz[t], a);
        *(LAS f32x4*)(L + w * 8192 + (t * 2 + m) * 1024 + lane * 16) = a;
      }
    bar();
    f32x2 e = {0, 0}, o = {0, 0};
#pragma unroll
    for (int q = 0; q < 8; ++q) {
      e += *(LAS f32x2*)(L + q * 8192 + (4 * ftp + fm) * 1024 + lane * 16 + frh * 8);
      o += *(LAS f32x2*)(L + q * 8192 + (4 * ftp + 2 + fm) * 1024 + lane * 16 + frh * 8);
    }
#pragma unroll
    for (int q = 0; q < 2; ++q) {
      st32(pack2(gt[q] * e[q] + bf2f(fb[q] & 0xFFFFu), gt[q] * o[q] + __uint_as_float(fb[q] & 0xFFFF0000u)), ro, ((tok + 2 * q) * DM + col) * 2, 0);
    }
    drain();
    bar();
    if (threadIdx.x == 0) set_flag(r, CTRL + (L_OP + s0 * 9 + cg) * 128, s);
  }
}

DEVI void role_O2(const Params& p, const WS& r, LAS unsigned char* L, int pr, int m, int s0) {
  const int lane = threadIdx.x & 63, w = uni(threadIdx.x >> 6), n = lane & 15, g = lane >> 4;
  int rounds = 0, seenY = -2, seenS = -2;
  const rsrc_t ro = mk_rsrc(p.out, 2048 * DM * 2);
  const int svo = CTRL + ((lane & 4 ? L_VP : L_SP) + 4 * w + (lane & 3)) * 128;
  bar();
  for (int s = s0; s < NSEG; s += 2) {
    wait_prog(r, L_YP + 4 * w, 4, s, lane, seenY, rounds);
    v4u bz[2][4];
#pragma unroll
    for (int t = 0; t < 2; ++t)
#pragma unroll
      for (int q = 0; q < 4; ++q) bz[t][q] = ld128(r, (4 * w + q) * 1024 + lane * 16, BZ_OFF + (s * NTILE + 2 * pr + t) * 32768);
    { int seenF = -2; wait_prog(r, L_FP + (s >> 1), 1, 0, lane, seenF, rounds); }
    const int tok = s * 32 + 2 * (4 * g + (w & 3)) + m, oo = (tok * DM + 32 * pr + 2 * n) * 2;
    const u32 fb = ld32(ro, oo, 0);
    const float gt = __uint_as_float(ld32(r, G_OFF + tok * 4, 0));
    POLL(seenS = get_flag(r, svo); bad = __builtin_amdgcn_ballot_w64(seenS < s) != 0; if (bad) { if (__builtin_amdgcn_ballot_w64(seenS < s - 2) != 0) __builtin_amdgcn_s_sleep(32); else __builtin_amdgcn_s_sleep(1); });
    v4u px[4], cz[2];
#pragma unroll
    for (int q = 0; q < 4; ++q) px[q] = ld128(r, ((4 * w + q) * 2 + m) * 1280 + lane * 16, PX_OFF + s * NH * 2560);
    const v4u pm = ld128(r, ((4 * w + g) * 2 + m) * 1280 + 1024 + n * 16, PX_OFF + s * NH * 2560);
#pragma unroll
    for (int t = 0; t < 2; ++t) cz[t] = ld128(r, w * 1024 + lane * 16, CZ_OFF + (s * NTILE + 2 * pr + t) * 8192);
#pragma unroll
    for (int t = 0; t < 2; ++t) {
      f32x4 a = {0, 0, 0, 0};
#pragma unroll
      for (int q = 0; q < 4; ++q) a = mfma(px[q], bz[t][q], a);
      a = mfma(pm, cz[t], a);
      *(LAS f32x4*)(L + (s & 2) * 8192 + w * 2048 + t * 1024 + lane * 16) = a;
    }
    bar();
    if (w < 4) {
      float e = 0.f, o = 0.f;
#pragma unroll
      for (int q = 0; q < 8; ++q) {
        e += *(LAS float*)(L + (s & 2) * 8192 + q * 2048 + lane * 16 + w * 4);
        o += *(LAS float*)(L + (s & 2) * 8192 + q * 2048 + 1024 + lane * 16 + w * 4);
      }
      st32(pack2(gt * e + bf2f(fb & 0xFFFFu), gt * o + __uint_as_float(fb & 0xFFFF0000u)), ro, oo, 0);
    }
  }
  if (pr == 0 && m == 0 && s0 == NSEG - 3 && threadIdx.x == 0) st64(v2u{r.klo, r.khi}, r, CTRL + L_EPOCH * 128, 0);
}

DEVI int get_role(WS& r, const Params& p, LAS unsigned char* L) {
  if (threadIdx.x == 0) {
    const int xcc = __builtin_amdgcn_s_getreg(20 | (3 << 11)) & 7;
    GAS u64* ctl = (GAS u64*)(p.ws + CTRL);
    const u64 key = (__hip_atomic_load(ctl + L_EPOCH * 16, __ATOMIC_RELAXED, __HIP_MEMORY_SCOPE_AGENT) + 256) & ~255ull;
    int role = xcc * 32 + ((blockIdx.x >> 3) & 31);
    for (int i = 0; i < 256; ++i) {
      if (__hip_atomic_exchange(ctl + L_CLAIM * 16 + role * 4, key, __ATOMIC_RELAXED, __HIP_MEMORY_SCOPE_AGENT) != key) break;
      role = (role + 1) & 255;
    }
    *(LAS v4u*)(L + LDS_BYTES - 16) = v4u{(u32)role, (u32)key, (u32)(key >> 32), 0};
  }
  __syncthreads();
  const v4u v = *(LAS v4u*)(L + LDS_BYTES - 16);
  r.klo = uni(v.y); r.khi = uni(v.z);
  return uni(v.x);
}

__global__ void __launch_bounds__(512, 2) __attribute__((amdgpu_num_vgpr(124))) chain_kernel(Params p) {
  LAS unsigned char* L = (LAS unsigned char*)smem_g;
  WS r; r.rs = __builtin_amdgcn_make_buffer_rsrc(p.ws, 0, WS_USED, 0x00020000);
  const int role = get_role(r, p, L);
  const int x = (role >> 5) & 3, t = role & 31;
  const bool near = (role >= 128) == NEARH;
  int t2 = -1;
  const int sv = PLACE == 2 ? !(x & 1) : PLACE == 5 ? (x & 1) : ((x ^ (x >> 1)) & 1) == 0, xi = x >> 1;
  const int kind = !near ? (t == 7 ? 3 : 4) : sv ? (INTL ? t & 1 : t >> 4) : t < 18 ? 2 : 3;
  const int id = kind < 2 ? xi * 16 + (INTL ? t >> 1 : t & 15) : kind == 2 ? xi * 18 + t : near ? xi * 14 + t - 18 : 28 + x;
  if (kind == 0) role_S(p, r, L, id);
  else if (kind == 1) role_V(p, r, L, id);
  else if (kind == 2) role_R(p, r, L, id);
  else if (kind == 3) { role_X(p, r, L, id); t2 = near ? -1 : 32 + x; }
  else {
    if (t & 8) role_Y(p, r, L, x * 8 + (t & 7), t >> 4);
    else { role_T(p, r, t < 7 ? x * 7 + t : 28 + x * 8 + t - 16); }
    if (t >= 16) { role_F(p, r, L, (x & 1) * 16 + t - 16, x >> 1, (x >> 1) * 18, (x >> 1) * 18 + 18); t2 = t < 25 ? 36 + x * 9 + t - 16 : -1; }
    else if (t >= 8) t2 = x * 8 + t - 8;
    else if (x * 7 + t < 27) role_O(p, r, L, (x * 7 + t) % 9, (x * 7 + t) / 9, 3);
  }
  if (t2 >= 0) role_O2(p, r, L, (t2 % 36) >> 1, t2 & 1, t2 < 36 ? NSEG - 3 : NSEG - 4);
}

extern "C" void kernel_launch(void* const* d_in, const int* in_sizes, int n_in, void* d_out, int out_size, void* d_ws, size_t ws_size, hipStream_t stream) {
  Params p;
  p.x = (const u16*)d_in[0]; p.mem0 = (const u16*)d_in[1]; p.wq = (const u16*)d_in[2]; p.wk = (const u16*)d_in[3]; p.wv = (const u16*)d_in[4]; p.wo = (const u16*)d_in[5];
  p.w1 = (const u16*)d_in[8]; p.b1 = (const u16*)d_in[9]; p.w2 = (const u16*)d_in[10]; p.b2 = (const u16*)d_in[11]; p.gw = (const u16*)d_in[12];
  p.out = (u16*)d_out;
  p.ws = (unsigned char*)(((uintptr_t)d_ws + RING_BYTES - 1) & ~(uintptr_t)(RING_BYTES - 1));
  hipFuncSetAttribute((const void*)chain_kernel, hipFuncAttributeMaxDynamicSharedMemorySize, LDS_BYTES);
  chain_kernel<<<256, 512, LDS_BYTES, stream>>>(p);
}
```

```cpp
#include <hip/hip_runtime.h>
#include <hip/hip_bf16.h>
#include <stdint.h>
#include <string.h>
#pragma clang diagnostic ignored "-Winline-asm"

typedef unsigned u32;
typedef unsigned short u16;
typedef unsigned long long u64;
typedef u32 v2u __attribute__((ext_vector_type(2)));
typedef u32 v4u __attribute__((ext_vector_type(4)));
typedef u32 v8u __attribute__((ext_vector_type(8)));
typedef float f32x2 __attribute__((ext_vector_type(2)));
typedef float f32x4 __attribute__((ext_vector_type(4)));
typedef __bf16 bf16x2 __attribute__((ext_vector_type(2)));
typedef __bf16 bf16x8 __attribute__((ext_vector_type(8)));
typedef __bf16 bf16x16 __attribute__((ext_vector_type(16)));
typedef short s16x4 __attribute__((ext_vector_type(4)));
typedef short s16x8 __attribute__((ext_vector_type(8)));
typedef __amdgpu_buffer_rsrc_t rsrc_t;
#define LAS __attribute__((address_space(3)))
#define GAS __attribute__((address_space(1)))
#define DEVI __device__ __forceinline__

constexpr int DM = 576, NH = 32, NSEG = 64, FF = 4608, NTILE = 36;
constexpr u32 SENT = 0xFFFFFFFFu;
constexpr int SC1 = 16;
constexpr int CHUNK = 16384;
constexpr int SLOT = 42 * CHUNK;
constexpr int P_OFF = 36 * CHUNK, MEM_OFF = 40 * CHUNK;
constexpr int T_HS = 41 * CHUNK + 1024, T_HV = T_HS + SLOT;
constexpr int RING_BYTES = 2 << 20;
constexpr int CTRL = 3 * SLOT;
constexpr int L_EPOCH = 0, L_ABORT = 1, L_PAR = 2, L_RP = 80, L_CLAIM = 416, L_SP = 16, L_VP = 48, L_XP = 128, L_YP = 192, L_FP = 256, L_OP = 320, L_TP = 352, NTB = 60;
#define HSEG 8
#define CUT 20
constexpr int KQ_OFF = RING_BYTES + 65536;
constexpr int BZ_OFF = KQ_OFF + NSEG * NH * 16384;
constexpr int CZ_OFF = BZ_OFF + NSEG * NTILE * NH * 1024;
constexpr int PX_OFF = CZ_OFF + NSEG * NTILE * NH * 256;
constexpr int FP_OFF = PX_OFF + NSEG * NH * 2560;
constexpr int W1T_OFF = FP_OFF + 2048 * DM * 4;
constexpr int W2T_OFF = W1T_OFF + DM * FF * 2;
constexpr int G_OFF = W2T_OFF + DM * FF * 2;
constexpr int JUNK = G_OFF + 8192;
constexpr int DBG_OFF = JUNK + SLOT + CHUNK;
constexpr int WS_USED = DBG_OFF + (4 << 20);
constexpr int SPIN_CAP = 1 << 18;
constexpr int LDS_BYTES = 148 * 1024;

struct Params {
  const u16 *x, *mem0, *wq, *wk, *wv, *wo, *w1, *b1, *w2, *b2, *gw;
  u16* out;
  unsigned char* ws;
};

struct WS {
  rsrc_t rs; u32 klo, khi;
  DEVI operator rsrc_t() const { return rs; }
};
DEVI float bf2f(u32 b) { return __uint_as_float(b << 16); }
DEVI u32 pack2(float lo, float hi) { f32x2 v = {lo, hi}; bf16x2 b = __builtin_convertvector(v, bf16x2); return __builtin_bit_cast(u32, b); }
DEVI u32 guard(u32 v) { return v < 0xFFFFFFFEu ? v : 0xFFFFFFFEu; }
DEVI void bar() { asm volatile("s_waitcnt lgkmcnt(0)\n\ts_barrier" ::: "memory"); }
DEVI void drain() { asm volatile("s_waitcnt vmcnt(0)" ::: "memory"); }
DEVI f32x4 smf64(v4u a, v8u b, f32x4 c, int idx) { return __builtin_amdgcn_smfmac_f32_16x16x64_bf16(__builtin_bit_cast(bf16x8, a), __builtin_bit_cast(bf16x16, b), c, idx, 0, 0); }
DEVI f32x4 smf32(v2u a, v4u b, f32x4 c, int idx) { return __builtin_amdgcn_smfmac_f32_16x16x32_bf16(__builtin_bit_cast(s16x4, a), __builtin_bit_cast(s16x8, b), c, idx, 0, 0); }
DEVI f32x4 mfma(v4u a, v4u b, f32x4 c) { return __builtin_amdgcn_mfma_f32_16x16x32_bf16(__builtin_bit_cast(bf16x8, a), __builtin_bit_cast(bf16x8, b), c, 0, 0, 0); }
DEVI v8u cat(v4u a, v4u b) { return __builtin_shufflevector(a, b, 0, 1, 2, 3, 4, 5, 6, 7); }
DEVI v4u lo4(v8u a) { return __builtin_shufflevector(a, a, 0, 1, 2, 3); }
DEVI v4u hi4(v8u a) { return __builtin_shufflevector(a, a, 4, 5, 6, 7); }
DEVI float swapadd(float a, float b) {
  auto r = __builtin_amdgcn_permlane32_swap(__float_as_uint(a), __float_as_uint(b), false, false);
  return __uint_as_float(r[0]) + __uint_as_float(r[1]);
}
template <int CTRL_> DEVI float dpp(float v) { return __uint_as_float(__builtin_amdgcn_update_dpp(0, __float_as_uint(v), CTRL_, 0xF, 0xF, true)); }
DEVI float row16_max(float v) { v = fmaxf(v, dpp<0xB1>(v)); v = fmaxf(v, dpp<0x4E>(v)); v = fmaxf(v, dpp<0x141>(v)); v = fmaxf(v, dpp<0x140>(v)); return v; }
DEVI float row16_sum(float v) { v += dpp<0xB1>(v); v += dpp<0x4E>(v); v += dpp<0x141>(v); v += dpp<0x140>(v); return v; }
DEVI float x16_max(float v) { auto r = __builtin_amdgcn_permlane16_swap(__float_as_uint(v), __float_as_uint(v), false, false); return fmaxf(__uint_as_float(r[0]), __uint_as_float(r[1])); }
DEVI float x32_max(float v) { auto r = __builtin_amdgcn_permlane32_swap(__float_as_uint(v), __float_as_uint(v), false, false); return fmaxf(__uint_as_float(r[0]), __uint_as_float(r[1])); }
DEVI float x16_sum(float v) { auto r = __builtin_amdgcn_permlane16_swap(__float_as_uint(v), __float_as_uint(v), false, false); return __uint_as_float(r[0]) + __uint_as_float(r[1]); }
DEVI float x32_sum(float v) { auto r = __builtin_amdgcn_permlane32_swap(__float_as_uint(v), __float_as_uint(v), false, false); return __uint_as_float(r[0]) + __uint_as_float(r[1]); }
DEVI v4u ld128(rsrc_t r, int vo, int so) { return __builtin_amdgcn_raw_buffer_load_b128(r, vo, so, SC1); }
DEVI v2u ld64(rsrc_t r, int vo, int so) { return __builtin_amdgcn_raw_buffer_load_b64(r, vo, so, SC1); }
DEVI u32 ld32(rsrc_t r, int vo, int so) { return __builtin_amdgcn_raw_buffer_load_b32(r, vo, so, SC1); }
DEVI void st128(v4u v, rsrc_t r, int vo, int so) { __builtin_amdgcn_raw_buffer_store_b128(v, r, vo + so, 0, SC1); }
DEVI void st64(v2u v, rsrc_t r, int vo, int so) { __builtin_amdgcn_raw_buffer_store_b64(v, r, vo, so, SC1); }
DEVI void st32(u32 v, rsrc_t r, int vo, int so) { __builtin_amdgcn_raw_buffer_store_b32(v, r, vo, so, SC1); }
DEVI rsrc_t mk_rsrc(const void* ptr, int bytes) { return __builtin_amdgcn_make_buffer_rsrc((void*)ptr, 0, bytes, 0x00020000); }
DEVI v4u pl128(rsrc_t r, int vo, int so) { return __builtin_amdgcn_raw_buffer_load_b128(r, vo, so, 0); }
DEVI u32 pl32(rsrc_t r, int vo, int so) { return __builtin_amdgcn_raw_buffer_load_b32(r, vo, so, 0); }
DEVI u32 umax(u32 a, u32 b) { return a > b ? a : b; }
DEVI u32 mx4(v4u v) { return umax(umax(v.x, v.y), umax(v.z, v.w)); }
DEVI int uni(int v) { return __builtin_amdgcn_readfirstlane(v); }
DEVI int opaque(int v) { asm volatile("" : "+v"(v)); return v; }
DEVI int sopaque(int v) { asm volatile("" : "+s"(v)); return v; }
DEVI void fence_c() { asm volatile("" ::: "memory"); }

#define XSLP 4
#define PSLP 23
#define RSLP 26
#define XUN 4
#define TDLY 700
#define MAR_C 8
#define VBG 6
#define VPRIO 1
#define FWIN 9
#define FPRIO 1
#define FPLAIN 0
#define VSLP 8
#define PLACE 2
#define WAUX 0
#define TAUX 2
#define INTL 0
#define NEARH 1
#define WARMC 0
#define CODE_BYTES 88064
#define MAR_R 8
#define NOUNROLL _Pragma("clang loop unroll(disable)")
#define SB() __builtin_amdgcn_sched_barrier(0)
constexpr int OOB = 0x7FFFFFF0;
DEVI bool any_sent(u32 m) { return __builtin_amdgcn_ballot_w64(m == SENT) != 0; }
DEVI void set_flag(const WS& r, int vo, int v) { st64(v2u{r.klo + (u32)(v + 2), r.khi}, r, vo, 0); }
DEVI int get_flag(const WS& r, int vo) {
  const v2u v = ld64(r, vo, 0);
  return (v.y == r.khi && v.x - r.klo < 256u) ? (int)(v.x - r.klo) - 2 : -2;
}
DEVI bool give_up(const WS& r, int& rounds) {
  if (++rounds > SPIN_CAP / 256) { set_flag(r, CTRL + L_ABORT * 128, 0); return true; }
  if (uni(get_flag(r, CTRL + L_ABORT * 128)) >= 0) { rounds = SPIN_CAP; return true; }
  return false;
}
#define POLL(BODY)                                                                                   \
  NOUNROLL for (int cnt_ = 1;; ++cnt_) {                                                             \
    bool bad;                                                                                        \
    fence_c();                                                                                       \
    BODY;                                                                                            \
    if (__builtin_expect(!bad, 1)) break;                                                            \
    if (((cnt_ & 255) == 0 || rounds >= SPIN_CAP / 256) && give_up(r, rounds)) break;                \
  }                                                                                                  \
  fence_c();
DEVI u32 rt7() {
  u64 t;
  asm volatile("s_memrealtime %0\n\ts_waitcnt lgkmcnt(0)" : "=s"(t)::"memory");
  return (u32)t << 7;
}
#define HC1 "v_and_b32 %0, 127, v248\n\tv_cmp_ne_u32 vcc, %5, %0\n\ts_cbranch_vccz 2f\n\t"
#define HC4 HC1 HC1 HC1 HC1
#define HCAD HC4 HC4 HC4 HC4
DEVI u32 hint_wait(rsrc_t r, int vo, int so, int step) {
  int cnt = step < 0 ? 0 : 1024;
  u32 hv;
  asm volatile(
      "v_mov_b32 v248, -1\n\ts_nop 3\n\t"
      "1:\n\t"
      "buffer_load_dword v248, %2, %3, %4 offen sc1\n\t"
      HCAD
      "s_sub_u32 %1, %1, 1\n\t"
      "s_cbranch_scc0 1b\n\t"
      "2:\n\t"
      "v_mov_b32 %0, v248"
      : "=&v"(hv), "+s"(cnt)
      : "v"(vo), "s"(r), "s"(so), "s"(step)
      : "memory", "vcc", "scc", "v248");
  return hv;
}
template <int HI>
DEVI void time_wait(u32 tv) {
  int cnt = 64;
  u32 t;
  u64 now;
  asm volatile(
      "1:\n\t"
      "s_memrealtime s[96:97]\n\t"
      "s_sub_u32 %1, %1, 1\n\t"
      "s_cbranch_scc1 2f\n\t"
      "s_waitcnt lgkmcnt(0)\n\t"
      "s_lshl_b32 s96, s96, 7\n\t"
      "v_sub_u32 %0, s96, %3\n\t"
      "v_cmp_gt_i32 s[96:97], 0, %0\n\t"
      "s_cmp_lg_u32 s%c4, 0\n\t"
      "s_cbranch_scc1 1b\n\t"
      "2:\n\t"
      "s_waitcnt lgkmcnt(0)"
      : "=&v"(t), "+s"(cnt), "=&{s[96:97]}"(now)
      : "v"(tv), "i"(96 + HI)
      : "memory", "scc");
}
DEVI int row_max_i(int d) {
  d = max(d, __builtin_amdgcn_update_dpp(d, d, 0xB1, 0xF, 0xF, false)); d = max(d, __builtin_amdgcn_update_dpp(d, d, 0x4E, 0xF, 0xF, false));
  d = max(d, __builtin_amdgcn_update_dpp(d, d, 0x141, 0xF, 0xF, false));
  return d;
}
DEVI int track(int dur, int m) { return (m > 0 && m < (200 << 7)) ? dur + ((m - dur) >> 2) : dur; }
#define DCHK "v_max3_u32 %0, v248, v249, v250\n\tv_max3_u32 %1, v252, v253, v254\n\tv_max3_u32 %0, %0, v251, v255\n\tv_max_u32 %0, %0, %1\n\tv_cmp_eq_u32 vcc, -1, %0\n\t"
#define DC1 DCHK "s_cbranch_vccz 2f\n\t"
#define DC2 DC1 DC1
#define DCAD DC2 DC2 DC2
template <int TWO>
DEVI bool data_wait(rsrc_t r, int vo, int so, int la) {
  int cnt = 4096;
  u32 t0, t1;
  asm volatile(
      "s_waitcnt vmcnt(0)\n\t"
      "v_mov_b32 v248, -1\n\tv_mov_b32 v249, -1\n\tv_mov_b32 v250, -1\n\tv_mov_b32 v251, -1\n\t"
      "v_mov_b32 v252, %6\n\tv_mov_b32 v253, %6\n\tv_mov_b32 v254, %6\n\tv_mov_b32 v255, %6\n\t"
      "1:\n\t"
      "buffer_load_dwordx4 v[248:251], %3, %4, %5 offen sc1\n\t"
      ".if %6\n\t"
      "buffer_load_dwordx4 v[252:255], %3, %4, %5 offen offset:1024 sc1\n\t"
      ".endif\n\t"
      "s_sub_u32 %2, %2, 1\n\t"
      "s_cbranch_scc1 2f\n\t"
      DCAD
      DCHK "s_cbranch_vccnz 1b\n\t"
      "2:\n\t"
      "ds_write_b128 %7, v[248:251]\n\t"
      ".if %6\n\t"
      "ds_write_b128 %7, v[252:255] offset:1024\n\t"
      ".endif"
      : "=&v"(t0), "=&v"(t1), "+s"(cnt)
      : "v"(vo), "s"(r), "s"(so), "i"(-TWO), "v"(la)
      : "memory", "vcc", "scc", "v248", "v249", "v250", "v251", "v252", "v253", "v254", "v255");
  return cnt >= 0;
}
DEVI void wait_prog(const WS& r, int line0, int n, int want, int lane, int& seen, int& rounds) {
  if (__builtin_amdgcn_ballot_w64(seen < want) != 0) {
    const int vo = CTRL + (line0 + (lane < n ? lane : 0)) * 128;
    POLL(seen = get_flag(r, vo); bad = __builtin_amdgcn_ballot_w64(seen < want) != 0; if (bad) __builtin_amdgcn_s_sleep(2));
  }
}

DEVI void wait_prog2(const WS& r, int line0, int n, int want, int lane, int& seen0, int& seen1, int& rounds) {
  const bool hp = (want & 1) && want < HSEG;
  int sn = hp ? seen1 : seen0;
  wait_prog(r, line0 + (hp ? 32 : 0), n, want, lane, sn, rounds);
  if (hp) seen1 = sn; else seen0 = sn;
}
extern __shared__ __attribute__((aligned(16))) unsigned char smem_g[];

template <int WO>
struct WLoad {
  rsrc_t r1, r2; int va, vb;
  DEVI void init(const u16* W1, const u16* W2, int h) {
    r1 = mk_rsrc(W1, DM * 4096 * 2); r2 = mk_rsrc(W2, DM * 4096 * 2);
    const int tid = threadIdx.x;
    va = ((tid >> 4) * 4096 + h * 128) * 2 + (tid & 15) * 16; vb = h * 128 * DM * 2 + tid * 16;
  }
  template <int M, int RD> DEVI void req(v4u (&st)[9]) {
#pragma unroll
    for (int k = 0; k < 9; ++k) st[k] = __builtin_amdgcn_raw_buffer_load_b128(M ? r2 : r1, (M && WO) ? vb : va, (9 * RD + k) * ((M && WO) ? 8192 : 32 * 8192), WAUX);
    SB();
  }
  template <int M> DEVI void put(LAS unsigned char* L, const v4u (&st)[9]) {
    const int tid = threadIdx.x;
#pragma unroll
    for (int k = 0; k < 9; ++k) {
      const int sw = (M && WO) ? (((k * 512 + tid) / 72) >> 3) & 1 : (tid >> 7) & 1;
      *(LAS v4u*)(L + M * 73728 + k * 8192 + ((tid * 16) ^ (sw << 6))) = st[k];
    }
    SB();
  }
};
DEVI u32 lo2(u32 a, u32 b) { return __builtin_amdgcn_perm(b, a, 0x05040100u); }
DEVI u32 hi2(u32 a, u32 b) { return __builtin_amdgcn_perm(b, a, 0x07060302u); }
template <int RD>
DEVI void get_w1_sparse(LAS unsigned char* M, int wq, int lane, v8u (&wt)[2][9]) {
  const int n = lane & 15, gb = lane >> 4;
  LAS unsigned char* b = M + ((gb >> 1) + 8 * (gb & 1)) * 256 + ((wq * 64) ^ ((gb & 1) << 6)) + n * 4;
#pragma unroll
  for (int i = 0; i < 36; ++i) {
    const int hs = 9 * RD + (i >> 2), d = 32 * (i >> 2) + 16 * (i & 1) + 4 * ((i >> 1) & 1);
    const u32 x = *(LAS u32*)(b + d * 256), y = *(LAS u32*)(b + (d + 2) * 256);
    wt[0][hs >> 1][4 * (hs & 1) + (i & 3)] = lo2(x, y);
    wt[1][hs >> 1][4 * (hs & 1) + (i & 3)] = hi2(x, y);
  }
}
template <int RD>
DEVI void get_w1_dense(LAS unsigned char* M, int wq, int lane, v4u (&wt)[2][18]) {
  const int n = lane & 15, gb = lane >> 4;
  LAS unsigned char* b = M + 8 * gb * 256 + ((wq * 64) ^ ((gb & 1) << 6)) + n * 4;
#pragma unroll
  for (int i = 0; i < 36; ++i) {
    const int d = 32 * (i >> 2) + 2 * (i & 3);
    const u32 x = *(LAS u32*)(b + d * 256), y = *(LAS u32*)(b + (d + 1) * 256);
    wt[0][9 * RD + (i >> 2)][i & 3] = lo2(x, y);
    wt[1][9 * RD + (i >> 2)][i & 3] = hi2(x, y);
  }
}
DEVI int tile_of(int c, int i) { return i < 8 ? 9 * c + (c & 1) + i : (c & 1 ? 9 * c : 9 * c + 8); }
template <int RD>
DEVI void get_w2(LAS unsigned char* M, int c, int lane, v8u (&wt)[9][2]) {
  const int n = lane & 15, gb = lane >> 4;
  const u32 sel = (c & 1) ? 0x07060302u : 0x05040100u;
  LAS unsigned char* b = M + 8 * gb * 1152 + n * 4;
#pragma unroll
  for (int pr = 0; pr < 5; ++pr) {
    LAS unsigned char* bp = b + ((64 * (tile_of(c, 2 * pr) >> 1)) ^ ((gb & 1) << 6));
#pragma unroll
    for (int q = 0; q < 8; ++q) {
      const int e = 32 * (q >> 2) + 2 * (q & 3);
      const u32 x = *(LAS u32*)(bp + e * 1152), y = *(LAS u32*)(bp + (e + 1) * 1152);
      if (pr < 4) { wt[2 * pr][RD][q] = lo2(x, y); wt[2 * pr + 1][RD][q] = hi2(x, y); }
      else wt[8][RD][q] = __builtin_amdgcn_perm(y, x, sel);
    }
  }
}
#define LOAD_W(WO, W1, W2, GET, MINE, IDX, LAST)                                                       \
  {                                                                                              \
    WLoad<WO> wl; wl.init(W1, W2, h);                                                            \
    v4u sa[9], sb_[9], sc[9];                                                                    \
    wl.req<0, 0>(sa); wl.req<1, 0>(sb_);                                                         \
    wl.put<0>(L, sa); wl.req<0, 1>(sc); bar();                                                   \
    wl.put<1>(L, sb_); wl.req<1, 1>(sa);                                                         \
    if (MINE == 0) GET<0>(L, IDX, lane, wt);                                                     \
    bar();                                                                                       \
    wl.put<0>(L, sc);                                                                            \
    if (MINE == 1) GET<0>(L + 73728, IDX, lane, wt);                                             \
    bar();                                                                                       \
    wl.put<1>(L, sa); drain();                                                                   \
    if (MINE == 0) GET<1>(L, IDX, lane, wt);                                                     \
    bar();                                                                                       \
    if (MINE == 1) GET<1>(L + 73728, IDX, lane, wt);                                             \
    LAST;                                                                                        \
    bar();                                                                                       \
  }

constexpr int AIMG = 1088, QIMG = 2 * AIMG, KIMG = 2304, IMG = QIMG + KIMG;
constexpr int LM_IMG = 0, LM_MEM = 5120, LM_RED = 0, LM_T = 14336, LM_KQ = 16384, LM_BZ = 4096;

DEVI int wait_parity(const WS& r, int& rounds) {
  int v;
  POLL(v = uni(get_flag(r, CTRL + L_PAR * 128)); bad = v < 0; if (bad) __builtin_amdgcn_s_sleep(4));
  return (v & 1) * 8192;
}
DEVI int wait_mem0(const WS& r, int lane, int& rounds) {
  int seen = -2;
  wait_prog(r, L_RP, NTILE, 0, lane, seen, rounds);
  return (uni(seen) & 1) * 8192;
}
DEVI int next_slot(int sb, int par) { return sb + SLOT >= par + 3 * SLOT ? par : sb + SLOT; }

DEVI f32x4 zero4() { f32x4 z = {0, 0, 0, 0}; asm volatile("" : "+v"(z)); return z; }
constexpr float SCL = 0.08838834764831845f * 1.4426950408889634f;
DEVI float mulf(float a, float b) { float o = a * b; asm("" : "+v"(o)); return o; }
DEVI f32x2 fold(f32x4 a) { float x = a[0] + a[2]; asm("" : "+v"(x)); float y = a[1] + a[3]; asm("" : "+v"(y)); return f32x2{x, y}; }
template <int TWO>
struct Gather {
  DEVI void run(const WS& r, LAS unsigned char* L, int l16, int c0, int sb, int& rounds) {
    const int so = sb + MEM_OFF + (TWO ? c0 * 1024 : CHUNK), la = (int)(size_t)(L + LM_MEM + c0 * 1024 + l16);
    if (__builtin_expect(!data_wait<TWO>(r, l16, so, la), 0)) {
      NOUNROLL for (;;) {
        rounds += 15;
        if (give_up(r, rounds) || data_wait<TWO>(r, l16, so, la)) break;
      }
    }
  }
};
template <int RSTEP, int SCALE, class F>
DEVI void stage1(LAS unsigned char* L, const v8u (&wt)[2][9], f32x4 a0, f32x4 a1, int l16, int idx, LAS unsigned char* wp, F mid) {
  v4u af[9];
#pragma unroll
  for (int ks = 0; ks < 9; ++ks) af[ks] = *(LAS v4u*)(L + LM_MEM + ks * 1024 + l16);
  SB();
#pragma unroll
  for (int ks = 0; ks < 9; ++ks) {
    a0 = smf64(af[ks], wt[0][ks], a0, idx); a1 = smf64(af[ks], wt[1][ks], a1, idx);
    if (ks == 5) { SB(); mid(); SB(); }
  }
  f32x2 p0 = fold(a0), p1 = fold(a1);
  if (SCALE) { p0.x = mulf(p0.x, SCL); p0.y = mulf(p0.y, SCL); p1.x = mulf(p1.x, SCL); p1.y = mulf(p1.y, SCL); }
  *(LAS u32*)wp = pack2(p0.x, p1.x);
  *(LAS u32*)(wp + RSTEP) = pack2(p0.y, p1.y);
}
DEVI int aimg_w(int wq, int lane) { const int n = lane & 15, g = lane >> 4; return (wq >> 1) * AIMG + (2 * (wq & 1) + (n >> 3)) * 272 + g * 64 + (n & 1) * 32 + ((n & 7) >> 1) * 4; }
DEVI int aimg_r(int lane) { return (lane >> 4) * 272 + (lane & 15) * 16; }
DEVI int kimg_w(int wk, int lane) { const int n = lane & 15, g = lane >> 4; return (wk >> 1) * 1152 + (n >> 2) * 288 + g * 64 + (wk & 1) * 16 + (n & 3) * 4; }
DEVI int kimg_r(int k, int g, int key) { return (k >> 1) * 1152 + g * 288 + key * 32 + (k & 1) * 16; }
DEVI float row16_max_asm(float v) {
  asm volatile("s_nop 1\n\tv_max_f32_dpp %0, %0, %0 quad_perm:[1,0,3,2] row_mask:0xf bank_mask:0xf\n\t"
               "s_nop 1\n\tv_max_f32_dpp %0, %0, %0 quad_perm:[2,3,0,1] row_mask:0xf bank_mask:0xf\n\t"
               "s_nop 1\n\tv_max_f32_dpp %0, %0, %0 row_half_mirror row_mask:0xf bank_mask:0xf\n\t"
               "s_nop 1\n\tv_max_f32_dpp %0, %0, %0 row_mirror row_mask:0xf bank_mask:0xf"
               : "+v"(v));
  return v;
}

DEVI void role_S(const Params& p, const WS& r, LAS unsigned char* L, int h) {
  const int lane = threadIdx.x & 63, w = uni(threadIdx.x >> 6), n = lane & 15, g = lane >> 4;
  const int idx = (lane & 2) ? 0xEEEE : 0x4444, l16 = opaque(lane * 16);
  int rounds = 0;
  if (w < 6) st128(v4u{SENT, SENT, SENT, SENT}, r, P_OFF + (h >> 3) * CHUNK + (h & 7) * 1024 + l16, (w & 1) * 8192 + (w >> 1) * SLOT);
  else st32(SENT, r, lane == 0 ? T_HS + h * 128 : OOB, (w & 1) * 8192);
  v8u wt[2][9];
  if (w < 4) LOAD_W(0, p.wq, p.wk, get_w1_sparse, 0, w, )
  else LOAD_W(0, p.wq, p.wk, get_w1_sparse, 1, w - 4, )
  if (w == 6 && lane == 0) set_flag(r, CTRL + (L_SP + h) * 128, -1);
  const int par = wait_mem0(r, lane, rounds);
  int sb = par;
  if (w < 2) {
    LAS unsigned char* wa = L + LM_IMG + aimg_w(w, lane);
    const int pvo = P_OFF + (h >> 3) * CHUNK + (h & 7) * 1024 + w * 512;
    const int pxo = pvo + (n >> 3) * 128 + (g >> 1) * 64 + ((n >> 2) & 1) * 32 + (g & 1) * 16 + (n & 3) * 4;
    const int pmo = n < 4 ? pvo + 256 + (g >> 1) * 32 + (n >> 1) * 16 + (g & 1) * 8 + (n & 1) * 4 : OOB;
    const int cn = (n & 8) | ((n & 3) << 1) | ((n >> 2) & 1);
    const int kmc = ((n & 1) << 2) | (n & 2) | ((n >> 2) & 1);
    const float mk = n < 8 ? 1.f : 0.f;
    int sprev = par + 2 * SLOT;
    for (int s = sopaque(-1); s < NSEG; ++s) {
      const f32x4 z0 = zero4(), z1 = zero4();
      f32x4 sa = zero4(), sbb = zero4(), sm = zero4();
      LAS unsigned char* kq = L + LM_KQ + (s & 1) * 16384;
      v4u kf[2][4];
      const int so = s < 0 ? JUNK : sb;
      Gather<1> ga;
      ga.run(r, L, l16, 2 * w, sb, rounds);
      bar();
      stage1<16, 1>(L, wt, z0, z1, l16, idx, wa, [&]() {
#pragma unroll
        for (int k = 0; k < 8; ++k) kf[k >> 2][k & 3] = *(LAS v4u*)(kq + k * 1024 + (g * 16 + cn) * 16);
      });
      bar();
      v4u qa[2], km[4];
#pragma unroll
      for (int k = 0; k < 2; ++k) qa[k] = *(LAS v4u*)(L + LM_IMG + k * AIMG + aimg_r(lane ^ w));
#pragma unroll
      for (int k = 0; k < 4; ++k) km[k] = *(LAS v4u*)(L + LM_IMG + QIMG + kimg_r(k, g, kmc));
      SB();
      sa = smf64(qa[0], cat(kf[0][0], kf[0][1]), sa, idx);
      sbb = smf64(qa[0], cat(kf[1][0], kf[1][1]), sbb, idx);
      sa = smf64(qa[1], cat(kf[0][2], kf[0][3]), sa, idx);
      sm = smf64(qa[0], cat(km[0], km[1]), sm, idx);
      sbb = smf64(qa[1], cat(kf[1][2], kf[1][3]), sbb, idx);
      sm = smf64(qa[1], cat(km[2], km[3]), sm, idx);
      SB();
      const float va = sa[0] + sa[2], vb = sbb[0] + sbb[2], vm = sm[0] + sm[2];
      const float mx = row16_max_asm(fmaxf(fmaxf(va, vb), vm));
      const float ea = __builtin_amdgcn_exp2f(va - mx), eb = __builtin_amdgcn_exp2f(vb - mx), em = __builtin_amdgcn_exp2f(vm - mx);
      const float em2 = dpp<0x104>(em);
      const float inv = __builtin_amdgcn_rcpf(row16_sum(__builtin_fmaf(em, mk, ea + eb)));
      st32(guard(pack2(ea * inv, eb * inv)), r, pxo, so);
      st32(guard(pack2(em * inv, em2 * inv)), r, pmo, so);
      SB();
      *(LAS u32*)(L + LM_T + w * 4) = rt7();

      st32(SENT, r, pxo, sprev); st32(SENT, r, pmo, sprev);
      if (s >= 0) __builtin_amdgcn_s_sleep(PSLP - 8);
      sprev = s < 0 ? sprev : sb;
      sb = s < 0 ? sb : next_slot(sb, par);
    }
  } else if (w < 4) {
    LAS unsigned char* wa = L + LM_IMG + aimg_w(w, lane);
    const int m = w - 2, qi = 2 * n + m;
    const int po = (h * 2 + m) * 1280;
    for (int s = sopaque(-1); s < NSEG; ++s) {
      const f32x4 z0 = zero4(), z1 = zero4();
      bar();
      stage1<16, 1>(L, wt, z0, z1, l16, idx, wa, []() {});
      drain();
      bar();
      if (s < NSEG - 1) __builtin_amdgcn_s_sleep(XSLP);
      LAS unsigned char* kq = L + LM_KQ + (s & 1) * 16384 + l16;
      f32x4 xa = {0, 0, 0, 0}, xb = {0, 0, 0, 0}, xm = {0, 0, 0, 0};
#pragma unroll
      for (int k = 0; k < 4; ++k) {
        const v4u qf = *(LAS v4u*)(kq + 8192 + (m * 4 + k) * 1024);
        xa = mfma(*(LAS v4u*)(kq + k * 1024), qf, xa);
        xb = mfma(*(LAS v4u*)(kq + (4 + k) * 1024), qf, xb);
        xm = mfma(*(LAS v4u*)(L + LM_IMG + QIMG + kimg_r(k, g, n & 7)), qf, xm);
      }
      float mx = fmaxf(fmaxf(xm[0], xm[1]), fmaxf(xm[2], xm[3]));
#pragma unroll
      for (int k = 0; k < 4; ++k) {
        xa[k] = (8 * g + 2 * k <= qi) ? xa[k] : -INFINITY;
        xb[k] = (8 * g + 2 * k + 1 <= qi) ? xb[k] : -INFINITY;
        mx = fmaxf(mx, fmaxf(xa[k], xb[k]));
      }
      mx = x32_max(x16_max(mx));
      float sum = 0.f, summ = 0.f;
#pragma unroll
      for (int k = 0; k < 4; ++k) {
        xa[k] = __builtin_amdgcn_exp2f((xa[k] - mx) * SCL); xb[k] = __builtin_amdgcn_exp2f((xb[k] - mx) * SCL); xm[k] = __builtin_amdgcn_exp2f((xm[k] - mx) * SCL);
        sum += xa[k] + xb[k]; summ += xm[k];
      }
      sum += g < 2 ? summ : 0.f;
      const float inv = __builtin_amdgcn_rcpf(x32_sum(x16_sum(sum)));
      v4u px; v2u pm2;
#pragma unroll
      for (int k = 0; k < 4; ++k) px[k] = pack2(xa[k] * inv, xb[k] * inv);
      pm2.x = pack2(xm[0] * inv, xm[1] * inv); pm2.y = pack2(xm[2] * inv, xm[3] * inv);
      const int so = s < 0 ? JUNK : PX_OFF + s * NH * 2560;
      st128(px, r, po + l16, so);
      st64(pm2, r, g < 2 ? po + 1024 + n * 16 + g * 8 : OOB, so);
    }
    drain();
  } else if (w < 6) {
    LAS unsigned char* wa = L + LM_IMG + QIMG + kimg_w(w - 4, lane);
    for (int s = sopaque(-1); s < NSEG; ++s) {
      const f32x4 z0 = zero4(), z1 = zero4();
      Gather<1> ga;
      ga.run(r, L, l16, 2 * w - 4, sb, rounds);
      bar();
      stage1<32, 0>(L, wt, z0, z1, l16, idx, wa, []() {});
      bar();
      if (s >= 0) __builtin_amdgcn_s_sleep(PSLP);
      sb = s < 0 ? sb : next_slot(sb, par);
    }
  } else if (w == 6) {
    LAS unsigned char* wa = L + LM_IMG + QIMG + kimg_w(2, lane);
    for (int s = sopaque(-1); s < NSEG; ++s) {
      const f32x4 z0 = zero4(), z1 = zero4();
      Gather<0> ga;
      ga.run(r, L, l16, 8, sb, rounds);
      bar();
      stage1<32, 0>(L, wt, z0, z1, l16, idx, wa, []() {});
      bar();
      if (s >= 0) __builtin_amdgcn_s_sleep(PSLP);
      if (s > 0 && lane == 0) set_flag(r, CTRL + (L_SP + h) * 128, s - 1);
      sb = s < 0 ? sb : next_slot(sb, par);
    }
  } else {
    LAS unsigned char* wa = L + LM_IMG + QIMG + kimg_w(3, lane);
    int seen0 = -2, dur = 60 << 7;
    auto fetch = [&](int s) {
      wait_prog(r, L_XP + h, 1, s, lane, seen0, rounds);
      v4u t[16];
#pragma unroll
      for (int k = 0; k < 16; ++k) t[k] = ld128(r, l16 + (k & 3) * 1024, KQ_OFF + (s * NH + h) * 16384 + (k >> 2) * 4096);
#pragma unroll
      for (int k = 0; k < 16; ++k) *(LAS v4u*)(L + LM_KQ + (s & 1) * 16384 + l16 + k * 1024) = t[k];
    };
    const int ho = lane == 0 ? T_HS + h * 128 : OOB;
    { const u32 t = ld32(r, KQ_OFF + (3 * NH + h) * 16384 + l16, 0); asm volatile("" ::"v"(t)); }
    for (int s = sopaque(-1); s < NSEG; ++s) {
      const f32x4 z0 = zero4(), z1 = zero4();
      bar();
      const u32 tb = rt7();
      st32(((tb + dur) & ~127u) | (s & 127), r, ho, par);
      stage1<32, 0>(L, wt, z0, z1, l16, idx, wa, []() {});
      bar();
      if (s + 1 < NSEG) fetch(s + 1);
      if (s >= 0) dur = track(dur, max((int)(uni(*(LAS u32*)(L + LM_T)) - tb), (int)(uni(*(LAS u32*)(L + LM_T + 4)) - tb)));
    }
  }
  bar();
  if (w == 6 && lane == 0) set_flag(r, CTRL + (L_SP + h) * 128, NSEG - 1);
}

template <int RD>
DEVI void get_v1(LAS unsigned char* M, int w, int lane, v8u (&w1)[9]) {
  const int n = lane & 15, gb = lane >> 4;
  const u32 sel = (w & 1) ? 0x07060302u : 0x05040100u;
  LAS unsigned char* b = M + ((gb >> 1) + 8 * (gb & 1)) * 256 + (((w >> 1) * 64) ^ ((gb & 1) << 6)) + n * 4;
#pragma unroll
  for (int i = 0; i < 36; ++i) {
    const int hs = 9 * RD + (i >> 2), d = 32 * (i >> 2) + 16 * (i & 1) + 4 * ((i >> 1) & 1);
    w1[hs >> 1][4 * (hs & 1) + (i & 3)] = __builtin_amdgcn_perm(*(LAS u32*)(b + (d + 2) * 256), *(LAS u32*)(b + d * 256), sel);
  }
}
template <int RD>
DEVI void get_v2(LAS unsigned char* M, int w, int lane, v8u (&w2)[5][2]) {
  const int n = lane & 15, gb = lane >> 4;
  const u32 sel = (w & 1) ? 0x07060302u : 0x05040100u;
  LAS unsigned char* b = M + 8 * gb * 1152 + n * 4;
#pragma unroll
  for (int pr = 0; pr < 3; ++pr) {
    LAS unsigned char* bp = b + ((64 * (pr < 2 ? 2 * w + pr : 16 + ((w >> 1) & 1))) ^ ((gb & 1) << 6));
#pragma unroll
    for (int q = 0; q < 8; ++q) {
      const int e = 32 * (q >> 2) + 2 * (q & 3);
      const u32 x = *(LAS u32*)(bp + e * 1152), y = *(LAS u32*)(bp + (e + 1) * 1152);
      if (pr < 2) { w2[2 * pr][RD][q] = lo2(x, y); w2[2 * pr + 1][RD][q] = hi2(x, y); }
      else w2[4][RD][q] = __builtin_amdgcn_perm(y, x, sel);
    }
  }
}
template <int GATHER, int FIVE, int HINT>
DEVI void v_loop(const Params& p, const WS& r, LAS unsigned char* L, int h, int w, const v8u (&w1)[9], const v8u (&w2)[5][2]) {
  const int lane = threadIdx.x & 63, n = lane & 15, g = lane >> 4;
  const int idx = (lane & 2) ? 0xEEEE : 0x4444, l16 = opaque(lane * 16);
  int rounds = 0;
  const int par = wait_mem0(r, lane, rounds);
  LAS unsigned char* wa = L + LM_IMG + aimg_w(w >> 1, lane) + (w & 1) * 2;
  LAS unsigned char* img = L + LM_IMG + aimg_r(lane);
  const int lr0 = (4 * w + g) * CHUNK + h * 256 + n * 16, lr1 = g == 0 ? (28 + w) * CHUNK + h * 256 + n * 16 : OOB;
  const int lz0 = (4 * w + g) * 8192 + h * 256 + n * 16, lz1 = g == 0 ? (28 + w) * 8192 + h * 256 + n * 16 : OOB;
  const int ho = (w == 7 && lane == 0) ? T_HV + h * 128 : OOB;
  int sb = par, sprev = par + 2 * SLOT, dur = 50 << 7;
  u32 tb = 0;
  for (int s = sopaque(-1); s < NSEG; ++s) {
    f32x4 a1 = zero4(), a[5];
#pragma unroll
    for (int i = 0; i < 4 + FIVE; ++i) a[i] = zero4();
    const int so0 = opaque(lr0 + (s < 0 ? JUNK : sb)), so1 = opaque(lr1 + (s < 0 ? JUNK : sb));
    Gather<GATHER == 2> ga;
    if (GATHER) ga.run(r, L, l16, GATHER == 2 ? 2 * w : 8, sb, rounds);
    else drain();
    if (HINT && s > 0) {
      const int m = row_max_i((int)(*(LAS u32*)(L + LM_T + (lane & 7) * 4) - tb));
      dur = track(dur, uni(m));
    }
    bar();
    if (HINT) tb = rt7();
    v4u af[9];
#pragma unroll
    for (int ks = 0; ks < 9; ++ks) af[ks] = *(LAS v4u*)(L + LM_MEM + ks * 1024 + l16);
    SB();
    if (HINT) { st32(((tb + dur) & ~127u) | (s & 127), r, ho, par); SB(); }
#pragma unroll
    for (int ks = 0; ks < 9; ++ks) a1 = smf64(af[ks], w1[ks], a1, idx);
    const f32x2 f1 = fold(a1);
    const u32 v2 = pack2(f1.x, f1.y);
    *(LAS u16*)wa = (u16)v2;
    *(LAS u16*)(wa + 16) = (u16)(v2 >> 16);
    bar();
    v4u va[2];
#pragma unroll
    for (int k = 0; k < 2; ++k) va[k] = *(LAS v4u*)(img + k * AIMG);
    auto fin = [&](int i) { const f32x2 f = fold(a[i]); return guard(pack2(f.x, f.y)); };
    auto xch = [&](u32 u0, u32 u1, u32 u2, u32 u3) {
      const auto e0 = __builtin_amdgcn_permlane32_swap(u0, u2, false, false);
      const auto e1 = __builtin_amdgcn_permlane32_swap(u1, u3, false, false);
      const auto f0 = __builtin_amdgcn_permlane16_swap(e0[0], e1[0], false, false);
      const auto f1 = __builtin_amdgcn_permlane16_swap(e0[1], e1[1], false, false);
      return v4u{f0[0], f0[1], f1[0], f1[1]};
    };
    v4u o1;
    if (FIVE) a[4] = smf64(va[0], w2[4][0], a[4], idx);
    a[0] = smf64(va[0], w2[0][0], a[0], idx);
    if (FIVE) a[4] = smf64(va[1], w2[4][1], a[4], idx);
    else a[2] = smf64(va[0], w2[2][0], a[2], idx);
    a[0] = smf64(va[1], w2[0][1], a[0], idx);
    if (FIVE) a[2] = smf64(va[0], w2[2][0], a[2], idx);
    a[1] = smf64(va[0], w2[1][0], a[1], idx);
    SB();
    u32 u4;
    if (FIVE) u4 = fin(4);
    a[2] = smf64(va[1], w2[2][1], a[2], idx);
    SB();
    if (FIVE) { o1 = xch(u4, u4, u4, u4); st128(o1, r, so1, 0); }
    a[3] = smf64(va[0], w2[3][0], a[3], idx);
    a[1] = smf64(va[1], w2[1][1], a[1], idx);
    SB();
    const u32 u0 = fin(0);
    a[3] = smf64(va[1], w2[3][1], a[3], idx);
    SB();
    const u32 u2 = fin(2);
    const auto e0 = __builtin_amdgcn_permlane32_swap(u0, u2, false, false);
    SB();
    const auto e1 = __builtin_amdgcn_permlane32_swap(fin(1), fin(3), false, false);
    const auto f0 = __builtin_amdgcn_permlane16_swap(e0[0], e1[0], false, false);
    const auto f1_ = __builtin_amdgcn_permlane16_swap(e0[1], e1[1], false, false);
    const v4u o0 = v4u{f0[0], f0[1], f1_[0], f1_[1]};
    st128(o0, r, so0, 0);
    SB();
    *(LAS u32*)(L + LM_T + w * 4) = rt7();

    if (s >= 0) __builtin_amdgcn_s_sleep(VBG);
    const int zo = s < 0 ? JUNK : CZ_OFF + s * NTILE * 8192;
    st128(o0, r, lz0, zo);
    st128(v4u{SENT, SENT, SENT, SENT}, r, lr0, sprev);
    if (FIVE) { st128(o1, r, lz1, zo); st128(v4u{SENT, SENT, SENT, SENT}, r, lr1, sprev); }
    if (w == 6 && s > 0 && lane == 0) set_flag(r, CTRL + (L_VP + h) * 128, s - 1);
    if (GATHER && s >= 0) __builtin_amdgcn_s_sleep(VSLP);
    sprev = s < 0 ? sprev : sb;
    sb = s < 0 ? sb : next_slot(sb, par);
  }
  drain();
}

DEVI void role_V(const Params& p, const WS& r, LAS unsigned char* L, int h) {
  const int lane = threadIdx.x & 63, w = uni(threadIdx.x >> 6);
  v8u w1[9], w2[5][2];
  {
    const int o = h * 256 + (lane & 15) * 16;
#pragma unroll
    for (int q = 0; q < 6; ++q) st128(v4u{SENT, SENT, SENT, SENT}, r, (4 * w + (lane >> 4)) * CHUNK + o, (q & 1) * 8192 + (q >> 1) * SLOT);
    if (w < 6) st128(v4u{SENT, SENT, SENT, SENT}, r, (32 + (lane >> 4)) * CHUNK + o, (w & 1) * 8192 + (w >> 1) * SLOT);
    else st32(SENT, r, lane == 0 ? T_HV + h * 128 : OOB, (w & 1) * 8192);
  }
  {
    WLoad<1> wl; wl.init(p.wv, p.wo, h);
    v4u sa[9], sb_[9], sc[9];
    wl.req<0, 0>(sa); wl.req<1, 0>(sb_);
    wl.put<0>(L, sa); wl.req<0, 1>(sc); bar();
    wl.put<1>(L, sb_); wl.req<1, 1>(sa); get_v1<0>(L, w, lane, w1); bar();
    wl.put<0>(L, sc); get_v2<0>(L + 73728, w, lane, w2); bar();
    wl.put<1>(L, sa); drain(); get_v1<1>(L, w, lane, w1); bar();
    get_v2<1>(L + 73728, w, lane, w2); bar();
  }
  if (w == 6 && lane == 0) set_flag(r, CTRL + (L_VP + h) * 128, -1);
  if (w < 4) { __builtin_amdgcn_s_setprio(VPRIO); v_loop<2, 0, 0>(p, r, L, h, w, w1, w2); }
  else if (w == 4) v_loop<1, 1, 0>(p, r, L, h, w, w1, w2);
  else if (w < 7) v_loop<0, 1, 0>(p, r, L, h, w, w1, w2);
  else v_loop<0, 1, 1>(p, r, L, h, w, w1, w2);
  bar();
  if (w == 6 && lane == 0) set_flag(r, CTRL + (L_VP + h) * 128, NSEG - 1);
}

DEVI u64 cyc() { u64 t; asm volatile("s_memtime %0\n\ts_waitcnt lgkmcnt(0)" : "=s"(t)::"memory"); return t; }

DEVI void role_R(const Params& p, const WS& r, LAS unsigned char* L, int j) {
  const int lane = threadIdx.x & 63, w = uni(threadIdx.x >> 6), n = lane & 15, g = lane >> 4;
  int rounds = 0;
  if (j == 0 && w == 4) {
    u32 best[2] = {~0u, ~0u};
    if (lane == 0) {
      for (int it = 0; it < 3; ++it)
        for (int q = 0; q < 2; ++q) {
          GAS u32* a = (GAS u32*)(p.ws + MEM_OFF + CHUNK + q * 8192 + 7168);
          const u64 t0 = cyc();
          u32 v = __hip_atomic_fetch_add(a, 1u, __ATOMIC_RELAXED, __HIP_MEMORY_SCOPE_AGENT);
          asm volatile("s_waitcnt vmcnt(0)" ::"v"(v) : "memory");
          const u32 d = (u32)(cyc() - t0);
          best[q] = d < best[q] ? d : best[q];
        }
      set_flag(r, CTRL + L_PAR * 128, best[1] < best[0] ? 1 : 0);
    }
  }
  const int par = wait_parity(r, rounds);
  int sb = par;
  if (w < 4) {
    const int idx = (lane & 2) ? 0xEEEE : 0x4444;
    const int pxo = opaque(P_OFF + w * CHUNK + (g >> 1) * 1024 + (n >> 3) * 512 + (g & 1) * 128 + (n & 7) * 16);
    const int pmo = P_OFF + w * CHUNK + g * 1024 + (n >> 3) * 512 + 256 + (n & 7) * 8;
    const int co = opaque(j * CHUNK + w * 2048 + lane * 16);
    const int redw = LM_RED + w * 512 + (g & 1) * 256 + (n >> 3) * 64 + (g >> 1) * 32 + (n & 7) * 4;
    const int hio = (lane < 32 ? T_HS : T_HV) + (8 * w + ((lane >> 2) & 7)) * 128;
    for (int s = sopaque(-2); s < NSEG - 1; ++s) {
      v4u bz[8];
#pragma unroll
      for (int q = 0; q < 8; ++q) bz[q] = *(LAS v4u*)(L + LM_BZ + (s & 1) * 32768 + (8 * w + q) * 1024 + lane * 16);
      if (s > 0) __builtin_amdgcn_s_sleep(RSLP);
      if (s == -1) { int seen = -2; wait_prog(r, L_SP, 64, -1, lane, seen, rounds); }
      const u32 hv = hint_wait(r, hio, par, s);
      const u32 tv = s < 0 ? rt7() : hv + (lane < 32 ? MAR_R : MAR_C) * 128;
      LAS unsigned char* rw = (LAS unsigned char*)(size_t)opaque((int)(size_t)(L + (s & 1) * 2048 + redw));
      auto attempt = [&](bool first) {
        v4u px[4]; v2u pm[2];
        if (first) time_wait<0>(tv);
#pragma unroll
        for (int q = 0; q < 4; ++q) { px[q] = ld128(r, pxo + (q & 1) * 2048, sb + (q >> 1) * 4096); SB(); }
#pragma unroll
        for (int q = 0; q < 2; ++q) { pm[q] = ld64(r, pmo, sb + q * 4096); SB(); }
        if (first) time_wait<1>(tv);
        const v4u c0 = ld128(r, co, sb);
        SB();
        const v4u c1 = ld128(r, co + 1024, sb);
        f32x4 a = zero4();
        u32 mp = 0;
        SB();
#pragma unroll
        for (int q = 0; q < 4; ++q) {
          a = smf64(px[q], cat(bz[2 * q], bz[2 * q + 1]), a, idx);
          SB();
          mp = umax(mp, mx4(px[q]));
          asm volatile("" : "+v"(mp));
          SB();
        }
        mp = umax(mp, umax(umax(pm[0].x, pm[0].y), umax(pm[1].x, pm[1].y)));
        asm volatile("" : "+v"(mp));
        SB();
        a = smf32(pm[0], c0, a, idx);
        SB();
        a = smf32(pm[1], c1, a, idx);
        SB();
        mp = umax(mp, umax(mx4(c0), mx4(c1)));
        const f32x2 fa = fold(a);
        *(LAS float*)rw = fa.x;
        *(LAS float*)(rw + 128) = fa.y;
        return any_sent(mp);
      };
      if (__builtin_expect(attempt(true), 0) && s >= 0) {
        POLL(bad = attempt(false));
      }
      bar();
      __builtin_amdgcn_s_sleep(4);
      sb = s < 0 ? sb : next_slot(sb, par);
    }
  } else {
    const int mo = MEM_OFF + (j >> 5) * CHUNK + (j & 31) * 256 + lane * 4, l16 = opaque(lane * 16);
    int snext = par + SLOT, seen0 = -2, seen1 = -2;
    auto fetch = [&](int s) {
      if (s >= 0) wait_prog2(r, L_YP + 8 * (w - 4), 8, s, lane, seen0, seen1, rounds);
      else s = 1;
      v4u t[8];
#pragma unroll
      for (int q = 0; q < 8; ++q) t[q] = ld128(r, l16 + (q & 3) * 1024, BZ_OFF + (s * NTILE + j) * 32768 + (w - 4) * 8192 + (q >> 2) * 4096);
#pragma unroll
      for (int q = 0; q < 8; ++q) *(LAS v4u*)(L + LM_BZ + (s & 1) * 32768 + (8 * (w - 4) + q) * 1024 + l16) = t[q];
    };
    if (w == 4) {
      const u16* src = p.mem0 + ((lane >> 4) * 2 + ((lane >> 2) & 1)) * DM + 32 * (j >> 1) + (j & 1) + 16 * ((lane >> 3) & 1) + 4 * (lane & 3);
      st32(guard((u32)src[0] | ((u32)src[2] << 16)), r, mo, sb);
      st32(SENT, r, mo, par + SLOT);
      st32(SENT, r, mo, par + 2 * SLOT);
      drain();
      if (lane == 0) set_flag(r, CTRL + (L_RP + j) * 128, par >> 13);
      __builtin_amdgcn_s_setprio(2);
      for (int s = sopaque(-2); s < NSEG - 1; ++s) {
        LAS unsigned char* red = L + LM_RED + (s & 1) * 2048 + lane * 8;
        const int so = s < 0 ? JUNK : snext;
        fetch(s + 1);
        drain();
        bar();
        f32x2 t[4];
#pragma unroll
        for (int q = 0; q < 4; ++q) t[q] = *(LAS f32x2*)(red + q * 512);
        const f32x2 u = (t[0] + t[1]) + (t[2] + t[3]);
        st32(guard(pack2(u.x, u.y)), r, mo, so);
        SB();
        if (s >= 0) {
          st32(SENT, r, mo, sb);
          sb = snext;
          snext = next_slot(snext, par);
        }
      }
    } else {
      for (int s = sopaque(-2); s < NSEG - 1; ++s) {
        fetch(s + 1);
        bar();
        __builtin_amdgcn_s_sleep(4);
      }
    }
  }
}

DEVI int ximg_rd(int lane) { const int n = lane & 15, g = lane >> 4; return n * 64 + (g ^ ((n >> 3) << 1)) * 16; }
DEVI int ximg_piece(int lane) { return ((lane & 3) ^ ((lane >> 5) << 1)) * 16; }
template <int DEPTH>
DEVI void proj32(LAS unsigned char* xb, const v4u (&wt)[2][18], v4u (&o)[2]) {
  f32x4 acc[2][2];
#pragma unroll
  for (int i = 0; i < 4; ++i) acc[i >> 1][i & 1] = zero4();
  v4u xf[DEPTH][2];
#pragma unroll
  for (int ks = 0; ks < DEPTH - 1; ++ks) { xf[ks][0] = *(LAS v4u*)(xb + ks * 1024); xf[ks][1] = *(LAS v4u*)(xb + (18 + ks) * 1024); }
  SB();
#pragma unroll
  for (int ks = 0; ks < 18; ++ks) {
    const int kn = ks + DEPTH - 1;
    if (kn < 18) { xf[kn % DEPTH][0] = *(LAS v4u*)(xb + kn * 1024); xf[kn % DEPTH][1] = *(LAS v4u*)(xb + (18 + kn) * 1024); }
    SB();
#pragma unroll
    for (int m = 0; m < 2; ++m) {
      acc[0][m] = mfma(wt[0][ks], xf[ks % DEPTH][m], acc[0][m]);
      acc[1][m] = mfma(wt[1][ks], xf[ks % DEPTH][m], acc[1][m]);
    }
    SB();
  }
#pragma unroll
  for (int m = 0; m < 2; ++m)
#pragma unroll
    for (int q = 0; q < 4; ++q) o[m][q] = pack2(acc[0][m][q], acc[1][m][q]);
}

struct XStage {
  rsrc_t rx; int go, lo; v4u st[9];
  DEVI void init(const Params& p, int tok, int q, int lane) {
    rx = mk_rsrc(p.x, 2048 * DM * 2);
    go = tok * (DM * 2) + (q & 1) * 9 * 64 + ximg_piece(lane);
    lo = q * 9 * 1024 + lane * 16;
  }
  DEVI void req(int s) {
#pragma unroll
    for (int k = 0; k < 9; ++k) st[k] = pl128(rx, go + k * 64, (s < NSEG ? s : NSEG - 1) * 32 * DM * 2);
  }
  DEVI void put(LAS unsigned char* L) {
#pragma unroll
    for (int k = 0; k < 9; ++k) *(LAS v4u*)(L + lo + k * 1024) = st[k];
  }
};

DEVI int seg_of(int hp, int i) { return i < HSEG / 2 ? 2 * i + hp : i + HSEG / 2; }

DEVI void role_X(const Params& p, const WS& r, LAS unsigned char* L, int h) {
  const int lane = threadIdx.x & 63, w = uni(threadIdx.x >> 6);
  const int rd = ximg_rd(lane), oo = KQ_OFF + h * 16384 + (w >> 2) * 8192 + (w & 3) * 1024 + lane * 16;
  constexpr int cnt = NSEG;
  v4u wt[2][18];
  if (w < 4) {
    XStage xs; xs.init(p, 2 * (lane >> 2) + (w >> 1), w, lane);
    xs.req(0);
    LOAD_W(0, p.wk, p.wq, get_w1_dense, 0, w, xs.put(L); xs.req(1))
    __builtin_amdgcn_s_setprio(1);
    const int fo = (w == 0 && lane == 0) ? CTRL + (L_XP + h) * 128 : OOB;
    for (int it = sopaque(0); it < cnt; ++it) {
      const int s = it;
      v4u o[2];
      proj32<3>(L + (it & 1) * 36864 + rd, wt, o);
      st128(o[0], r, oo, s * NH * 16384);
      st128(o[1], r, oo + 4096, s * NH * 16384);
      SB();
      xs.put(L + ((it + 1) & 1) * 36864);
      SB();
      xs.req(it + 2);
      asm volatile("s_waitcnt vmcnt(9)" ::: "memory");
      bar();
      set_flag(r, fo, it < XUN || it + 1 == cnt ? s : it - 1);
    }
    __builtin_amdgcn_s_setprio(0);
  } else {
    LOAD_W(0, p.wk, p.wq, get_w1_dense, 1, w & 3, )
    for (int it = sopaque(0); it < cnt; ++it) {
      const int s = it;
      v4u o[2];
      proj32<4>(L + (it & 1) * 36864 + rd, wt, o);
      st128(o[0], r, oo, s * NH * 16384);
      st128(o[1], r, oo + 4096, s * NH * 16384);
      if (it < XUN || it + 1 == cnt) drain();
      asm volatile("s_waitcnt vmcnt(2)" ::: "memory");
      bar();
    }
  }
}

DEVI void role_Y(const Params& p, const WS& r, LAS unsigned char* L, int h, int hp) {
  const int lane = threadIdx.x & 63, w = uni(threadIdx.x >> 6);
  const int cnt = hp ? HSEG / 2 : NSEG - HSEG / 2;
  constexpr int VB = 73728;
  if (w < 4) {
    v4u wt[2][18];
    {
      const int row = lane >> 2;
      XStage xs; xs.init(p, 8 * (row >> 2) + (row & 3) + 4 * (w >> 1), w, lane);
      xs.req(seg_of(hp, 0));
      LOAD_W(1, p.wv, p.wo, get_w1_dense, 0, w, xs.put(L))
    }
    const int rd = ximg_rd(lane);
    const int fo = (w == 0 && lane == 0) ? CTRL + (L_YP + hp * 32 + h) * 128 : OOB;
    for (int it = sopaque(0); it < cnt; ++it) {
      v4u o[2];
      proj32<4>(L + (it & 1) * 36864 + rd, wt, o);
      *(LAS v4u*)(L + VB + (it & 1) * 8192 + w * 1024 + lane * 16) = o[0];
      *(LAS v4u*)(L + VB + (it & 1) * 8192 + 4096 + w * 1024 + lane * 16) = o[1];
      bar();
      if (it == 0) { bar(); set_flag(r, fo, seg_of(hp, 0)); }
      if (it > 1) set_flag(r, fo, seg_of(hp, it - 1));
    }
    bar();
    set_flag(r, fo, seg_of(hp, cnt - 1));
  } else {
    const int c = w - 4, row = lane >> 2;
    XStage xs; xs.init(p, 8 * (row >> 2) + (row & 3) + 4 * (c >> 1), c, lane);
    xs.req(seg_of(hp, 1));
    v8u wt[9][2];
    LOAD_W(1, p.wv, p.wo, get_w2, 1, c, )
    __builtin_amdgcn_s_setprio(1);
    xs.put(L + 36864);
    xs.req(seg_of(hp, 2));
    bar();
    const int oo = BZ_OFF + h * 1024 + lane * 16;
    const int t0o = tile_of(c, 0) * 32768, t8o = tile_of(c, 8) * 32768;
    for (int it = sopaque(0); it < cnt; ++it) {
      const int s = seg_of(hp, it);
      v4u vf[2][4];
#pragma unroll
      for (int k = 0; k < 8; ++k) vf[k >> 2][k & 3] = *(LAS v4u*)(L + VB + (it & 1) * 8192 + k * 1024 + lane * 16);
#pragma unroll
      for (int i = 0; i < 9; ++i) {
        f32x4 a0 = {0, 0, 0, 0}, a1 = {0, 0, 0, 0};
#pragma unroll
        for (int k = 0; k < 2; ++k) {
          a0 = mfma(vf[0][2 * k], lo4(wt[i][k]), a0); a1 = mfma(vf[1][2 * k], lo4(wt[i][k]), a1);
          a0 = mfma(vf[0][2 * k + 1], hi4(wt[i][k]), a0); a1 = mfma(vf[1][2 * k + 1], hi4(wt[i][k]), a1);
        }
        st128(v4u{pack2(a0[0], a0[1]), pack2(a0[2], a0[3]), pack2(a1[0], a1[1]), pack2(a1[2], a1[3])}, r, oo,
              s * NTILE * 32768 + (i < 8 ? t0o + i * 32768 : t8o));
      }
      SB();
      xs.put(L + (it & 1) * 36864);
      SB();
      xs.req(seg_of(hp, it + 3));
      asm volatile("s_waitcnt vmcnt(9)" ::: "memory");
      bar();
      if (it == 0) bar();
    }
    __builtin_amdgcn_s_setprio(0);
  }
}

DEVI void role_T(const Params& p, const WS& r, int ti) {
  const int lane = threadIdx.x & 63, w = uni(threadIdx.x >> 6), n = lane & 15, g = lane >> 4;
  const rsrc_t r1 = mk_rsrc(p.w1, DM * FF * 2), r2 = mk_rsrc(p.w2, DM * FF * 2);
  { const u64 t0 = __builtin_amdgcn_s_memrealtime(); while (__builtin_amdgcn_s_memrealtime() - t0 < TDLY) __builtin_amdgcn_s_sleep(16); }
  {
    const rsrc_t rg = mk_rsrc(p.gw, DM * 2), rx = mk_rsrc(p.x, 2048 * DM * 2);
    const int o = lane < 36 ? lane * 32 : OOB, t0 = (ti * 8 + w) * 5;
    const v4u g0 = pl128(rg, o, 0), g1 = pl128(rg, o + 16, 0);
    float mine = 0.f;
#pragma unroll
    for (int t = 0; t < 5; ++t) {
      const int tok = t0 + t < 2048 ? t0 + t : 2047;
      const v4u x0 = pl128(rx, o, tok * DM * 2), x1 = pl128(rx, o + 16, tok * DM * 2);
      float a = 0.f;
#pragma unroll
      for (int q = 0; q < 4; ++q) {
        a += bf2f(x0[q] & 0xFFFFu) * bf2f(g0[q] & 0xFFFFu) + __uint_as_float(x0[q] & 0xFFFF0000u) * __uint_as_float(g0[q] & 0xFFFF0000u);
        a += bf2f(x1[q] & 0xFFFFu) * bf2f(g1[q] & 0xFFFFu) + __uint_as_float(x1[q] & 0xFFFF0000u) * __uint_as_float(g1[q] & 0xFFFF0000u);
      }
      a = x32_sum(x16_sum(row16_sum(a)));
      mine = lane == t ? a : mine;
    }
    const float gv = __builtin_amdgcn_rcpf(1.f + __builtin_amdgcn_exp2f(-1.4426950408889634f * mine));
    st32(__float_as_uint(gv), r, (lane < 5 && t0 + lane < 2048) ? G_OFF + (t0 + lane) * 4 : OOB, 0);
  }
  const int u0 = ti * 8 + w;
  v4u raw[2][8];
#pragma unroll
  for (int i = 0; i < 2; ++i) {
    const int u = u0 + i * 480 < 648 ? u0 + i * 480 : u0, c = u / 18, ks = u - 18 * c;
#pragma unroll
    for (int q = 0; q < 8; ++q) raw[i][q] = __builtin_amdgcn_raw_buffer_load_b128(r1, (8 * g * FF + 8 * n) * 2, ((32 * ks + q) * FF + 128 * c) * 2, TAUX);
  }
#pragma unroll
  for (int i = 0; i < 2; ++i) {
    const int u = u0 + i * 480 < 648 ? u0 + i * 480 : u0, c = u / 18, ks = u - 18 * c;
#pragma unroll
    for (int t = 0; t < 8; ++t) {
      v4u o;
#pragma unroll
      for (int q = 0; q < 4; ++q) o[q] = __builtin_amdgcn_perm(raw[i][2 * q + 1][t >> 1], raw[i][2 * q][t >> 1], (t & 1) ? 0x07060302u : 0x05040100u);
      st128(o, r, W1T_OFF + lane * 16, ((c * 4 + (t >> 1)) * 18 + ks) * 2048 + (t & 1) * 1024);
    }
  }
  u32 r9[2][8];
#pragma unroll
  for (int i = 0; i < 2; ++i) {
    const int u = u0 + i * 480 < 576 ? u0 + i * 480 : u0, k = u >> 2, c4 = u & 3;
    const int so = ((128 * (k >> 2) + 2 * (k & 3)) * DM + 144 * c4) * 2;
#pragma unroll
    for (int q = 0; q < 8; ++q) raw[i][q] = __builtin_amdgcn_raw_buffer_load_b128(r2, (32 * g * DM + 8 * n) * 2, so + (8 * (q >> 1) + (q & 1)) * DM * 2, TAUX);
#pragma unroll
    for (int q = 0; q < 8; ++q) r9[i][q] = __builtin_amdgcn_raw_buffer_load_b16(r2, (32 * g * DM + 128 + n) * 2, so + (8 * (q >> 1) + (q & 1)) * DM * 2, 0);
  }
#pragma unroll
  for (int i = 0; i < 2; ++i) {
    const int u = u0 + i * 480 < 576 ? u0 + i * 480 : u0;
#pragma unroll
    for (int t = 0; t < 9; ++t) {
      v4u o;
#pragma unroll
      for (int q = 0; q < 4; ++q)
        o[q] = t < 8 ? __builtin_amdgcn_perm(raw[i][2 * q + 1][(t & 7) >> 1], raw[i][2 * q][(t & 7) >> 1], (t & 1) ? 0x07060302u : 0x05040100u)
                     : __builtin_amdgcn_perm(r9[i][2 * q + 1], r9[i][2 * q], 0x05040100u);
      st128(o, r, W2T_OFF + lane * 16, u * 9216 + t * 1024);
    }
  }
  drain();
  bar();
  if (threadIdx.x == 0) set_flag(r, CTRL + (L_TP + ti) * 128, 0);
}

DEVI float gelu_tanh(float x) {
  const float e = __builtin_amdgcn_exp2f(x * (-2.302208198f - 0.1029432397f * x * x));
  return x * __builtin_amdgcn_rcpf(1.f + e);
}

#define FLD ld128
DEVI void role_F(const Params& p, const WS& r, LAS unsigned char* L, int tt, int sp, int c0, int c1) {
  const int lane = threadIdx.x & 63, w = uni(threadIdx.x >> 6), n = lane & 15, g = lane >> 4;
  constexpr int HB = 73728;
  const rsrc_t rx = mk_rsrc(p.x, 2048 * DM * 2);
  { int rounds = 0, seen = -2; wait_prog(r, L_TP, NTB, 0, lane, seen, rounds); }
  bar();
  {
    const int go = (tt * 64 + (w >> 1) * 16 + (lane >> 2)) * (DM * 2) + (w & 1) * 9 * 64 + ximg_piece(lane);
    v4u st[9];
#pragma unroll
    for (int k = 0; k < 9; ++k) st[k] = pl128(rx, go + k * 64, 0);
#pragma unroll
    for (int k = 0; k < 9; ++k) *(LAS v4u*)(L + (w * 9 + k) * 1024 + lane * 16) = st[k];
  }
  if (w < 4) {
    const rsrc_t rb = mk_rsrc(p.b1, FF * 2);
    const int vo = W1T_OFF + w * 18 * 2048 + lane * 16;
    constexpr int WIN = FWIN;
    __builtin_amdgcn_s_setprio(FPRIO);
    v4u wf[WIN][2];
    auto req = [&](int slot, int c, int ks) {
      wf[slot][0] = FLD(r, vo, (c * 72 + ks) * 2048);
      wf[slot][1] = FLD(r, vo + 1024, (c * 72 + ks) * 2048);
    };
#pragma unroll
    for (int k = 0; k < WIN - 1; ++k) req(k, c0, k);
    bar();
    const int rd = ximg_rd(lane);
    for (int c = c0; c < c1; ++c) {
      const int cn = c + 1 < c1 ? c + 1 : c;
      v4u bias;
#pragma unroll
      for (int q = 0; q < 4; ++q) bias[q] = pl32(rb, (32 * g + 2 * w) * 2 + q * 16, c * 256);
      f32x4 acc[2][4];
#pragma unroll
      for (int i = 0; i < 8; ++i) acc[i >> 2][i & 3] = zero4();
      v4u xf[2][4];
#pragma unroll
      for (int m = 0; m < 4; ++m) xf[0][m] = *(LAS v4u*)(L + (m * 18) * 1024 + rd);
#pragma unroll
      for (int ks = 0; ks < 18; ++ks) {
        const int kn = ks + WIN - 1;
        req(kn % WIN, kn < 18 ? c : cn, kn % 18);
        if (ks + 1 < 18) {
#pragma unroll
          for (int m = 0; m < 4; ++m) xf[(ks + 1) & 1][m] = *(LAS v4u*)(L + (m * 18 + ks + 1) * 1024 + rd);
        }
        SB();
#pragma unroll
        for (int m = 0; m < 4; ++m) { acc[0][m] = mfma(wf[ks % WIN][0], xf[ks & 1][m], acc[0][m]); acc[1][m] = mfma(wf[ks % WIN][1], xf[ks & 1][m], acc[1][m]); }
        SB();
      }
#pragma unroll
      for (int m = 0; m < 4; ++m) {
        v4u o;
#pragma unroll
        for (int q = 0; q < 4; ++q)
          o[q] = pack2(gelu_tanh(acc[0][m][q] + bf2f(bias[q] & 0xFFFFu)), gelu_tanh(acc[1][m][q] + __uint_as_float(bias[q] & 0xFFFF0000u)));
        *(LAS v4u*)(L + HB + ((c - c0) & 1) * 16384 + (m * 4 + w) * 1024 + lane * 16) = o;
      }
      bar();
    }
  } else {
    const int c4 = w - 4;
    const int vo = W2T_OFF + c4 * 9216 + lane * 16;
    v4u bw[2][9];
#pragma unroll
    for (int t = 0; t < 9; ++t) bw[0][t] = FLD(r, vo + t * 1024, 4 * c0 * 36864);
    f32x4 acc[9][4];
#pragma unroll
    for (int i = 0; i < 36; ++i) acc[i >> 2][i & 3] = zero4();
    bar();
    bar();
    for (int c = c0; c < c1; ++c) {
#pragma unroll
      for (int ks = 0; ks < 4; ++ks) {
        const int kn = 4 * c + ks + 1 < 4 * c1 ? 4 * c + ks + 1 : 4 * c + ks;
        v4u hf[4];
#pragma unroll
        for (int m = 0; m < 4; ++m) hf[m] = *(LAS v4u*)(L + HB + ((c - c0) & 1) * 16384 + (m * 4 + ks) * 1024 + lane * 16);
#pragma unroll
        for (int t = 0; t < 9; ++t) {
          bw[(ks + 1) & 1][t] = FLD(r, vo + t * 1024, kn * 36864);
          SB();
#pragma unroll
          for (int m = 0; m < 4; ++m) acc[t][m] = mfma(hf[m], bw[ks & 1][t], acc[t][m]);
          SB();
        }
      }
      if (c + 1 < c1) bar();
    }
    const int xo = FP_OFF + (tt * 4 + c4) * 36864 + lane * 16;
    if (sp == 1) {
#pragma unroll
      for (int i = 0; i < 36; ++i) st128(__builtin_bit_cast(v4u, acc[i >> 2][i & 3]), r, xo, i * 1024);
    } else {
      int rounds = 0, seen = -2;
      wait_prog(r, L_FP + 32 + tt, 1, 0, lane, seen, rounds);
#pragma unroll
      for (int j = 0; j < 3; ++j) {
        v4u t[12];
#pragma unroll
        for (int i = 0; i < 12; ++i) t[i] = ld128(r, xo, (12 * j + i) * 1024);
#pragma unroll
        for (int i = 0; i < 12; ++i) { acc[3 * j + (i >> 2)][i & 3] += __builtin_bit_cast(f32x4, t[i]); asm volatile("" : "+v"(acc[3 * j + (i >> 2)][i & 3])); }
        SB();
      }
      const rsrc_t ro = mk_rsrc(p.out, 2048 * DM * 2), rb = mk_rsrc(p.b2, DM * 2);
      const v4u b8 = pl128(rb, (144 * c4 + 8 * n) * 2, 0);
      const float b9 = bf2f(__builtin_amdgcn_raw_buffer_load_b16(rb, (144 * c4 + 128 + n) * 2, 0, 0));
      if (tt < CUT) {
        const int sg = 2 * tt + (lane >= 9), want = lane < 18 ? sg : -2;
        const int vo2 = CTRL + (L_OP + (sg % 3) * 9 + lane % 9) * 128;
        POLL(const int v = get_flag(r, vo2); bad = __builtin_amdgcn_ballot_w64(v < want) != 0; if (bad) __builtin_amdgcn_s_sleep(32));
      }
      const int o8 = ((tt * 64 + 4 * g) * DM + 144 * c4 + 8 * n) * 2, o9 = ((tt * 64 + 4 * g) * DM + 144 * c4 + 128 + n) * 2;
#pragma unroll
      for (int m = 0; m < 4; ++m) {
        const v4u gq = ld128(r, G_OFF + (tt * 64 + 16 * m + 4 * g) * 4, 0);
        v4u a8[4]; u32 a9[4];
#pragma unroll
        for (int q = 0; q < 4; ++q) {
          a8[q] = ld128(ro, tt < CUT ? o8 : OOB, (16 * m + q) * DM * 2);
          a9[q] = __builtin_amdgcn_raw_buffer_load_b16(ro, tt < CUT ? o9 : OOB, (16 * m + q) * DM * 2, SC1);
        }
#pragma unroll
        for (int q = 0; q < 4; ++q) {
          const float k = 1.f - __uint_as_float(gq[q]);
          v4u o;
#pragma unroll
          for (int t = 0; t < 4; ++t)
            o[t] = pack2(k * (acc[2 * t][m][q] + bf2f(b8[t] & 0xFFFFu)) + bf2f(a8[q][t] & 0xFFFFu),
                         k * (acc[2 * t + 1][m][q] + __uint_as_float(b8[t] & 0xFFFF0000u)) + __uint_as_float(a8[q][t] & 0xFFFF0000u));
          st128(o, ro, o8, (16 * m + q) * DM * 2);
          __builtin_amdgcn_raw_buffer_store_b16((u16)pack2(k * (acc[8][m][q] + b9) + bf2f(a9[q]), 0.f), ro, o9, (16 * m + q) * DM * 2, SC1);
        }
        SB();
      }
    }
  }
  drain();
  bar();
  if (threadIdx.x == 0) set_flag(r, CTRL + (L_FP + sp * 32 + tt) * 128, 0);
}

DEVI void role_O(const Params& p, const WS& r, LAS unsigned char* L, int cg, int s0, int ds) {
  const int lane = threadIdx.x & 63, w = uni(threadIdx.x >> 6), n = lane & 15, g = lane >> 4;
  int rounds = 0, seenY0 = -2, seenY1 = -2, seenS = -2;
  const int fm = w & 1, ftp = (w >> 1) & 1, frh = w >> 2;
  const int col = 64 * cg + 32 * ftp + 2 * n;
  const rsrc_t ro = mk_rsrc(p.out, 2048 * DM * 2);
  { int seen = -2; wait_prog(r, L_TP, NTB, 0, lane, seen, rounds); }
  const int svo = CTRL + ((lane & 4 ? L_VP : L_SP) + 4 * w + (lane & 3)) * 128;
  for (int s = s0; s < NSEG - 4; s += ds) {
    wait_prog2(r, L_YP + 4 * w, 4, s, lane, seenY0, seenY1, rounds);
    v4u bz[4][4];
#pragma unroll
    for (int t = 0; t < 4; ++t)
#pragma unroll
      for (int q = 0; q < 4; ++q) bz[t][q] = ld128(r, (4 * w + q) * 1024 + lane * 16, BZ_OFF + (s * NTILE + 4 * cg + t) * 32768);
    const int tt = s >> 1;
    if (tt >= CUT) { int seenF = -2; wait_prog(r, L_FP + tt, 1, 0, lane, seenF, rounds); }
    const int tok = s * 32 + 2 * (4 * g + 2 * frh) + fm;
    u32 fb[2];
    float gt[2];
#pragma unroll
    for (int q = 0; q < 2; ++q) {
      fb[q] = ld32(ro, tt >= CUT ? ((tok + 2 * q) * DM + col) * 2 : OOB, 0);
      gt[q] = __uint_as_float(ld32(r, G_OFF + (tok + 2 * q) * 4, 0));
    }
    if (__builtin_amdgcn_ballot_w64(seenS < s) != 0) {
      POLL(seenS = get_flag(r, svo); bad = __builtin_amdgcn_ballot_w64(seenS < s) != 0; if (bad) __builtin_amdgcn_s_sleep(4));
    }
    v4u px[2][4], pm[2], cz[4];
#pragma unroll
    for (int m = 0; m < 2; ++m) {
#pragma unroll
      for (int q = 0; q < 4; ++q) px[m][q] = ld128(r, ((4 * w + q) * 2 + m) * 1280 + lane * 16, PX_OFF + s * NH * 2560);
      pm[m] = ld128(r, ((4 * w + g) * 2 + m) * 1280 + 1024 + n * 16, PX_OFF + s * NH * 2560);
    }
#pragma unroll
    for (int t = 0; t < 4; ++t) cz[t] = ld128(r, w * 1024 + lane * 16, CZ_OFF + (s * NTILE + 4 * cg + t) * 8192);
#pragma unroll
    for (int t = 0; t < 4; ++t)
#pragma unroll
      for (int m = 0; m < 2; ++m) {
        f32x4 a = {0, 0, 0, 0};
#pragma unroll
        for (int q = 0; q < 4; ++q) a = mfma(px[m][q], bz[t][q], a);
        a = mfma(pm[m], cz[t], a);
        *(LAS f32x4*)(L + w * 8192 + (t * 2 + m) * 1024 + lane * 16) = a;
      }
    bar();
    f32x2 e = {0, 0}, o = {0, 0};
#pragma unroll
    for (int q = 0; q < 8; ++q) {
      e += *(LAS f32x2*)(L + q * 8192 + (4 * ftp + fm) * 1024 + lane * 16 + frh * 8);
      o += *(LAS f32x2*)(L + q * 8192 + (4 * ftp + 2 + fm) * 1024 + lane * 16 + frh * 8);
    }
#pragma unroll
    for (int q = 0; q < 2; ++q) {
      st32(pack2(gt[q] * e[q] + bf2f(fb[q] & 0xFFFFu), gt[q] * o[q] + __uint_as_float(fb[q] & 0xFFFF0000u)), ro, ((tok + 2 * q) * DM + col) * 2, 0);
    }
    drain();
    bar();
    if (threadIdx.x == 0) set_flag(r, CTRL + (L_OP + s0 * 9 + cg) * 128, s);
  }
}

DEVI void role_O2(const Params& p, const WS& r, LAS unsigned char* L, int pr, int m, int s0) {
  const int lane = threadIdx.x & 63, w = uni(threadIdx.x >> 6), n = lane & 15, g = lane >> 4;
  int rounds = 0, seenY = -2, seenS = -2;
  const rsrc_t ro = mk_rsrc(p.out, 2048 * DM * 2);
  const int svo = CTRL + ((lane & 4 ? L_VP : L_SP) + 4 * w + (lane & 3)) * 128;
  bar();
  for (int s = s0; s < NSEG; s += 2) {
    wait_prog(r, L_YP + 4 * w, 4, s, lane, seenY, rounds);
    v4u bz[2][4];
#pragma unroll
    for (int t = 0; t < 2; ++t)
#pragma unroll
      for (int q = 0; q < 4; ++q) bz[t][q] = ld128(r, (4 * w + q) * 1024 + lane * 16, BZ_OFF + (s * NTILE + 2 * pr + t) * 32768);
    { int seenF = -2; wait_prog(r, L_FP + (s >> 1), 1, 0, lane, seenF, rounds); }
    const int tok = s * 32 + 2 * (4 * g + (w & 3)) + m, oo = (tok * DM + 32 * pr + 2 * n) * 2;
    const u32 fb = ld32(ro, oo, 0);
    const float gt = __uint_as_float(ld32(r, G_OFF + tok * 4, 0));
    POLL(seenS = get_flag(r, svo); bad = __builtin_amdgcn_ballot_w64(seenS < s) != 0; if (bad) { if (__builtin_amdgcn_ballot_w64(seenS < s - 2) != 0) __builtin_amdgcn_s_sleep(32); else __builtin_amdgcn_s_sleep(1); });
    v4u px[4], cz[2];
#pragma unroll
    for (int q = 0; q < 4; ++q) px[q] = ld128(r, ((4 * w + q) * 2 + m) * 1280 + lane * 16, PX_OFF + s * NH * 2560);
    const v4u pm = ld128(r, ((4 * w + g) * 2 + m) * 1280 + 1024 + n * 16, PX_OFF + s * NH * 2560);
#pragma unroll
    for (int t = 0; t < 2; ++t) cz[t] = ld128(r, w * 1024 + lane * 16, CZ_OFF + (s * NTILE + 2 * pr + t) * 8192);
#pragma unroll
    for (int t = 0; t < 2; ++t) {
      f32x4 a = {0, 0, 0, 0};
#pragma unroll
      for (int q = 0; q < 4; ++q) a = mfma(px[q], bz[t][q], a);
      a = mfma(pm, cz[t], a);
      *(LAS f32x4*)(L + (s & 2) * 8192 + w * 2048 + t * 1024 + lane * 16) = a;
    }
    bar();
    if (w < 4) {
      float e = 0.f, o = 0.f;
#pragma unroll
      for (int q = 0; q < 8; ++q) {
        e += *(LAS float*)(L + (s & 2) * 8192 + q * 2048 + lane * 16 + w * 4);
        o += *(LAS float*)(L + (s & 2) * 8192 + q * 2048 + 1024 + lane * 16 + w * 4);
      }
      st32(pack2(gt * e + bf2f(fb & 0xFFFFu), gt * o + __uint_as_float(fb & 0xFFFF0000u)), ro, oo, 0);
    }
  }
  if (pr == 0 && m == 0 && s0 == NSEG - 3 && threadIdx.x == 0) st64(v2u{r.klo, r.khi}, r, CTRL + L_EPOCH * 128, 0);
}

DEVI int get_role(WS& r, const Params& p, LAS unsigned char* L) {
  if (threadIdx.x == 0) {
    const int xcc = __builtin_amdgcn_s_getreg(20 | (3 << 11)) & 7;
    GAS u64* ctl = (GAS u64*)(p.ws + CTRL);
    const u64 key = (__hip_atomic_load(ctl + L_EPOCH * 16, __ATOMIC_RELAXED, __HIP_MEMORY_SCOPE_AGENT) + 256) & ~255ull;
    int role = xcc * 32 + ((blockIdx.x >> 3) & 31);
    NOUNROLL for (int i = 0; i < 256; ++i) {
      if (__hip_atomic_exchange(ctl + L_CLAIM * 16 + role * 4, key, __ATOMIC_RELAXED, __HIP_MEMORY_SCOPE_AGENT) != key) break;
      role = (role + 1) & 255;
    }
    *(LAS v4u*)(L + LDS_BYTES - 16) = v4u{(u32)role, (u32)key, (u32)(key >> 32), 0};
  }
  bar();
  const v4u v = *(LAS v4u*)(L + LDS_BYTES - 16);
  r.klo = uni(v.y); r.khi = uni(v.z);
  return uni(v.x);
}

__global__ void __launch_bounds__(512, 2) __attribute__((amdgpu_num_vgpr(124))) chain_kernel(Params p) {
  LAS unsigned char* L = (LAS unsigned char*)smem_g;
  WS r; r.rs = __builtin_amdgcn_make_buffer_rsrc(p.ws, 0, WS_USED, 0x00020000);
  if (WARMC) {
    const rsrc_t rc = mk_rsrc((const void*)(__builtin_amdgcn_s_getpc() & ~127ull), CODE_BYTES);
    const int i = threadIdx.x - 64;
    const int o0 = i >= 0 ? i * 128 : OOB, o1 = i >= 0 ? (i + 448) * 128 : OOB;
    const int o2 = (WARMC > 1 && i >= 320) ? RING_BYTES + 131072 + (i - 320) * (2 << 20) : OOB;
    asm volatile("buffer_load_dword v249, %0, %3, 0 offen\n\tbuffer_load_dword v250, %1, %3, 0 offen\n\tbuffer_load_dword v251, %2, %4, 0 offen sc1"
                 ::"v"(o0), "v"(o1), "v"(o2), "s"(rc), "s"(r.rs) : "v249", "v250", "v251");
  }
  const int role = get_role(r, p, L);
  const int x = (role >> 5) & 3, t = role & 31;
  const bool near = (role >= 128) == NEARH;
  int t2 = -1;
  const int sv = PLACE == 2 ? !(x & 1) : PLACE == 5 ? (x & 1) : ((x ^ (x >> 1)) & 1) == 0, xi = x >> 1;
  const int kind = !near ? (t == 7 ? 3 : 4) : sv ? (INTL ? t & 1 : t >> 4) : t < 18 ? 2 : 3;
  const int id = kind < 2 ? xi * 16 + (INTL ? t >> 1 : t & 15) : kind == 2 ? xi * 18 + t : near ? xi * 14 + t - 18 : 28 + x;
  if (kind == 0) role_S(p, r, L, id);
  else if (kind == 1) role_V(p, r, L, id);
  else if (kind == 2) role_R(p, r, L, id);
  else if (kind == 3) { role_X(p, r, L, id); t2 = near ? -1 : 32 + x; }
  else {
    if (t & 8) role_Y(p, r, L, x * 8 + (t & 7), t >> 4);
    else { role_T(p, r, t < 7 ? x * 7 + t : 28 + x * 8 + t - 16); }
    if (t >= 16) { role_F(p, r, L, (x & 1) * 16 + t - 16, x >> 1, (x >> 1) * 18, (x >> 1) * 18 + 18); t2 = t < 25 ? 36 + x * 9 + t - 16 : -1; }
    else if (t >= 8) t2 = x * 8 + t - 8;
    else if (x * 7 + t < 27) role_O(p, r, L, (x * 7 + t) % 9, (x * 7 + t) / 9, 3);
  }
  if (t2 >= 0) role_O2(p, r, L, (t2 % 36) >> 1, t2 & 1, t2 < 36 ? NSEG - 3 : NSEG - 4);
}

extern "C" void kernel_launch(void* const* d_in, const int* in_sizes, int n_in, void* d_out, int out_size, void* d_ws, size_t ws_size, hipStream_t stream) {
  Params p;
  p.x = (const u16*)d_in[0]; p.mem0 = (const u16*)d_in[1]; p.wq = (const u16*)d_in[2]; p.wk = (const u16*)d_in[3]; p.wv = (const u16*)d_in[4]; p.wo = (const u16*)d_in[5];
  p.w1 = (const u16*)d_in[8]; p.b1 = (const u16*)d_in[9]; p.w2 = (const u16*)d_in[10]; p.b2 = (const u16*)d_in[11]; p.gw = (const u16*)d_in[12];
  p.out = (u16*)d_out;
  p.ws = (unsigned char*)(((uintptr_t)d_ws + RING_BYTES - 1) & ~(uintptr_t)(RING_BYTES - 1));
  hipFuncSetAttribute((const void*)chain_kernel, hipFuncAttributeMaxDynamicSharedMemorySize, LDS_BYTES);
  chain_kernel<<<256, 512, LDS_BYTES, stream>>>(p);
}
```

```cpp
#include <hip/hip_runtime.h>
#include <hip/hip_bf16.h>
#include <stdint.h>
#include <string.h>
#pragma clang diagnostic ignored "-Winline-asm"

typedef unsigned u32;
typedef unsigned short u16;
typedef unsigned long long u64;
typedef u32 v2u __attribute__((ext_vector_type(2)));
typedef u32 v4u __attribute__((ext_vector_type(4)));
typedef u32 v8u __attribute__((ext_vector_type(8)));
typedef float f32x2 __attribute__((ext_vector_type(2)));
typedef float f32x4 __attribute__((ext_vector_type(4)));
typedef __bf16 bf16x2 __attribute__((ext_vector_type(2)));
typedef __bf16 bf16x8 __attribute__((ext_vector_type(8)));
typedef __bf16 bf16x16 __attribute__((ext_vector_type(16)));
typedef short s16x4 __attribute__((ext_vector_type(4)));
typedef short s16x8 __attribute__((ext_vector_type(8)));
typedef __amdgpu_buffer_rsrc_t rsrc_t;
#define LAS __attribute__((address_space(3)))
#define GAS __attribute__((address_space(1)))
#define DEVI __device__ __forceinline__

constexpr int DM = 576, NH = 32, NSEG = 64, FF = 4608, NTILE = 36;
constexpr u32 SENT = 0xFFFFFFFFu;
constexpr int SC1 = 16;
constexpr int CHUNK = 16384;
constexpr int SLOT = 42 * CHUNK;
constexpr int P_OFF = 36 * CHUNK, MEM_OFF = 40 * CHUNK;
constexpr int T_HS = 41 * CHUNK + 1024, T_HV = T_HS + SLOT;
constexpr int RING_BYTES = 2 << 20;
constexpr int CTRL = 3 * SLOT;
constexpr int L_EPOCH = 0, L_ABORT = 1, L_PAR = 2, L_RP = 80, L_CLAIM = 416, L_SP = 16, L_VP = 48, L_XP = 128, L_YP = 192, L_FP = 256, L_OP = 320, L_TP = 352, NTB = 64, L_GP = 160;
constexpr int HSEG = 4;
constexpr int CUT = 20;
constexpr int O2S = 56;
constexpr int KQ_OFF = RING_BYTES + 65536;
constexpr int BZ_OFF = KQ_OFF + NSEG * NH * 16384;
constexpr int CZ_OFF = BZ_OFF + NSEG * NTILE * NH * 1024;
constexpr int PX_OFF = CZ_OFF + NSEG * NTILE * NH * 256;
constexpr int FP_OFF = PX_OFF + NSEG * NH * 2560;
constexpr int W1T_OFF = FP_OFF + 2048 * DM * 4;
constexpr int W2T_OFF = W1T_OFF + DM * FF * 2;
constexpr int G_OFF = W2T_OFF + DM * FF * 2;
constexpr int JUNK = G_OFF + 8192;
constexpr int WS_USED = JUNK + SLOT + CHUNK;
static_assert(WS_USED + RING_BYTES <= (256 << 20), "workspace");
constexpr int SPIN_CAP = 1 << 18;
constexpr int LDS_BYTES = 148 * 1024;

struct Params {
  const u16 *x, *mem0, *wq, *wk, *wv, *wo, *w1, *b1, *w2, *b2, *gw;
  u16* out;
  unsigned char* ws;
};

struct WS {
  rsrc_t rs; u32 klo, khi;
  DEVI operator rsrc_t() const { return rs; }
};
DEVI float bf2f(u32 b) { return __uint_as_float(b << 16); }
DEVI u32 pack2(float lo, float hi) { f32x2 v = {lo, hi}; bf16x2 b = __builtin_convertvector(v, bf16x2); return __builtin_bit_cast(u32, b); }
DEVI u32 guard(u32 v) { return v < 0xFFFFFFFEu ? v : 0xFFFFFFFEu; }
DEVI void bar() { asm volatile("s_waitcnt lgkmcnt(0)\n\ts_barrier" ::: "memory"); }
DEVI void drain() { asm volatile("s_waitcnt vmcnt(0)" ::: "memory"); }
DEVI f32x4 smf64(v4u a, v8u b, f32x4 c, int idx) { return __builtin_amdgcn_smfmac_f32_16x16x64_bf16(__builtin_bit_cast(bf16x8, a), __builtin_bit_cast(bf16x16, b), c, idx, 0, 0); }
DEVI f32x4 smf32(v2u a, v4u b, f32x4 c, int idx) { return __builtin_amdgcn_smfmac_f32_16x16x32_bf16(__builtin_bit_cast(s16x4, a), __builtin_bit_cast(s16x8, b), c, idx, 0, 0); }
DEVI f32x4 mfma(v4u a, v4u b, f32x4 c) { return __builtin_amdgcn_mfma_f32_16x16x32_bf16(__builtin_bit_cast(bf16x8, a), __builtin_bit_cast(bf16x8, b), c, 0, 0, 0); }
DEVI v8u cat(v4u a, v4u b) { return __builtin_shufflevector(a, b, 0, 1, 2, 3, 4, 5, 6, 7); }
DEVI v4u lo4(v8u a) { return __builtin_shufflevector(a, a, 0, 1, 2, 3); }
DEVI v4u hi4(v8u a) { return __builtin_shufflevector(a, a, 4, 5, 6, 7); }
template <int CTRL_> DEVI float dpp(float v) { return __uint_as_float(__builtin_amdgcn_update_dpp(0, __float_as_uint(v), CTRL_, 0xF, 0xF, true)); }
DEVI float row16_sum(float v) { v += dpp<0xB1>(v); v += dpp<0x4E>(v); v += dpp<0x141>(v); v += dpp<0x140>(v); return v; }
DEVI float x16_max(float v) { auto r = __builtin_amdgcn_permlane16_swap(__float_as_uint(v), __float_as_uint(v), false, false); return fmaxf(__uint_as_float(r[0]), __uint_as_float(r[1])); }
DEVI float x32_max(float v) { auto r = __builtin_amdgcn_permlane32_swap(__float_as_uint(v), __float_as_uint(v), false, false); return fmaxf(__uint_as_float(r[0]), __uint_as_float(r[1])); }
DEVI float x16_sum(float v) { auto r = __builtin_amdgcn_permlane16_swap(__float_as_uint(v), __float_as_uint(v), false, false); return __uint_as_float(r[0]) + __uint_as_float(r[1]); }
DEVI float x32_sum(float v) { auto r = __builtin_amdgcn_permlane32_swap(__float_as_uint(v), __float_as_uint(v), false, false); return __uint_as_float(r[0]) + __uint_as_float(r[1]); }
DEVI v4u ld128(rsrc_t r, int vo, int so) { return __builtin_amdgcn_raw_buffer_load_b128(r, vo, so, SC1); }
DEVI v2u ld64(rsrc_t r, int vo, int so) { return __builtin_amdgcn_raw_buffer_load_b64(r, vo, so, SC1); }
DEVI u32 ld32(rsrc_t r, int vo, int so) { return __builtin_amdgcn_raw_buffer_load_b32(r, vo, so, SC1); }
DEVI void st128(v4u v, rsrc_t r, int vo, int so) { __builtin_amdgcn_raw_buffer_store_b128(v, r, vo + so, 0, SC1); }
DEVI void st64(v2u v, rsrc_t r, int vo, int so) { __builtin_amdgcn_raw_buffer_store_b64(v, r, vo, so, SC1); }
DEVI void st32(u32 v, rsrc_t r, int vo, int so) { __builtin_amdgcn_raw_buffer_store_b32(v, r, vo, so, SC1); }
DEVI rsrc_t mk_rsrc(const void* ptr, int bytes) { return __builtin_amdgcn_make_buffer_rsrc((void*)ptr, 0, bytes, 0x00020000); }
DEVI v4u pl128(rsrc_t r, int vo, int so) { return __builtin_amdgcn_raw_buffer_load_b128(r, vo, so, 0); }
DEVI u32 pl32(rsrc_t r, int vo, int so) { return __builtin_amdgcn_raw_buffer_load_b32(r, vo, so, 0); }
DEVI u32 umax(u32 a, u32 b) { return a > b ? a : b; }
DEVI u32 mx4(v4u v) { return umax(umax(v.x, v.y), umax(v.z, v.w)); }
DEVI int uni(int v) { return __builtin_amdgcn_readfirstlane(v); }
DEVI int opaque(int v) { asm volatile("" : "+v"(v)); return v; }
DEVI int sopaque(int v) { asm volatile("" : "+s"(v)); return v; }
DEVI void fence_c() { asm volatile("" ::: "memory"); }

constexpr int XSLP = 4, PSLP = 23, RSLP = 26, VBG = 6, VSLP = 8, MAR_R = 8, MAR_C = 8;
constexpr int XUN = 4;
constexpr int TDLY = 900;
#define NOUNROLL _Pragma("clang loop unroll(disable)")
#define SB() __builtin_amdgcn_sched_barrier(0)
constexpr int OOB = 0x7FFFFFF0;
DEVI bool any_sent(u32 m) { return __builtin_amdgcn_ballot_w64(m == SENT) != 0; }
DEVI void set_flag(const WS& r, int vo, int v) { st64(v2u{r.klo + (u32)(v + 2), r.khi}, r, vo, 0); }
DEVI int get_flag(const WS& r, int vo) {
  const v2u v = ld64(r, vo, 0);
  return (v.y == r.khi && v.x - r.klo < 256u) ? (int)(v.x - r.klo) - 2 : -2;
}
DEVI bool give_up(const WS& r, int& rounds) {
  if (++rounds > SPIN_CAP / 256) { set_flag(r, CTRL + L_ABORT * 128, 0); return true; }
  if (uni(get_flag(r, CTRL + L_ABORT * 128)) >= 0) { rounds = SPIN_CAP; return true; }
  return false;
}
#define POLL(BODY)                                                                                   \
  NOUNROLL for (int cnt_ = 1;; ++cnt_) {                                                             \
    bool bad;                                                                                        \
    fence_c();                                                                                       \
    BODY;                                                                                            \
    if (__builtin_expect(!bad, 1)) break;                                                            \
    if (((cnt_ & 255) == 0 || rounds >= SPIN_CAP / 256) && give_up(r, rounds)) break;                \
  }                                                                                                  \
  fence_c();
DEVI u32 rt7() {
  u64 t;
  asm volatile("s_memrealtime %0\n\ts_waitcnt lgkmcnt(0)" : "=s"(t)::"memory");
  return (u32)t << 7;
}
#define HC1 "v_and_b32 %0, 127, v248\n\tv_cmp_ne_u32 vcc, %5, %0\n\ts_cbranch_vccz 2f\n\t"
#define HC4 HC1 HC1 HC1 HC1
#define HCAD HC4 HC4 HC4 HC4
DEVI u32 hint_wait(rsrc_t r, int vo, int so, int step) {
  int cnt = step < 0 ? 0 : 1024;
  u32 hv;
  asm volatile(
      "v_mov_b32 v248, -1\n\ts_nop 3\n\t"
      "1:\n\t"
      "buffer_load_dword v248, %2, %3, %4 offen sc1\n\t"
      HCAD
      "s_sub_u32 %1, %1, 1\n\t"
      "s_cbranch_scc0 1b\n\t"
      "2:\n\t"
      "v_mov_b32 %0, v248"
      : "=&v"(hv), "+s"(cnt)
      : "v"(vo), "s"(r), "s"(so), "s"(step)
      : "memory", "vcc", "scc", "v248");
  return hv;
}
template <int HI>
DEVI void time_wait(u32 tv) {
  int cnt = 64;
  u32 t;
  u64 now;
  asm volatile(
      "1:\n\t"
      "s_memrealtime s[96:97]\n\t"
      "s_sub_u32 %1, %1, 1\n\t"
      "s_cbranch_scc1 2f\n\t"
      "s_waitcnt lgkmcnt(0)\n\t"
      "s_lshl_b32 s96, s96, 7\n\t"
      "v_sub_u32 %0, s96, %3\n\t"
      "v_cmp_gt_i32 s[96:97], 0, %0\n\t"
      "s_cmp_lg_u32 s%c4, 0\n\t"
      "s_cbranch_scc1 1b\n\t"
      "2:\n\t"
      "s_waitcnt lgkmcnt(0)"
      : "=&v"(t), "+s"(cnt), "=&{s[96:97]}"(now)
      : "v"(tv), "i"(96 + HI)
      : "memory", "scc");
}
DEVI int row_max_i(int d) {
  d = max(d, __builtin_amdgcn_update_dpp(d, d, 0xB1, 0xF, 0xF, false)); d = max(d, __builtin_amdgcn_update_dpp(d, d, 0x4E, 0xF, 0xF, false));
  d = max(d, __builtin_amdgcn_update_dpp(d, d, 0x141, 0xF, 0xF, false));
  return d;
}
DEVI int track(int dur, int m) { return (m > 0 && m < (200 << 7)) ? dur + ((m - dur) >> 2) : dur; }
#define DCHK "v_max3_u32 %0, v248, v249, v250\n\tv_max3_u32 %1, v252, v253, v254\n\tv_max3_u32 %0, %0, v251, v255\n\tv_max_u32 %0, %0, %1\n\tv_cmp_eq_u32 vcc, -1, %0\n\t"
#define DC1 DCHK "s_cbranch_vccz 2f\n\t"
#define DC2 DC1 DC1
#define DCAD DC2 DC2 DC2
template <int TWO>
DEVI bool data_wait(rsrc_t r, int vo, int so, int la) {
  int cnt = 4096;
  u32 t0, t1;
  asm volatile(
      "s_waitcnt vmcnt(0)\n\t"
      "v_mov_b32 v248, -1\n\tv_mov_b32 v249, -1\n\tv_mov_b32 v250, -1\n\tv_mov_b32 v251, -1\n\t"
      "v_mov_b32 v252, %6\n\tv_mov_b32 v253, %6\n\tv_mov_b32 v254, %6\n\tv_mov_b32 v255, %6\n\t"
      "1:\n\t"
      "buffer_load_dwordx4 v[248:251], %3, %4, %5 offen sc1\n\t"
      ".if %6\n\t"
      "buffer_load_dwordx4 v[252:255], %3, %4, %5 offen offset:1024 sc1\n\t"
      ".endif\n\t"
      "s_sub_u32 %2, %2, 1\n\t"
      "s_cbranch_scc1 2f\n\t"
      DCAD
      DCHK "s_cbranch_vccnz 1b\n\t"
      "2:\n\t"
      "ds_write_b128 %7, v[248:251]\n\t"
      ".if %6\n\t"
      "ds_write_b128 %7, v[252:255] offset:1024\n\t"
      ".endif"
      : "=&v"(t0), "=&v"(t1), "+s"(cnt)
      : "v"(vo), "s"(r), "s"(so), "i"(-TWO), "v"(la)
      : "memory", "vcc", "scc", "v248", "v249", "v250", "v251", "v252", "v253", "v254", "v255");
  return cnt >= 0;
}
DEVI void wait_prog(const WS& r, int line0, int n, int want, int lane, int& seen, int& rounds) {
  if (__builtin_amdgcn_ballot_w64(seen < want) != 0) {
    const int vo = CTRL + (line0 + (lane < n ? lane : 0)) * 128;
    POLL(seen = get_flag(r, vo); bad = __builtin_amdgcn_ballot_w64(seen < want) != 0; if (bad) __builtin_amdgcn_s_sleep(2));
  }
}

DEVI void wait_prog2(const WS& r, int line0, int n, int want, int lane, int& seen0, int& seen1, int& rounds) {
  const bool hp = (want & 1) && want < HSEG;
  int sn = hp ? seen1 : seen0;
  wait_prog(r, line0 + (hp ? 32 : 0), n, want, lane, sn, rounds);
  if (hp) seen1 = sn; else seen0 = sn;
}
extern __shared__ __attribute__((aligned(16))) unsigned char smem_g[];

template <int WO>
struct WLoad {
  rsrc_t r1, r2; int va, vb;
  DEVI void init(const u16* W1, const u16* W2, int h) {
    r1 = mk_rsrc(W1, DM * 4096 * 2); r2 = mk_rsrc(W2, DM * 4096 * 2);
    const int tid = threadIdx.x;
    va = ((tid >> 4) * 4096 + h * 128) * 2 + (tid & 15) * 16; vb = h * 128 * DM * 2 + tid * 16;
  }
  template <int M, int RD> DEVI void req(v4u (&st)[9]) {
#pragma unroll
    for (int k = 0; k < 9; ++k) st[k] = __builtin_amdgcn_raw_buffer_load_b128(M ? r2 : r1, (M && WO) ? vb : va, (9 * RD + k) * ((M && WO) ? 8192 : 32 * 8192), 0);
    SB();
  }
  template <int M> DEVI void put(LAS unsigned char* L, const v4u (&st)[9]) {
    const int tid = threadIdx.x;
#pragma unroll
    for (int k = 0; k < 9; ++k) {
      const int sw = (M && WO) ? (((k * 512 + tid) / 72) >> 3) & 1 : (tid >> 7) & 1;
      *(LAS v4u*)(L + M * 73728 + k * 8192 + ((tid * 16) ^ (sw << 6))) = st[k];
    }
    SB();
  }
};
DEVI u32 sel2(u32 a, u32 b, u32 sel) { u32 o = __builtin_amdgcn_perm(b, a, sel); asm volatile("" : "+v"(o)); return o; }
DEVI u32 lo2(u32 a, u32 b) { return sel2(a, b, 0x05040100u); }
DEVI u32 hi2(u32 a, u32 b) { return sel2(a, b, 0x07060302u); }
template <int RD>
DEVI void get_w1_sparse(LAS unsigned char* M, int wq, int lane, v8u (&wt)[2][9]) {
  const int n = lane & 15, gb = lane >> 4;
  LAS unsigned char* b = M + ((gb >> 1) + 8 * (gb & 1)) * 256 + ((wq * 64) ^ ((gb & 1) << 6)) + n * 4;
  u32 x[36], y[36];
#pragma unroll
  for (int i = 0; i < 36; ++i) {
    const int d = 32 * (i >> 2) + 16 * (i & 1) + 4 * ((i >> 1) & 1);
    x[i] = *(LAS u32*)(b + d * 256); y[i] = *(LAS u32*)(b + (d + 2) * 256);
  }
#pragma unroll
  for (int i = 0; i < 36; ++i) {
    const int hs = 9 * RD + (i >> 2);
    wt[0][hs >> 1][4 * (hs & 1) + (i & 3)] = lo2(x[i], y[i]);
    wt[1][hs >> 1][4 * (hs & 1) + (i & 3)] = hi2(x[i], y[i]);
  }
}
template <int RD>
DEVI void get_w1_dense(LAS unsigned char* M, int wq, int lane, v4u (&wt)[2][18]) {
  const int n = lane & 15, gb = lane >> 4;
  LAS unsigned char* b = M + 8 * gb * 256 + ((wq * 64) ^ ((gb & 1) << 6)) + n * 4;
  u32 x[36], y[36];
#pragma unroll
  for (int i = 0; i < 36; ++i) {
    const int d = 32 * (i >> 2) + 2 * (i & 3);
    x[i] = *(LAS u32*)(b + d * 256); y[i] = *(LAS u32*)(b + (d + 1) * 256);
  }
#pragma unroll
  for (int i = 0; i < 36; ++i) {
    wt[0][9 * RD + (i >> 2)][i & 3] = lo2(x[i], y[i]);
    wt[1][9 * RD + (i >> 2)][i & 3] = hi2(x[i], y[i]);
  }
}
DEVI int tile_of(int c, int i) { return i < 8 ? 9 * c + (c & 1) + i : (c & 1 ? 9 * c : 9 * c + 8); }
template <int RD>
DEVI void get_w2(LAS unsigned char* M, int c, int lane, v8u (&wt)[9][2]) {
  const int n = lane & 15, gb = lane >> 4;
  const u32 sel = (c & 1) ? 0x07060302u : 0x05040100u;
  LAS unsigned char* b = M + 8 * gb * 1152 + n * 4;
  u32 x[40], y[40];
#pragma unroll
  for (int i = 0; i < 40; ++i) {
    LAS unsigned char* bp = b + ((64 * (tile_of(c, 2 * (i >> 3)) >> 1)) ^ ((gb & 1) << 6));
    const int e = 32 * ((i >> 2) & 1) + 2 * (i & 3);
    x[i] = *(LAS u32*)(bp + e * 1152); y[i] = *(LAS u32*)(bp + (e + 1) * 1152);
  }
#pragma unroll
  for (int i = 0; i < 40; ++i) {
    if (i < 32) { wt[2 * (i >> 3)][RD][i & 7] = lo2(x[i], y[i]); wt[2 * (i >> 3) + 1][RD][i & 7] = hi2(x[i], y[i]); }
    else wt[8][RD][i & 7] = sel2(x[i], y[i], sel);
  }
}
#define LOAD_W(WO, W1, W2, GET, MINE, IDX, MID, LAST)                                                 \
  {                                                                                              \
    WLoad<WO> wl; wl.init(W1, W2, h);                                                            \
    v4u sa[9], sc[9];                                                                            \
    wl.req<0, 1>(sc); wl.req<1, 1>(sa);                                                          \
    asm volatile("s_waitcnt vmcnt(18)" ::: "memory"); bar();                                     \
    GET<0>(L + MINE * 73728, IDX, lane, wt); bar();                                              \
    wl.put<0>(L, sc); bar();                                                                     \
    wl.put<1>(L, sa); MID; drain();                                                              \
    if (MINE == 0) GET<1>(L, IDX, lane, wt);                                                     \
    bar();                                                                                       \
    if (MINE == 1) GET<1>(L + 73728, IDX, lane, wt);                                             \
    LAST;                                                                                        \
    bar();                                                                                       \
  }

constexpr int AIMG = 1088, QIMG = 2 * AIMG;
constexpr int LM_IMG = 0, LM_MEM = 5120, LM_RED = 0, LM_T = 14336, LM_KQ = 16384, LM_BZ = 4096;

DEVI int wait_parity(const WS& r, int& rounds) {
  int v;
  POLL(v = uni(get_flag(r, CTRL + L_PAR * 128)); bad = v < 0; if (bad) __builtin_amdgcn_s_sleep(4));
  return (v & 1) * 8192;
}
DEVI int peek_mem0(const WS& r, int lane) {
  const int v = get_flag(r, CTRL + (L_RP + (lane < NTILE ? lane : 0)) * 128);
  return __builtin_amdgcn_ballot_w64(v < 0) != 0 ? -2 : uni(v);
}
DEVI int wait_mem0(const WS& r, int lane, int early, int& rounds) {
  wait_prog(r, L_RP, NTILE, 0, lane, early, rounds);
  return (uni(early) & 1) * 8192;
}
DEVI int next_slot(int sb, int par) { return sb + SLOT >= par + 3 * SLOT ? par : sb + SLOT; }

DEVI f32x4 zero4() { f32x4 z = {0, 0, 0, 0}; asm volatile("" : "+v"(z)); return z; }
constexpr float SCL = 0.08838834764831845f * 1.4426950408889634f;
DEVI float mulf(float a, float b) { float o = a * b; asm("" : "+v"(o)); return o; }
DEVI f32x2 fold(f32x4 a) { float x = a[0] + a[2]; asm("" : "+v"(x)); float y = a[1] + a[3]; asm("" : "+v"(y)); return f32x2{x, y}; }
template <int TWO>
struct Gather {
  DEVI void run(const WS& r, LAS unsigned char* L, int l16, int c0, int sb, int& rounds) {
    const int so = sb + MEM_OFF + (TWO ? c0 * 1024 : CHUNK), la = (int)(size_t)(L + LM_MEM + c0 * 1024 + l16);
    if (__builtin_expect(!data_wait<TWO>(r, l16, so, la), 0)) {
      NOUNROLL for (;;) {
        rounds += 15;
        if (give_up(r, rounds) || data_wait<TWO>(r, l16, so, la)) break;
      }
    }
  }
};
template <int RSTEP, int SCALE, class F>
DEVI void stage1(LAS unsigned char* L, const v8u (&wt)[2][9], f32x4 a0, f32x4 a1, int l16, int idx, LAS unsigned char* wp, F mid) {
  v4u af[9];
#pragma unroll
  for (int ks = 0; ks < 9; ++ks) af[ks] = *(LAS v4u*)(L + LM_MEM + ks * 1024 + l16);
  SB();
#pragma unroll
  for (int ks = 0; ks < 9; ++ks) {
    a0 = smf64(af[ks], wt[0][ks], a0, idx); a1 = smf64(af[ks], wt[1][ks], a1, idx);
    if (ks == 5) { SB(); mid(); SB(); }
  }
  f32x2 p0 = fold(a0), p1 = fold(a1);
  if (SCALE) { p0.x = mulf(p0.x, SCL); p0.y = mulf(p0.y, SCL); p1.x = mulf(p1.x, SCL); p1.y = mulf(p1.y, SCL); }
  *(LAS u32*)wp = pack2(p0.x, p1.x);
  *(LAS u32*)(wp + RSTEP) = pack2(p0.y, p1.y);
}
DEVI int aimg_w(int wq, int lane) { const int n = lane & 15, g = lane >> 4; return (wq >> 1) * AIMG + (2 * (wq & 1) + (n >> 3)) * 272 + g * 64 + (n & 1) * 32 + ((n & 7) >> 1) * 4; }
DEVI int aimg_r(int lane) { return (lane >> 4) * 272 + (lane & 15) * 16; }
DEVI int kimg_w(int wk, int lane) { const int n = lane & 15, g = lane >> 4; return (wk >> 1) * 1152 + (n >> 2) * 288 + g * 64 + (wk & 1) * 16 + (n & 3) * 4; }
DEVI int kimg_r(int k, int g, int key) { return (k >> 1) * 1152 + g * 288 + key * 32 + (k & 1) * 16; }
DEVI float row16_max_asm(float v) {
  asm volatile("s_nop 1\n\tv_max_f32_dpp %0, %0, %0 quad_perm:[1,0,3,2] row_mask:0xf bank_mask:0xf\n\t"
               "s_nop 1\n\tv_max_f32_dpp %0, %0, %0 quad_perm:[2,3,0,1] row_mask:0xf bank_mask:0xf\n\t"
               "s_nop 1\n\tv_max_f32_dpp %0, %0, %0 row_half_mirror row_mask:0xf bank_mask:0xf\n\t"
               "s_nop 1\n\tv_max_f32_dpp %0, %0, %0 row_mirror row_mask:0xf bank_mask:0xf"
               : "+v"(v));
  return v;
}

DEVI void role_S(const Params& p, const WS& r, LAS unsigned char* L, int h) {
  const int lane = threadIdx.x & 63, w = uni(threadIdx.x >> 6), n = lane & 15, g = lane >> 4;
  const int idx = (lane & 2) ? 0xEEEE : 0x4444, l16 = opaque(lane * 16);
  int rounds = 0;
  if (w < 6) st128(v4u{SENT, SENT, SENT, SENT}, r, P_OFF + (h >> 3) * CHUNK + (h & 7) * 1024 + l16, (w & 1) * 8192 + (w >> 1) * SLOT);
  else st32(SENT, r, lane == 0 ? T_HS + h * 128 : OOB, (w & 1) * 8192);
  v8u wt[2][9];
  int seenR;
  if (w < 4) LOAD_W(0, p.wq, p.wk, get_w1_sparse, 0, w, seenR = peek_mem0(r, lane), )
  else LOAD_W(0, p.wq, p.wk, get_w1_sparse, 1, w - 4, seenR = peek_mem0(r, lane), )
  if (w == 6 && lane == 0) set_flag(r, CTRL + (L_SP + h) * 128, -1);
  const int par = wait_mem0(r, lane, seenR, rounds);
  int sb = par;
  if (w < 2) {
    LAS unsigned char* wa = L + LM_IMG + aimg_w(w, lane);
    const int pvo = P_OFF + (h >> 3) * CHUNK + (h & 7) * 1024 + w * 512;
    const int pxo = pvo + lane * 4;
    const int pmo = n < 4 ? pvo + 256 + (g >> 1) * 32 + (n >> 1) * 16 + (g & 1) * 8 + (n & 1) * 4 : OOB;
    const int cn = (n & 8) | ((n & 3) << 1) | ((n >> 2) & 1);
    const int kmc = ((n & 1) << 2) | (n & 2) | ((n >> 2) & 1);
    const float mk = n < 8 ? 1.f : 0.f;
    int sprev = par + 2 * SLOT;
    for (int s = sopaque(-1); s < NSEG; ++s) {
      const f32x4 z0 = zero4(), z1 = zero4();
      f32x4 sa = zero4(), sbb = zero4(), sm = zero4();
      LAS unsigned char* kq = L + LM_KQ + (s & 1) * 16384;
      v4u kf[2][4];
      const int so = s < 0 ? JUNK : sb;
      Gather<1> ga;
      ga.run(r, L, l16, 2 * w, sb, rounds);
      bar();
      stage1<16, 1>(L, wt, z0, z1, l16, idx, wa, [&]() {
#pragma unroll
        for (int k = 0; k < 8; ++k) kf[k >> 2][k & 3] = *(LAS v4u*)(kq + k * 1024 + (g * 16 + cn) * 16);
      });
      bar();
      v4u qa[2], km[4];
#pragma unroll
      for (int k = 0; k < 2; ++k) qa[k] = *(LAS v4u*)(L + LM_IMG + k * AIMG + aimg_r(lane ^ w));
#pragma unroll
      for (int k = 0; k < 4; ++k) km[k] = *(LAS v4u*)(L + LM_IMG + QIMG + kimg_r(k, g, kmc));
      SB();
      sa = smf64(qa[0], cat(kf[0][0], kf[0][1]), sa, idx);
      sbb = smf64(qa[0], cat(kf[1][0], kf[1][1]), sbb, idx);
      sa = smf64(qa[1], cat(kf[0][2], kf[0][3]), sa, idx);
      sm = smf64(qa[0], cat(km[0], km[1]), sm, idx);
      sbb = smf64(qa[1], cat(kf[1][2], kf[1][3]), sbb, idx);
      sm = smf64(qa[1], cat(km[2], km[3]), sm, idx);
      SB();
      const float va = sa[0] + sa[2], vb = sbb[0] + sbb[2], vm = sm[0] + sm[2];
      const float mx = row16_max_asm(fmaxf(fmaxf(va, vb), vm));
      const float ea = __builtin_amdgcn_exp2f(va - mx), eb = __builtin_amdgcn_exp2f(vb - mx), em = __builtin_amdgcn_exp2f(vm - mx);
      const float em2 = dpp<0x104>(em);
      const float inv = __builtin_amdgcn_rcpf(row16_sum(__builtin_fmaf(em, mk, ea + eb)));
      st32(guard(pack2(ea * inv, eb * inv)), r, pxo, so);
      st32(guard(pack2(em * inv, em2 * inv)), r, pmo, so);
      SB();
      *(LAS u32*)(L + LM_T + w * 4) = rt7();

      st32(SENT, r, pxo, sprev); st32(SENT, r, pmo, sprev);
      if (s >= 0) __builtin_amdgcn_s_sleep(PSLP - 8);
      sprev = s < 0 ? sprev : sb;
      sb = s < 0 ? sb : next_slot(sb, par);
    }
  } else if (w < 4) {
    LAS unsigned char* wa = L + LM_IMG + aimg_w(w, lane);
    const int m = w - 2, qi = 2 * n + m;
    const int po = (h * 2 + m) * 1280;
    for (int s = sopaque(-1); s < NSEG; ++s) {
      const f32x4 z0 = zero4(), z1 = zero4();
      bar();
      stage1<16, 1>(L, wt, z0, z1, l16, idx, wa, []() {});
      drain();
      bar();
      if (s < NSEG - 1) __builtin_amdgcn_s_sleep(XSLP);
      LAS unsigned char* kq = L + LM_KQ + (s & 1) * 16384 + l16;
      f32x4 xa = {0, 0, 0, 0}, xb = {0, 0, 0, 0}, xm = {0, 0, 0, 0};
#pragma unroll
      for (int k = 0; k < 4; ++k) {
        const v4u qf = *(LAS v4u*)(kq + 8192 + (m * 4 + k) * 1024);
        xa = mfma(*(LAS v4u*)(kq + k * 1024), qf, xa);
        xb = mfma(*(LAS v4u*)(kq + (4 + k) * 1024), qf, xb);
        xm = mfma(*(LAS v4u*)(L + LM_IMG + QIMG + kimg_r(k, g, n & 7)), qf, xm);
      }
      float mx = fmaxf(fmaxf(xm[0], xm[1]), fmaxf(xm[2], xm[3]));
#pragma unroll
      for (int k = 0; k < 4; ++k) {
        xa[k] = (8 * g + 2 * k <= qi) ? xa[k] : -INFINITY;
        xb[k] = (8 * g + 2 * k + 1 <= qi) ? xb[k] : -INFINITY;
        mx = fmaxf(mx, fmaxf(xa[k], xb[k]));
      }
      mx = x32_max(x16_max(mx));
      float sum = 0.f, summ = 0.f;
#pragma unroll
      for (int k = 0; k < 4; ++k) {
        xa[k] = __builtin_amdgcn_exp2f((xa[k] - mx) * SCL); xb[k] = __builtin_amdgcn_exp2f((xb[k] - mx) * SCL); xm[k] = __builtin_amdgcn_exp2f((xm[k] - mx) * SCL);
        sum += xa[k] + xb[k]; summ += xm[k];
      }
      sum += g < 2 ? summ : 0.f;
      const float inv = __builtin_amdgcn_rcpf(x32_sum(x16_sum(sum)));
      v4u px; v2u pm2;
#pragma unroll
      for (int k = 0; k < 4; ++k) px[k] = pack2(xa[k] * inv, xb[k] * inv);
      pm2.x = pack2(xm[0] * inv, xm[1] * inv); pm2.y = pack2(xm[2] * inv, xm[3] * inv);
      const int so = s < 0 ? JUNK : PX_OFF + s * NH * 2560;
      st128(px, r, po + l16, so);
      st64(pm2, r, g < 2 ? po + 1024 + n * 16 + g * 8 : OOB, so);
    }
    drain();
  } else if (w < 6) {
    LAS unsigned char* wa = L + LM_IMG + QIMG + kimg_w(w - 4, lane);
    for (int s = sopaque(-1); s < NSEG; ++s) {
      const f32x4 z0 = zero4(), z1 = zero4();
      Gather<1> ga;
      ga.run(r, L, l16, 2 * w - 4, sb, rounds);
      bar();
      stage1<32, 0>(L, wt, z0, z1, l16, idx, wa, []() {});
      bar();
      if (s >= 0) __builtin_amdgcn_s_sleep(PSLP);
      sb = s < 0 ? sb : next_slot(sb, par);
    }
  } else if (w == 6) {
    LAS unsigned char* wa = L + LM_IMG + QIMG + kimg_w(2, lane);
    for (int s = sopaque(-1); s < NSEG; ++s) {
      const f32x4 z0 = zero4(), z1 = zero4();
      Gather<0> ga;
      ga.run(r, L, l16, 8, sb, rounds);
      bar();
      stage1<32, 0>(L, wt, z0, z1, l16, idx, wa, []() {});
      bar();
      if (s > 0 && lane == 0) set_flag(r, CTRL + (L_SP + h) * 128, s - 1);
      if (s >= 0) __builtin_amdgcn_s_sleep(PSLP);
      sb = s < 0 ? sb : next_slot(sb, par);
    }
  } else {
    LAS unsigned char* wa = L + LM_IMG + QIMG + kimg_w(3, lane);
    int seen0 = -2, dur = 60 << 7;
    auto fetch = [&](int s) {
      wait_prog(r, L_XP + h, 1, s, lane, seen0, rounds);
      v4u t[16];
#pragma unroll
      for (int k = 0; k < 16; ++k) t[k] = ld128(r, l16 + (k & 3) * 1024, KQ_OFF + (s * NH + h) * 16384 + (k >> 2) * 4096);
#pragma unroll
      for (int k = 0; k < 16; ++k) *(LAS v4u*)(L + LM_KQ + (s & 1) * 16384 + l16 + k * 1024) = t[k];
    };
    const int ho = lane == 0 ? T_HS + h * 128 : OOB;
    { const u32 t = ld32(r, KQ_OFF + (3 * NH + h) * 16384 + l16, 0); asm volatile("" ::"v"(t)); }
    for (int s = sopaque(-1); s < NSEG; ++s) {
      const f32x4 z0 = zero4(), z1 = zero4();
      bar();
      const u32 tb = rt7();
      st32(((tb + dur) & ~127u) | (s & 127), r, ho, par);
      stage1<32, 0>(L, wt, z0, z1, l16, idx, wa, []() {});
      bar();
      if (s + 1 < NSEG) fetch(s + 1);
      if (s >= 0) dur = track(dur, max((int)(uni(*(LAS u32*)(L + LM_T)) - tb), (int)(uni(*(LAS u32*)(L + LM_T + 4)) - tb)));
    }
  }
  bar();
  if (w == 6 && lane == 0) set_flag(r, CTRL + (L_SP + h) * 128, NSEG - 1);
}

template <int RD>
DEVI void get_v1(LAS unsigned char* M, int w, int lane, v8u (&w1)[9]) {
  const int n = lane & 15, gb = lane >> 4;
  const u32 sel = (w & 1) ? 0x07060302u : 0x05040100u;
  LAS unsigned char* b = M + ((gb >> 1) + 8 * (gb & 1)) * 256 + (((w >> 1) * 64) ^ ((gb & 1) << 6)) + n * 4;
  u32 x[36], y[36];
#pragma unroll
  for (int i = 0; i < 36; ++i) {
    const int d = 32 * (i >> 2) + 16 * (i & 1) + 4 * ((i >> 1) & 1);
    x[i] = *(LAS u32*)(b + d * 256); y[i] = *(LAS u32*)(b + (d + 2) * 256);
  }
#pragma unroll
  for (int i = 0; i < 36; ++i) {
    const int hs = 9 * RD + (i >> 2);
    w1[hs >> 1][4 * (hs & 1) + (i & 3)] = sel2(x[i], y[i], sel);
  }
}
template <int RD>
DEVI void get_v2(LAS unsigned char* M, int w, int lane, v8u (&w2)[5][2]) {
  const int n = lane & 15, gb = lane >> 4;
  const u32 sel = (w & 1) ? 0x07060302u : 0x05040100u;
  LAS unsigned char* b = M + 8 * gb * 1152 + n * 4;
  u32 x[24], y[24];
#pragma unroll
  for (int i = 0; i < 24; ++i) {
    LAS unsigned char* bp = b + ((64 * (i < 16 ? 2 * w + (i >> 3) : 16 + ((w >> 1) & 1))) ^ ((gb & 1) << 6));
    const int e = 32 * ((i >> 2) & 1) + 2 * (i & 3);
    x[i] = *(LAS u32*)(bp + e * 1152); y[i] = *(LAS u32*)(bp + (e + 1) * 1152);
  }
#pragma unroll
  for (int i = 0; i < 24; ++i) {
    if (i < 16) { w2[2 * (i >> 3)][RD][i & 7] = lo2(x[i], y[i]); w2[2 * (i >> 3) + 1][RD][i & 7] = hi2(x[i], y[i]); }
    else w2[4][RD][i & 7] = sel2(x[i], y[i], sel);
  }
}
template <int GATHER, int FIVE, int HINT>
DEVI void v_loop(const Params& p, const WS& r, LAS unsigned char* L, int h, int w, const v8u (&w1)[9], const v8u (&w2)[5][2], int seenR) {
  const int lane = threadIdx.x & 63, n = lane & 15, g = lane >> 4;
  const int idx = (lane & 2) ? 0xEEEE : 0x4444, l16 = opaque(lane * 16);
  int rounds = 0;
  const int par = wait_mem0(r, lane, seenR, rounds);
  LAS unsigned char* wa = L + LM_IMG + aimg_w(w >> 1, lane) + (w & 1) * 2;
  LAS unsigned char* img = L + LM_IMG + aimg_r(lane);
  const int lr0 = (4 * w + g) * CHUNK + h * 256 + n * 16, lr1 = g == 0 ? (28 + w) * CHUNK + h * 256 + n * 16 : OOB;
  const int lz0 = (4 * w + g) * 8192 + h * 256 + n * 16, lz1 = g == 0 ? (28 + w) * 8192 + h * 256 + n * 16 : OOB;
  const int ho = (w == 7 && lane == 0) ? T_HV + h * 128 : OOB;
  int sb = par, sprev = par + 2 * SLOT, dur = 50 << 7;
  u32 tb = 0;
  for (int s = sopaque(0); s < NSEG; ++s) {
    f32x4 a1 = zero4(), a[5];
#pragma unroll
    for (int i = 0; i < 4 + FIVE; ++i) a[i] = zero4();
    const int so0 = opaque(lr0 + sb), so1 = opaque(lr1 + sb);
    Gather<GATHER == 2> ga;
    if (GATHER) ga.run(r, L, l16, GATHER == 2 ? 2 * w : 8, sb, rounds);
    else drain();
    if (HINT && s > 0) {
      const int m = row_max_i((int)(*(LAS u32*)(L + LM_T + (lane & 7) * 4) - tb));
      dur = track(dur, uni(m));
    }
    bar();
    if (HINT) tb = rt7();
    v4u af[9];
#pragma unroll
    for (int ks = 0; ks < 9; ++ks) af[ks] = *(LAS v4u*)(L + LM_MEM + ks * 1024 + l16);
    SB();
    if (HINT) { st32(((tb + dur) & ~127u) | s, r, ho, par); SB(); }
#pragma unroll
    for (int ks = 0; ks < 9; ++ks) a1 = smf64(af[ks], w1[ks], a1, idx);
    const f32x2 f1 = fold(a1);
    const u32 v2 = pack2(f1.x, f1.y);
    *(LAS u16*)wa = (u16)v2;
    *(LAS u16*)(wa + 16) = (u16)(v2 >> 16);
    bar();
    v4u va[2];
#pragma unroll
    for (int k = 0; k < 2; ++k) va[k] = *(LAS v4u*)(img + k * AIMG);
    auto fin = [&](int i) { const f32x2 f = fold(a[i]); return guard(pack2(f.x, f.y)); };
    auto xch = [&](u32 u0, u32 u1, u32 u2, u32 u3) {
      const auto e0 = __builtin_amdgcn_permlane32_swap(u0, u2, false, false);
      const auto e1 = __builtin_amdgcn_permlane32_swap(u1, u3, false, false);
      const auto f0 = __builtin_amdgcn_permlane16_swap(e0[0], e1[0], false, false);
      const auto f1 = __builtin_amdgcn_permlane16_swap(e0[1], e1[1], false, false);
      return v4u{f0[0], f0[1], f1[0], f1[1]};
    };
    v4u o1;
    if (FIVE) a[4] = smf64(va[0], w2[4][0], a[4], idx);
    a[0] = smf64(va[0], w2[0][0], a[0], idx);
    if (FIVE) a[4] = smf64(va[1], w2[4][1], a[4], idx);
    else a[2] = smf64(va[0], w2[2][0], a[2], idx);
    a[0] = smf64(va[1], w2[0][1], a[0], idx);
    if (FIVE) a[2] = smf64(va[0], w2[2][0], a[2], idx);
    a[1] = smf64(va[0], w2[1][0], a[1], idx);
    SB();
    u32 u4;
    if (FIVE) u4 = fin(4);
    a[2] = smf64(va[1], w2[2][1], a[2], idx);
    SB();
    if (FIVE) { o1 = xch(u4, u4, u4, u4); st128(o1, r, so1, 0); }
    a[3] = smf64(va[0], w2[3][0], a[3], idx);
    a[1] = smf64(va[1], w2[1][1], a[1], idx);
    SB();
    const u32 u0 = fin(0);
    a[3] = smf64(va[1], w2[3][1], a[3], idx);
    SB();
    const u32 u2 = fin(2);
    const auto e0 = __builtin_amdgcn_permlane32_swap(u0, u2, false, false);
    SB();
    const auto e1 = __builtin_amdgcn_permlane32_swap(fin(1), fin(3), false, false);
    const auto f0 = __builtin_amdgcn_permlane16_swap(e0[0], e1[0], false, false);
    const auto f1_ = __builtin_amdgcn_permlane16_swap(e0[1], e1[1], false, false);
    const v4u o0 = v4u{f0[0], f0[1], f1_[0], f1_[1]};
    st128(o0, r, so0, 0);
    SB();
    *(LAS u32*)(L + LM_T + w * 4) = rt7();

    __builtin_amdgcn_s_sleep(VBG);
    const int zo = CZ_OFF + s * NTILE * 8192;
    st128(o0, r, lz0, zo);
    st128(v4u{SENT, SENT, SENT, SENT}, r, lr0, sprev);
    if (FIVE) { st128(o1, r, lz1, zo); st128(v4u{SENT, SENT, SENT, SENT}, r, lr1, sprev); }
    if (w == 6 && s > 0 && lane == 0) set_flag(r, CTRL + (L_VP + h) * 128, s - 1);
    if (GATHER) __builtin_amdgcn_s_sleep(VSLP);
    sprev = sb;
    sb = next_slot(sb, par);
  }
  drain();
}

DEVI void role_V(const Params& p, const WS& r, LAS unsigned char* L, int h) {
  const int lane = threadIdx.x & 63, w = uni(threadIdx.x >> 6);
  v8u w1[9], w2[5][2];
  int seenR;
  {
    const int o = h * 256 + (lane & 15) * 16;
#pragma unroll
    for (int q = 0; q < 6; ++q) st128(v4u{SENT, SENT, SENT, SENT}, r, (4 * w + (lane >> 4)) * CHUNK + o, (q & 1) * 8192 + (q >> 1) * SLOT);
    if (w < 6) st128(v4u{SENT, SENT, SENT, SENT}, r, (32 + (lane >> 4)) * CHUNK + o, (w & 1) * 8192 + (w >> 1) * SLOT);
    else st32(SENT, r, lane == 0 ? T_HV + h * 128 : OOB, (w & 1) * 8192);
  }
  {
    WLoad<1> wl; wl.init(p.wv, p.wo, h);
    v4u sa[9], sc[9];
    wl.req<0, 1>(sc); wl.req<1, 1>(sa);
    asm volatile("s_waitcnt vmcnt(18)" ::: "memory"); bar();
    get_v1<0>(L, w, lane, w1); get_v2<0>(L + 73728, w, lane, w2); bar();
    wl.put<0>(L, sc); bar();
    wl.put<1>(L, sa); seenR = peek_mem0(r, lane); drain(); get_v1<1>(L, w, lane, w1); bar();
    get_v2<1>(L + 73728, w, lane, w2); bar();
  }
  if (w == 6 && lane == 0) set_flag(r, CTRL + (L_VP + h) * 128, -1);
  if (w < 4) { __builtin_amdgcn_s_setprio(1); v_loop<2, 0, 0>(p, r, L, h, w, w1, w2, seenR); }
  else if (w == 4) v_loop<1, 1, 0>(p, r, L, h, w, w1, w2, seenR);
  else if (w < 7) v_loop<0, 1, 0>(p, r, L, h, w, w1, w2, seenR);
  else v_loop<0, 1, 1>(p, r, L, h, w, w1, w2, seenR);
  bar();
  if (w == 6 && lane == 0) set_flag(r, CTRL + (L_VP + h) * 128, NSEG - 1);
}

DEVI u64 cyc() { u64 t; asm volatile("s_memtime %0\n\ts_waitcnt lgkmcnt(0)" : "=s"(t)::"memory"); return t; }

DEVI void role_R(const Params& p, const WS& r, LAS unsigned char* L, int j) {
  const int lane = threadIdx.x & 63, w = uni(threadIdx.x >> 6), n = lane & 15, g = lane >> 4;
  int rounds = 0;
  if (j == 0 && w == 4) {
    u32 best[2] = {~0u, ~0u};
    if (lane == 0) {
      NOUNROLL for (int it = 0; it < 5; ++it)
        for (int q = 0; q < 2; ++q) {
          GAS u32* a = (GAS u32*)(p.ws + MEM_OFF + CHUNK + q * 8192 + 7168);
          const u64 t0 = cyc();
          u32 v = __hip_atomic_fetch_add(a, 1u, __ATOMIC_RELAXED, __HIP_MEMORY_SCOPE_AGENT);
          asm volatile("s_waitcnt vmcnt(0)" ::"v"(v) : "memory");
          const u32 d = (u32)(cyc() - t0);
          best[q] = d < best[q] ? d : best[q];
        }
      set_flag(r, CTRL + L_PAR * 128, best[1] < best[0] ? 1 : 0);
    }
  }
  const int par = wait_parity(r, rounds);
  int sb = par;
  if (w < 4) {
    const int idx = (lane & 2) ? 0xEEEE : 0x4444;
    const int pxo = opaque(P_OFF + w * CHUNK + (g >> 1) * 1024 + (n >> 3) * 512 + ((n >> 2) & 1) * 128 + (n & 1) * 64 + (g & 1) * 32 + ((n >> 1) & 1) * 16);
    const int pmo = P_OFF + w * CHUNK + g * 1024 + (n >> 3) * 512 + 256 + (n & 7) * 8;
    const int co = opaque(j * CHUNK + w * 2048 + lane * 16);
    const int redw = LM_RED + w * 512 + (g & 1) * 256 + (n >> 3) * 64 + (g >> 1) * 32 + (n & 7) * 4;
    const int hio = (lane < 32 ? T_HS : T_HV) + (8 * w + ((lane >> 2) & 7)) * 128;
    for (int s = sopaque(-1); s < NSEG - 1; ++s) {
      v4u bz[8];
#pragma unroll
      for (int q = 0; q < 8; ++q) bz[q] = *(LAS v4u*)(L + LM_BZ + (s & 1) * 32768 + (8 * w + q) * 1024 + lane * 16);
      if (s > 0) __builtin_amdgcn_s_sleep(RSLP);
      if (s == 0) { int seen = -2; wait_prog(r, L_SP, 64, -1, lane, seen, rounds); }
      if (s == 0) {
        int seen = -2;
        wait_prog(r, L_YP + 8 * w, 8, 0, lane, seen, rounds);
#pragma unroll
        for (int q = 0; q < 8; ++q) bz[q] = ld128(r, (8 * w + q) * 1024 + lane * 16, BZ_OFF + j * 32768);
      }
      const u32 hv = hint_wait(r, hio, par, s);
      const u32 tv = s < 0 ? rt7() : hv + (lane < 32 ? MAR_R : MAR_C) * 128;
      LAS unsigned char* rw = (LAS unsigned char*)(size_t)opaque((int)(size_t)(L + (s & 1) * 2048 + redw));
      auto attempt = [&](bool first) {
        v4u px[4]; v2u pm[2];
        if (first) time_wait<0>(tv);
#pragma unroll
        for (int q = 0; q < 4; ++q) { px[q] = ld128(r, pxo + (q & 1) * 2048, sb + (q >> 1) * 4096); SB(); }
#pragma unroll
        for (int q = 0; q < 2; ++q) { pm[q] = ld64(r, pmo, sb + q * 4096); SB(); }
        if (first) time_wait<1>(tv);
        const v4u c0 = ld128(r, co, sb);
        SB();
        const v4u c1 = ld128(r, co + 1024, sb);
        f32x4 a = zero4();
        u32 mp = 0;
        SB();
#pragma unroll
        for (int q = 0; q < 4; ++q) {
          a = smf64(px[q], cat(bz[2 * q], bz[2 * q + 1]), a, idx);
          SB();
          mp = umax(mp, mx4(px[q]));
          asm volatile("" : "+v"(mp));
          SB();
        }
        mp = umax(mp, umax(umax(pm[0].x, pm[0].y), umax(pm[1].x, pm[1].y)));
        asm volatile("" : "+v"(mp));
        SB();
        a = smf32(pm[0], c0, a, idx);
        SB();
        a = smf32(pm[1], c1, a, idx);
        SB();
        mp = umax(mp, umax(mx4(c0), mx4(c1)));
        const f32x2 fa = fold(a);
        *(LAS float*)rw = fa.x;
        *(LAS float*)(rw + 128) = fa.y;
        return any_sent(mp);
      };
      if (__builtin_expect(attempt(true), 0) && s >= 0) {
        POLL(bad = attempt(false));
      }
      bar();
      __builtin_amdgcn_s_sleep(4);
      sb = s < 0 ? sb : next_slot(sb, par);
    }
  } else {
    const int mo = MEM_OFF + (j >> 5) * CHUNK + (j & 31) * 256 + lane * 4, l16 = opaque(lane * 16);
    int snext = par + SLOT, seen0 = -2, seen1 = -2;
    auto fetch = [&](int s) {
      if (s > 0) wait_prog2(r, L_YP + 8 * (w - 4), 8, s, lane, seen0, seen1, rounds);
      v4u t[8];
#pragma unroll
      for (int q = 0; q < 8; ++q) t[q] = ld128(r, l16 + (q & 3) * 1024, BZ_OFF + (s * NTILE + j) * 32768 + (w - 4) * 8192 + (q >> 2) * 4096);
#pragma unroll
      for (int q = 0; q < 8; ++q) *(LAS v4u*)(L + LM_BZ + (s & 1) * 32768 + (8 * (w - 4) + q) * 1024 + l16) = t[q];
    };
    if (w == 4) {
      const u16* src = p.mem0 + ((lane >> 4) * 2 + ((lane >> 2) & 1)) * DM + 32 * (j >> 1) + (j & 1) + 16 * ((lane >> 3) & 1) + 4 * (lane & 3);
      st32(guard((u32)src[0] | ((u32)src[2] << 16)), r, mo, sb);
      st32(SENT, r, mo, par + SLOT);
      st32(SENT, r, mo, par + 2 * SLOT);
      drain();
      if (lane == 0) set_flag(r, CTRL + (L_RP + j) * 128, par >> 13);
      __builtin_amdgcn_s_setprio(2);
      for (int s = sopaque(-1); s < NSEG - 1; ++s) {
        LAS unsigned char* red = L + LM_RED + (s & 1) * 2048 + lane * 8;
        const int so = s < 0 ? JUNK : snext;
        fetch(s + 1);
        drain();
        bar();
        f32x2 t[4];
#pragma unroll
        for (int q = 0; q < 4; ++q) t[q] = *(LAS f32x2*)(red + q * 512);
        const f32x2 u = (t[0] + t[1]) + (t[2] + t[3]);
        st32(guard(pack2(u.x, u.y)), r, mo, so);
        SB();
        if (s >= 0) {
          st32(SENT, r, mo, sb);
          sb = snext;
          snext = next_slot(snext, par);
        }
      }
    } else {
      for (int s = sopaque(-1); s < NSEG - 1; ++s) {
        fetch(s + 1);
        bar();
        __builtin_amdgcn_s_sleep(4);
      }
    }
  }
}

DEVI int ximg_rd(int lane) { const int n = lane & 15, g = lane >> 4; return n * 64 + (g ^ ((n >> 3) << 1)) * 16; }
DEVI int ximg_piece(int lane) { return ((lane & 3) ^ ((lane >> 5) << 1)) * 16; }
template <int DEPTH>
DEVI void proj32(LAS unsigned char* xb, const v4u (&wt)[2][18], v4u (&o)[2]) {
  f32x4 acc[2][2];
#pragma unroll
  for (int i = 0; i < 4; ++i) acc[i >> 1][i & 1] = zero4();
  v4u xf[DEPTH][2];
#pragma unroll
  for (int ks = 0; ks < DEPTH - 1; ++ks) { xf[ks][0] = *(LAS v4u*)(xb + ks * 1024); xf[ks][1] = *(LAS v4u*)(xb + (18 + ks) * 1024); }
  SB();
#pragma unroll
  for (int ks = 0; ks < 18; ++ks) {
    const int kn = ks + DEPTH - 1;
    if (kn < 18) { xf[kn % DEPTH][0] = *(LAS v4u*)(xb + kn * 1024); xf[kn % DEPTH][1] = *(LAS v4u*)(xb + (18 + kn) * 1024); }
    SB();
#pragma unroll
    for (int m = 0; m < 2; ++m) {
      acc[0][m] = mfma(wt[0][ks], xf[ks % DEPTH][m], acc[0][m]);
      acc[1][m] = mfma(wt[1][ks], xf[ks % DEPTH][m], acc[1][m]);
    }
    SB();
  }
#pragma unroll
  for (int m = 0; m < 2; ++m)
#pragma unroll
    for (int q = 0; q < 4; ++q) o[m][q] = pack2(acc[0][m][q], acc[1][m][q]);
}

struct XStage {
  rsrc_t rx; int go, lo; v4u st[9];
  DEVI void init(const Params& p, int tok, int q, int lane) {
    rx = mk_rsrc(p.x, 2048 * DM * 2);
    go = tok * (DM * 2) + (q & 1) * 9 * 64 + ximg_piece(lane);
    lo = q * 9 * 1024 + lane * 16;
  }
  DEVI void req(int s) {
#pragma unroll
    for (int k = 0; k < 9; ++k) st[k] = pl128(rx, go + k * 64, (s < NSEG ? s : NSEG - 1) * 32 * DM * 2);
  }
  DEVI void put(LAS unsigned char* L) {
#pragma unroll
    for (int k = 0; k < 9; ++k) *(LAS v4u*)(L + lo + k * 1024) = st[k];
  }
};

DEVI int seg_of(int hp, int i) { return i < HSEG / 2 ? 2 * i + hp : i + HSEG / 2; }

DEVI void role_X(const Params& p, const WS& r, LAS unsigned char* L, int h) {
  const int lane = threadIdx.x & 63, w = uni(threadIdx.x >> 6);
  const int rd = ximg_rd(lane), oo = KQ_OFF + h * 16384 + (w >> 2) * 8192 + (w & 3) * 1024 + lane * 16;
  constexpr int cnt = NSEG;
  v4u wt[2][18];
  if (w < 4) {
    XStage xs; xs.init(p, 2 * (lane >> 2) + (w >> 1), w, lane);
    xs.req(0);
    LOAD_W(0, p.wk, p.wq, get_w1_dense, 0, w, , xs.put(L); xs.req(1))
    __builtin_amdgcn_s_setprio(1);
    const int fo = (w == 0 && lane == 0) ? CTRL + (L_XP + h) * 128 : OOB;
    for (int it = sopaque(0); it < cnt; ++it) {
      const int s = it;
      v4u o[2];
      proj32<3>(L + (it & 1) * 36864 + rd, wt, o);
      st128(o[0], r, oo, s * NH * 16384);
      st128(o[1], r, oo + 4096, s * NH * 16384);
      SB();
      xs.put(L + ((it + 1) & 1) * 36864);
      SB();
      xs.req(it + 2);
      asm volatile("s_waitcnt vmcnt(9)" ::: "memory");
      bar();
      set_flag(r, fo, it < XUN || it + 1 == cnt ? s : it - 1);
    }
    __builtin_amdgcn_s_setprio(0);
  } else {
    LOAD_W(0, p.wk, p.wq, get_w1_dense, 1, w & 3, , )
    for (int it = sopaque(0); it < cnt; ++it) {
      const int s = it;
      v4u o[2];
      proj32<4>(L + (it & 1) * 36864 + rd, wt, o);
      st128(o[0], r, oo, s * NH * 16384);
      st128(o[1], r, oo + 4096, s * NH * 16384);
      if (it < XUN || it + 1 == cnt) drain();
      asm volatile("s_waitcnt vmcnt(2)" ::: "memory");
      bar();
    }
  }
}

DEVI void role_Y(const Params& p, const WS& r, LAS unsigned char* L, int h, int hp) {
  const int lane = threadIdx.x & 63, w = uni(threadIdx.x >> 6);
  const int cnt = hp ? HSEG / 2 : NSEG - HSEG / 2;
  constexpr int VB = 73728;
  if (w < 4) {
    v4u wt[2][18];
    {
      const int row = lane >> 2;
      XStage xs; xs.init(p, 8 * (row >> 2) + (row & 3) + 4 * (w >> 1), w, lane);
      xs.req(seg_of(hp, 0));
      LOAD_W(1, p.wv, p.wo, get_w1_dense, 0, w, , xs.put(L))
    }
    const int rd = ximg_rd(lane);
    const int fo = (w == 0 && lane == 0) ? CTRL + (L_YP + hp * 32 + h) * 128 : OOB;
    for (int it = sopaque(0); it < cnt; ++it) {
      v4u o[2];
      proj32<4>(L + (it & 1) * 36864 + rd, wt, o);
      *(LAS v4u*)(L + VB + (it & 1) * 8192 + w * 1024 + lane * 16) = o[0];
      *(LAS v4u*)(L + VB + (it & 1) * 8192 + 4096 + w * 1024 + lane * 16) = o[1];
      bar();
      if (it == 0) { bar(); set_flag(r, fo, seg_of(hp, 0)); }
      if (it > 1) set_flag(r, fo, seg_of(hp, it - 1));
    }
    bar();
    set_flag(r, fo, seg_of(hp, cnt - 1));
  } else {
    const int c = w - 4, row = lane >> 2;
    XStage xs; xs.init(p, 8 * (row >> 2) + (row & 3) + 4 * (c >> 1), c, lane);
    xs.req(seg_of(hp, 1));
    v8u wt[9][2];
    LOAD_W(1, p.wv, p.wo, get_w2, 1, c, , )
    __builtin_amdgcn_s_setprio(1);
    xs.put(L + 36864);
    xs.req(seg_of(hp, 2));
    bar();
    const int oo = BZ_OFF + h * 1024 + lane * 16;
    const int t0o = tile_of(c, 0) * 32768, t8o = tile_of(c, 8) * 32768;
    for (int it = sopaque(0); it < cnt; ++it) {
      const int s = seg_of(hp, it);
      v4u vf[2][4];
#pragma unroll
      for (int k = 0; k < 8; ++k) vf[k >> 2][k & 3] = *(LAS v4u*)(L + VB + (it & 1) * 8192 + k * 1024 + lane * 16);
#pragma unroll
      for (int i = 0; i < 9; ++i) {
        f32x4 a0 = {0, 0, 0, 0}, a1 = {0, 0, 0, 0};
#pragma unroll
        for (int k = 0; k < 2; ++k) {
          a0 = mfma(vf[0][2 * k], lo4(wt[i][k]), a0); a1 = mfma(vf[1][2 * k], lo4(wt[i][k]), a1);
          a0 = mfma(vf[0][2 * k + 1], hi4(wt[i][k]), a0); a1 = mfma(vf[1][2 * k + 1], hi4(wt[i][k]), a1);
        }
        st128(v4u{pack2(a0[0], a0[1]), pack2(a0[2], a0[3]), pack2(a1[0], a1[1]), pack2(a1[2], a1[3])}, r, oo,
              s * NTILE * 32768 + (i < 8 ? t0o + i * 32768 : t8o));
      }
      SB();
      xs.put(L + (it & 1) * 36864);
      SB();
      xs.req(seg_of(hp, it + 3));
      asm volatile("s_waitcnt vmcnt(9)" ::: "memory");
      bar();
      if (it == 0) bar();
    }
    __builtin_amdgcn_s_setprio(0);
  }
}

DEVI void role_G(const Params& p, const WS& r, int gi) {
  const int lane = threadIdx.x & 63, w = uni(threadIdx.x >> 6);
  const rsrc_t rg = mk_rsrc(p.gw, DM * 2), rx = mk_rsrc(p.x, 2048 * DM * 2);
  const int o = lane < 36 ? lane * 32 : OOB, t0 = (gi * 8 + w) * 10;
  const v4u g0 = pl128(rg, o, 0), g1 = pl128(rg, o + 16, 0);
  float mine = 0.f;
#pragma unroll
  for (int t = 0; t < 10; ++t) {
    const int tok = t0 + t < 2048 ? t0 + t : 2047;
    const v4u x0 = pl128(rx, o, tok * DM * 2), x1 = pl128(rx, o + 16, tok * DM * 2);
    float a = 0.f;
#pragma unroll
    for (int q = 0; q < 4; ++q) {
      a += bf2f(x0[q] & 0xFFFFu) * bf2f(g0[q] & 0xFFFFu) + __uint_as_float(x0[q] & 0xFFFF0000u) * __uint_as_float(g0[q] & 0xFFFF0000u);
      a += bf2f(x1[q] & 0xFFFFu) * bf2f(g1[q] & 0xFFFFu) + __uint_as_float(x1[q] & 0xFFFF0000u) * __uint_as_float(g1[q] & 0xFFFF0000u);
    }
    a = x32_sum(x16_sum(row16_sum(a)));
    mine = lane == t ? a : mine;
  }
  const float gv = __builtin_amdgcn_rcpf(1.f + __builtin_amdgcn_exp2f(-1.4426950408889634f * mine));
  st32(__float_as_uint(gv), r, (lane < 10 && t0 + lane < 2048) ? G_OFF + (t0 + lane) * 4 : OOB, 0);
  drain();
  bar();
  if (threadIdx.x == 0) set_flag(r, CTRL + (L_GP + gi) * 128, 0);
}

DEVI void role_T(const Params& p, const WS& r, int ti) {
  const int lane = threadIdx.x & 63, w = uni(threadIdx.x >> 6), n = lane & 15, g = lane >> 4;
  const rsrc_t r1 = mk_rsrc(p.w1, DM * FF * 2), r2 = mk_rsrc(p.w2, DM * FF * 2);
  const int u0 = ti * 8 + w, u1 = u0 ^ 192;
  v4u raw[2][8];
#pragma unroll
  for (int i = 0; i < 2; ++i) {
    if (u0 + i * 512 >= 648) continue;
    const int u = u0 + i * 512, c = u / 18, ks = u - 18 * c;
#pragma unroll
    for (int q = 0; q < 8; ++q) raw[i][q] = __builtin_amdgcn_raw_buffer_load_b128(r1, (8 * g * FF + 8 * n) * 2, ((32 * ks + q) * FF + 128 * c) * 2, 2);
  }
#pragma unroll
  for (int i = 0; i < 2; ++i) {
    if (u0 + i * 512 >= 648) continue;
    const int u = u0 + i * 512, c = u / 18, ks = u - 18 * c;
#pragma unroll
    for (int t = 0; t < 8; ++t) {
      v4u o;
#pragma unroll
      for (int q = 0; q < 4; ++q) o[q] = __builtin_amdgcn_perm(raw[i][2 * q + 1][t >> 1], raw[i][2 * q][t >> 1], (t & 1) ? 0x07060302u : 0x05040100u);
      st128(o, r, W1T_OFF + lane * 16, ((c * 4 + (t >> 1)) * 18 + ks) * 2048 + (t & 1) * 1024);
    }
  }
  u32 r9[2][8];
#pragma unroll
  for (int i = 0; i < 2; ++i) {
    if (u1 + i * 512 >= 576) continue;
    const int u = u1 + i * 512, k = u >> 2, c4 = u & 3;
    const int so = ((128 * (k >> 2) + 2 * (k & 3)) * DM + 144 * c4) * 2;
#pragma unroll
    for (int q = 0; q < 8; ++q) raw[i][q] = __builtin_amdgcn_raw_buffer_load_b128(r2, (32 * g * DM + 8 * n) * 2, so + (8 * (q >> 1) + (q & 1)) * DM * 2, 2);
#pragma unroll
    for (int q = 0; q < 8; ++q) r9[i][q] = __builtin_amdgcn_raw_buffer_load_b16(r2, (32 * g * DM + 128 + n) * 2, so + (8 * (q >> 1) + (q & 1)) * DM * 2, 0);
  }
#pragma unroll
  for (int i = 0; i < 2; ++i) {
    if (u1 + i * 512 >= 576) continue;
    const int u = u1 + i * 512;
#pragma unroll
    for (int t = 0; t < 9; ++t) {
      v4u o;
#pragma unroll
      for (int q = 0; q < 4; ++q)
        o[q] = t < 8 ? __builtin_amdgcn_perm(raw[i][2 * q + 1][(t & 7) >> 1], raw[i][2 * q][(t & 7) >> 1], (t & 1) ? 0x07060302u : 0x05040100u)
                     : __builtin_amdgcn_perm(r9[i][2 * q + 1], r9[i][2 * q], 0x05040100u);
      st128(o, r, W2T_OFF + lane * 16, u * 9216 + t * 1024);
    }
  }
  drain();
  bar();
  if (threadIdx.x == 0) set_flag(r, CTRL + (L_TP + ti) * 128, 0);
}

DEVI float gelu_tanh(float x) {
  const float e = __builtin_amdgcn_exp2f(x * (-2.302208198f - 0.1029432397f * x * x));
  return x * __builtin_amdgcn_rcpf(1.f + e);
}

DEVI void role_F(const Params& p, const WS& r, LAS unsigned char* L, int tt, int sp, int c0, int c1) {
  const int lane = threadIdx.x & 63, w = uni(threadIdx.x >> 6), n = lane & 15, g = lane >> 4;
  constexpr int HB = 73728;
  const rsrc_t rx = mk_rsrc(p.x, 2048 * DM * 2);
  { int rounds = 0, seen = -2, seen2 = -2; wait_prog(r, L_TP, NTB, 0, lane, seen, rounds); wait_prog(r, L_GP, 28, 0, lane, seen2, rounds); }
  bar();
  {
    const int go = (tt * 64 + (w >> 1) * 16 + (lane >> 2)) * (DM * 2) + (w & 1) * 9 * 64 + ximg_piece(lane);
    v4u st[9];
#pragma unroll
    for (int k = 0; k < 9; ++k) st[k] = pl128(rx, go + k * 64, 0);
#pragma unroll
    for (int k = 0; k < 9; ++k) *(LAS v4u*)(L + (w * 9 + k) * 1024 + lane * 16) = st[k];
  }
  if (w < 4) {
    const rsrc_t rb = mk_rsrc(p.b1, FF * 2);
    const int vo = W1T_OFF + w * 18 * 2048 + lane * 16;
    constexpr int WIN = 9;
    __builtin_amdgcn_s_setprio(1);
    v4u wf[WIN][2];
    auto req = [&](int slot, int c, int ks) {
      wf[slot][0] = ld128(r, vo, (c * 72 + ks) * 2048);
      wf[slot][1] = ld128(r, vo + 1024, (c * 72 + ks) * 2048);
    };
#pragma unroll
    for (int k = 0; k < WIN - 1; ++k) req(k, c0, k);
    bar();
    const int rd = ximg_rd(lane);
    for (int c = c0; c < c1; ++c) {
      const int cn = c + 1 < c1 ? c + 1 : c;
      v4u bias;
#pragma unroll
      for (int q = 0; q < 4; ++q) bias[q] = pl32(rb, (32 * g + 2 * w) * 2 + q * 16, c * 256);
      f32x4 acc[2][4];
#pragma unroll
      for (int i = 0; i < 8; ++i) acc[i >> 2][i & 3] = zero4();
      v4u xf[2][4];
#pragma unroll
      for (int m = 0; m < 4; ++m) xf[0][m] = *(LAS v4u*)(L + (m * 18) * 1024 + rd);
#pragma unroll
      for (int ks = 0; ks < 18; ++ks) {
        const int kn = ks + WIN - 1;
        req(kn % WIN, kn < 18 ? c : cn, kn % 18);
        if (ks + 1 < 18) {
#pragma unroll
          for (int m = 0; m < 4; ++m) xf[(ks + 1) & 1][m] = *(LAS v4u*)(L + (m * 18 + ks + 1) * 1024 + rd);
        }
        SB();
#pragma unroll
        for (int m = 0; m < 4; ++m) { acc[0][m] = mfma(wf[ks % WIN][0], xf[ks & 1][m], acc[0][m]); acc[1][m] = mfma(wf[ks % WIN][1], xf[ks & 1][m], acc[1][m]); }
        SB();
      }
#pragma unroll
      for (int m = 0; m < 4; ++m) {
        v4u o;
#pragma unroll
        for (int q = 0; q < 4; ++q)
          o[q] = pack2(gelu_tanh(acc[0][m][q] + bf2f(bias[q] & 0xFFFFu)), gelu_tanh(acc[1][m][q] + __uint_as_float(bias[q] & 0xFFFF0000u)));
        *(LAS v4u*)(L + HB + ((c - c0) & 1) * 16384 + (m * 4 + w) * 1024 + lane * 16) = o;
      }
      bar();
    }
  } else {
    const int c4 = w - 4;
    const int vo = W2T_OFF + c4 * 9216 + lane * 16;
    v4u bw[2][9];
#pragma unroll
    for (int t = 0; t < 9; ++t) bw[0][t] = ld128(r, vo + t * 1024, 4 * c0 * 36864);
    f32x4 acc[9][4];
#pragma unroll
    for (int i = 0; i < 36; ++i) acc[i >> 2][i & 3] = zero4();
    bar();
    bar();
    for (int c = c0; c < c1; ++c) {
#pragma unroll
      for (int ks = 0; ks < 4; ++ks) {
        const int kn = 4 * c + ks + 1 < 4 * c1 ? 4 * c + ks + 1 : 4 * c + ks;
        v4u hf[4];
#pragma unroll
        for (int m = 0; m < 4; ++m) hf[m] = *(LAS v4u*)(L + HB + ((c - c0) & 1) * 16384 + (m * 4 + ks) * 1024 + lane * 16);
#pragma unroll
        for (int t = 0; t < 9; ++t) {
          bw[(ks + 1) & 1][t] = ld128(r, vo + t * 1024, kn * 36864);
          SB();
#pragma unroll
          for (int m = 0; m < 4; ++m) acc[t][m] = mfma(hf[m], bw[ks & 1][t], acc[t][m]);
          SB();
        }
      }
      if (c + 1 < c1) bar();
    }
    const int xo = FP_OFF + (tt * 4 + c4) * 36864 + lane * 16;
    if (sp == 1) {
#pragma unroll
      for (int i = 0; i < 36; ++i) st128(__builtin_bit_cast(v4u, acc[i >> 2][i & 3]), r, xo, i * 1024);
    } else {
      int rounds = 0, seen = -2;
      wait_prog(r, L_FP + 32 + tt, 1, 0, lane, seen, rounds);
#pragma unroll
      for (int j = 0; j < 3; ++j) {
        v4u t[12];
#pragma unroll
        for (int i = 0; i < 12; ++i) t[i] = ld128(r, xo, (12 * j + i) * 1024);
#pragma unroll
        for (int i = 0; i < 12; ++i) { acc[3 * j + (i >> 2)][i & 3] += __builtin_bit_cast(f32x4, t[i]); asm volatile("" : "+v"(acc[3 * j + (i >> 2)][i & 3])); }
        SB();
      }
      const rsrc_t ro = mk_rsrc(p.out, 2048 * DM * 2), rb = mk_rsrc(p.b2, DM * 2);
      const v4u b8 = pl128(rb, (144 * c4 + 8 * n) * 2, 0);
      const float b9 = bf2f(__builtin_amdgcn_raw_buffer_load_b16(rb, (144 * c4 + 128 + n) * 2, 0, 0));
      if (tt < CUT) {
        const int sg = 2 * tt + (lane >= 9), want = lane < 18 ? sg : -2;
        const int vo2 = CTRL + (L_OP + (sg % 3) * 9 + lane % 9) * 128;
        POLL(const int v = get_flag(r, vo2); bad = __builtin_amdgcn_ballot_w64(v < want) != 0; if (bad) __builtin_amdgcn_s_sleep(32));
      }
      const int o8 = ((tt * 64 + 4 * g) * DM + 144 * c4 + 8 * n) * 2, o9 = ((tt * 64 + 4 * g) * DM + 144 * c4 + 128 + n) * 2;
#pragma unroll
      for (int m = 0; m < 4; ++m) {
        const v4u gq = ld128(r, G_OFF + (tt * 64 + 16 * m + 4 * g) * 4, 0);
        v4u a8[4]; u32 a9[4];
#pragma unroll
        for (int q = 0; q < 4; ++q) {
          a8[q] = ld128(ro, tt < CUT ? o8 : OOB, (16 * m + q) * DM * 2);
          a9[q] = __builtin_amdgcn_raw_buffer_load_b16(ro, tt < CUT ? o9 : OOB, (16 * m + q) * DM * 2, SC1);
        }
#pragma unroll
        for (int q = 0; q < 4; ++q) {
          const float k = 1.f - __uint_as_float(gq[q]);
          v4u o;
#pragma unroll
          for (int t = 0; t < 4; ++t)
            o[t] = pack2(k * (acc[2 * t][m][q] + bf2f(b8[t] & 0xFFFFu)) + bf2f(a8[q][t] & 0xFFFFu),
                         k * (acc[2 * t + 1][m][q] + __uint_as_float(b8[t] & 0xFFFF0000u)) + __uint_as_float(a8[q][t] & 0xFFFF0000u));
          st128(o, ro, o8, (16 * m + q) * DM * 2);
          __builtin_amdgcn_raw_buffer_store_b16((u16)pack2(k * (acc[8][m][q] + b9) + bf2f(a9[q]), 0.f), ro, o9, (16 * m + q) * DM * 2, SC1);
        }
        SB();
      }
    }
  }
  drain();
  bar();
  if (threadIdx.x == 0) set_flag(r, CTRL + (L_FP + sp * 32 + tt) * 128, 0);
}

DEVI void role_O(const Params& p, const WS& r, LAS unsigned char* L, int cg, int s0, int ds) {
  const int lane = threadIdx.x & 63, w = uni(threadIdx.x >> 6), n = lane & 15, g = lane >> 4;
  int rounds = 0, seenY0 = -2, seenY1 = -2, seenS = -2;
  const int fm = w & 1, ftp = (w >> 1) & 1, frh = w >> 2;
  const int col = 64 * cg + 32 * ftp + 2 * n;
  const rsrc_t ro = mk_rsrc(p.out, 2048 * DM * 2);
  { int seen = -2; wait_prog(r, L_GP, 28, 0, lane, seen, rounds); }
  const int svo = CTRL + ((lane & 4 ? L_VP : L_SP) + 4 * w + (lane & 3)) * 128;
  for (int s = s0; s < O2S; s += ds) {
    wait_prog2(r, L_YP + 4 * w, 4, s, lane, seenY0, seenY1, rounds);
    v4u bz[4][4];
#pragma unroll
    for (int t = 0; t < 4; ++t)
#pragma unroll
      for (int q = 0; q < 4; ++q) bz[t][q] = ld128(r, (4 * w + q) * 1024 + lane * 16, BZ_OFF + (s * NTILE + 4 * cg + t) * 32768);
    const int tt = s >> 1;
    if (tt >= CUT) { int seenF = -2; wait_prog(r, L_FP + tt, 1, 0, lane, seenF, rounds); }
    const int tok = s * 32 + 2 * (4 * g + 2 * frh) + fm;
    u32 fb[2];
    float gt[2];
#pragma unroll
    for (int q = 0; q < 2; ++q) {
      fb[q] = ld32(ro, tt >= CUT ? ((tok + 2 * q) * DM + col) * 2 : OOB, 0);
      gt[q] = __uint_as_float(ld32(r, G_OFF + (tok + 2 * q) * 4, 0));
    }
    if (__builtin_amdgcn_ballot_w64(seenS < s) != 0) {
      POLL(seenS = get_flag(r, svo); bad = __builtin_amdgcn_ballot_w64(seenS < s) != 0; if (bad) __builtin_amdgcn_s_sleep(4));
    }
    v4u px[2][4], pm[2], cz[4];
#pragma unroll
    for (int m = 0; m < 2; ++m) {
#pragma unroll
      for (int q = 0; q < 4; ++q) px[m][q] = ld128(r, ((4 * w + q) * 2 + m) * 1280 + lane * 16, PX_OFF + s * NH * 2560);
      pm[m] = ld128(r, ((4 * w + g) * 2 + m) * 1280 + 1024 + n * 16, PX_OFF + s * NH * 2560);
    }
#pragma unroll
    for (int t = 0; t < 4; ++t) cz[t] = ld128(r, w * 1024 + lane * 16, CZ_OFF + (s * NTILE + 4 * cg + t) * 8192);
#pragma unroll
    for (int t = 0; t < 4; ++t)
#pragma unroll
      for (int m = 0; m < 2; ++m) {
        f32x4 a = {0, 0, 0, 0};
#pragma unroll
        for (int q = 0; q < 4; ++q) a = mfma(px[m][q], bz[t][q], a);
        a = mfma(pm[m], cz[t], a);
        *(LAS f32x4*)(L + w * 8192 + (t * 2 + m) * 1024 + lane * 16) = a;
      }
    bar();
    f32x2 e = {0, 0}, o = {0, 0};
#pragma unroll
    for (int q = 0; q < 8; ++q) {
      e += *(LAS f32x2*)(L + q * 8192 + (4 * ftp + fm) * 1024 + lane * 16 + frh * 8);
      o += *(LAS f32x2*)(L + q * 8192 + (4 * ftp + 2 + fm) * 1024 + lane * 16 + frh * 8);
    }
#pragma unroll
    for (int q = 0; q < 2; ++q) {
      st32(pack2(gt[q] * e[q] + bf2f(fb[q] & 0xFFFFu), gt[q] * o[q] + __uint_as_float(fb[q] & 0xFFFF0000u)), ro, ((tok + 2 * q) * DM + col) * 2, 0);
    }
    drain();
    bar();
    if (threadIdx.x == 0) set_flag(r, CTRL + (L_OP + s0 * 9 + cg) * 128, s);
  }
}

DEVI void role_O2(const Params& p, const WS& r, LAS unsigned char* L, int pr, int m, int s0) {
  const int lane = threadIdx.x & 63, w = uni(threadIdx.x >> 6), n = lane & 15, g = lane >> 4;
  int rounds = 0, seenY = -2, seenS = -2;
  const rsrc_t ro = mk_rsrc(p.out, 2048 * DM * 2);
  const int svo = CTRL + ((lane & 4 ? L_VP : L_SP) + 4 * w + (lane & 3)) * 128;
  bar();
  for (int s = s0; s < NSEG; s += 3) {
    wait_prog(r, L_YP + 4 * w, 4, s, lane, seenY, rounds);
    v4u bz[2][4];
#pragma unroll
    for (int t = 0; t < 2; ++t)
#pragma unroll
      for (int q = 0; q < 4; ++q) bz[t][q] = ld128(r, (4 * w + q) * 1024 + lane * 16, BZ_OFF + (s * NTILE + 2 * pr + t) * 32768);
    { int seenF = -2; wait_prog(r, L_FP + (s >> 1), 1, 0, lane, seenF, rounds); }
    const int tok = s * 32 + 2 * (4 * g + (w & 3)) + m, oo = (tok * DM + 32 * pr + 2 * n) * 2;
    const u32 fb = ld32(ro, oo, 0);
    const float gt = __uint_as_float(ld32(r, G_OFF + tok * 4, 0));
    POLL(seenS = get_flag(r, svo); bad = __builtin_amdgcn_ballot_w64(seenS < s) != 0; if (bad) { if (__builtin_amdgcn_ballot_w64(seenS < s - 2) != 0) __builtin_amdgcn_s_sleep(32); else __builtin_amdgcn_s_sleep(1); });
    v4u px[4], cz[2];
#pragma unroll
    for (int q = 0; q < 4; ++q) px[q] = ld128(r, ((4 * w + q) * 2 + m) * 1280 + lane * 16, PX_OFF + s * NH * 2560);
    const v4u pm = ld128(r, ((4 * w + g) * 2 + m) * 1280 + 1024 + n * 16, PX_OFF + s * NH * 2560);
#pragma unroll
    for (int t = 0; t < 2; ++t) cz[t] = ld128(r, w * 1024 + lane * 16, CZ_OFF + (s * NTILE + 2 * pr + t) * 8192);
#pragma unroll
    for (int t = 0; t < 2; ++t) {
      f32x4 a = {0, 0, 0, 0};
#pragma unroll
      for (int q = 0; q < 4; ++q) a = mfma(px[q], bz[t][q], a);
      a = mfma(pm, cz[t], a);
      *(LAS f32x4*)(L + (s & 1) * 16384 + w * 2048 + t * 1024 + lane * 16) = a;
    }
    bar();
    if (w < 4) {
      float e = 0.f, o = 0.f;
#pragma unroll
      for (int q = 0; q < 8; ++q) {
        e += *(LAS float*)(L + (s & 1) * 16384 + q * 2048 + lane * 16 + w * 4);
        o += *(LAS float*)(L + (s & 1) * 16384 + q * 2048 + 1024 + lane * 16 + w * 4);
      }
      st32(pack2(gt * e + bf2f(fb & 0xFFFFu), gt * o + __uint_as_float(fb & 0xFFFF0000u)), ro, oo, 0);
    }
  }
  if (pr == 0 && m == 0 && s0 == O2S + 1 && threadIdx.x == 0) st64(v2u{r.klo, r.khi}, r, CTRL + L_EPOCH * 128, 0);
}

DEVI void claim(const Params& p, LAS unsigned char* L, int role) {
  if (threadIdx.x == 0) {
    GAS u64* ctl = (GAS u64*)(p.ws + CTRL);
    const u64 key = (__hip_atomic_load(ctl + L_EPOCH * 16, __ATOMIC_RELAXED, __HIP_MEMORY_SCOPE_AGENT) + 256) & ~255ull;
    NOUNROLL for (int i = 0; i < 256; ++i) {
      if (__hip_atomic_exchange(ctl + L_CLAIM * 16 + role * 4, key, __ATOMIC_RELAXED, __HIP_MEMORY_SCOPE_AGENT) != key) break;
      role = (role + 1) & 255;
    }
    *(LAS v4u*)(L + LDS_BYTES - 16) = v4u{(u32)role, (u32)key, (u32)(key >> 32), 0};
  }
}
DEVI int get_role(WS& r, LAS unsigned char* L) {
  bar();
  const v4u v = *(LAS v4u*)(L + LDS_BYTES - 16);
  r.klo = uni(v.y); r.khi = uni(v.z);
  return uni(v.x);
}

DEVI int kind_of(int role) { const int x = (role >> 5) & 3, t = role & 31; return role < 128 ? (t == 7 ? 3 : 4) : !(x & 1) ? t >> 4 : t < 18 ? 2 : 3; }
DEVI int id_of(int role) {
  const int x = (role >> 5) & 3, t = role & 31, xi = x >> 1, kind = kind_of(role);
  return kind < 2 ? xi * 16 + (t & 15) : kind == 2 ? xi * 18 + t : kind == 4 ? x * 8 + (t & 7) : role >= 128 ? xi * 14 + t - 18 : 28 + x;
}
DEVI void first_req(const Params& p, LAS unsigned char* L, int role) {
  const int kind = kind_of(role), h = id_of(role), tid = threadIdx.x;
  const bool vo = kind == 1 || kind == 4;
  if (kind == 4 ? (role & 8) == 0 : kind == 2) return;
  const rsrc_t r1 = mk_rsrc(vo ? p.wv : kind == 0 ? p.wq : p.wk, DM * 4096 * 2), r2 = mk_rsrc(vo ? p.wo : kind == 0 ? p.wk : p.wq, DM * 4096 * 2);
  const int va = ((tid >> 4) * 4096 + h * 128) * 2 + (((tid & 15) * 16) ^ (((tid >> 7) & 1) << 6));
  const int lb = uni((int)(size_t)L + (tid >> 6) * 1024);
#pragma unroll
  for (int k = 0; k < 18; ++k) {
    const int kk = k % 9;
    const int v = (k >= 9 && vo) ? h * 128 * DM * 2 + kk * 8192 + ((tid * 16) ^ (((((kk * 512 + tid) / 72) >> 3) & 1) << 6)) : va + kk * 32 * 8192;
    u32 keep;
    asm volatile("s_mov_b32 %0, m0\n\ts_mov_b32 m0, %3\n\ts_nop 2\n\tbuffer_load_dwordx4 %1, %2, 0 offen lds\n\ts_mov_b32 m0, %0"
                 : "=&s"(keep) : "v"(v), "s"(k < 9 ? r1 : r2), "s"(lb + k * 8192) : "memory");
  }
}

__global__ void __launch_bounds__(512, 2) __attribute__((amdgpu_num_vgpr(124))) chain_kernel(Params p) {
  LAS unsigned char* L = (LAS unsigned char*)smem_g;
  p.ws = (unsigned char*)(((uintptr_t)p.ws + RING_BYTES - 1) & ~(uintptr_t)(RING_BYTES - 1));
  WS r; r.rs = __builtin_amdgcn_make_buffer_rsrc(p.ws, 0, WS_USED, 0x00020000);
  const int guess = uni((__builtin_amdgcn_s_getreg(20 | (3 << 11)) & 7) * 32 + ((blockIdx.x >> 3) & 31));
  claim(p, L, guess);
  first_req(p, L, guess);
  const int role = get_role(r, L);
  if (__builtin_expect(role != guess, 0)) { drain(); bar(); first_req(p, L, role); }
  const int x = (role >> 5) & 3, t = role & 31, kind = kind_of(role), id = id_of(role);
  int t2 = -1;
  if (kind == 0) role_S(p, r, L, id);
  else if (kind == 1) role_V(p, r, L, id);
  else if (kind == 2) role_R(p, r, L, id);
  else if (kind == 3) { role_X(p, r, L, id); t2 = role >= 128 ? -1 : 32 + x; }
  else {
    if (t & 8) role_Y(p, r, L, id, t >> 4);
    else {
      const u64 t0 = __builtin_amdgcn_s_memrealtime();
      while (__builtin_amdgcn_s_memrealtime() - t0 < TDLY) __builtin_amdgcn_s_sleep(16);
    }
    if (t >= 16) role_T(p, r, (t & 8) * 4 + x * 8 + (t & 7));
    else if (t < 7) role_G(p, r, x * 7 + t);
    if (t >= 16) { role_F(p, r, L, (x & 1) * 16 + t - 16, x >> 1, (x >> 1) * 18, (x >> 1) * 18 + 18); t2 = t < 25 ? 36 + x * 9 + t - 16 : 72 + x * 7 + t - 25; }
    else if (t >= 8) t2 = x * 8 + t - 8;
    else {
      const int q = x * 7 + t;
      if (q < 27) role_O(p, r, L, q % 9, q / 9, 3);
      if (q >= 20) t2 = 80 + q;
    }
  }
  if (t2 >= 0) role_O2(p, r, L, (t2 % 36) >> 1, t2 & 1, t2 < 36 ? O2S + 1 : t2 < 72 ? O2S : O2S + 2);
}

extern "C" void kernel_launch(void* const* d_in, const int* in_sizes, int n_in, void* d_out, int out_size, void* d_ws, size_t ws_size, hipStream_t stream) {
  Params p;
  p.x = (const u16*)d_in[0]; p.mem0 = (const u16*)d_in[1]; p.wq = (const u16*)d_in[2]; p.wk = (const u16*)d_in[3]; p.wv = (const u16*)d_in[4]; p.wo = (const u16*)d_in[5];
  p.w1 = (const u16*)d_in[8]; p.b1 = (const u16*)d_in[9]; p.w2 = (const u16*)d_in[10]; p.b2 = (const u16*)d_in[11]; p.gw = (const u16*)d_in[12];
  p.out = (u16*)d_out;
  p.ws = (unsigned char*)d_ws;
  (void)hipFuncSetAttribute((const void*)chain_kernel, hipFuncAttributeMaxDynamicSharedMemorySize, LDS_BYTES);
  chain_kernel<<<256, 512, LDS_BYTES, stream>>>(p);
}
```

```cpp
#include <hip/hip_runtime.h>
#include <hip/hip_bf16.h>
#include <stdint.h>
#include <string.h>
#pragma clang diagnostic ignored "-Winline-asm"

typedef unsigned u32;
typedef unsigned short u16;
typedef unsigned long long u64;
typedef u32 v2u __attribute__((ext_vector_type(2)));
typedef u32 v4u __attribute__((ext_vector_type(4)));
typedef u32 v8u __attribute__((ext_vector_type(8)));
typedef float f32x2 __attribute__((ext_vector_type(2)));
typedef float f32x4 __attribute__((ext_vector_type(4)));
typedef __bf16 bf16x2 __attribute__((ext_vector_type(2)));
typedef __bf16 bf16x8 __attribute__((ext_vector_type(8)));
typedef __bf16 bf16x16 __attribute__((ext_vector_type(16)));
typedef short s16x4 __attribute__((ext_vector_type(4)));
typedef short s16x8 __attribute__((ext_vector_type(8)));
typedef __amdgpu_buffer_rsrc_t rsrc_t;
#define LAS __attribute__((address_space(3)))
#define GAS __attribute__((address_space(1)))
#define DEVI __device__ __forceinline__

constexpr int DM = 576, NH = 32, NSEG = 64, FF = 4608, NTILE = 36;
constexpr u32 SENT = 0xFFFFFFFFu;
constexpr int SC1 = 16;
constexpr int CHUNK = 32768;
constexpr int SLOT = 4 << 20;
constexpr int P_CH = 36, MEM_CH = 40, H_CH = 42;
constexpr int ALIGN = 2 << 20, RING_BYTES = 10 << 20;
constexpr int CTRL = 2 << 20;
constexpr int L_EPOCH = 0, L_ABORT = 1, L_PAR = 480, L_RP = 80, L_CLAIM = 416, L_SP = 16, L_VP = 48, L_XP = 128, L_YP = 192, L_FP = 256, L_OP = 320, L_TP = 352, NTB = 64, L_GP = 160;
constexpr int HSEG = 4;
constexpr int CUT = 20;
constexpr int O2S = 56;
constexpr int KQ_OFF = RING_BYTES;
constexpr int BZ_OFF = KQ_OFF + NSEG * NH * 16384;
constexpr int CZ_OFF = BZ_OFF + NSEG * NTILE * NH * 1024;
constexpr int PX_OFF = CZ_OFF + NSEG * NTILE * NH * 256;
constexpr int FP_OFF = PX_OFF + NSEG * NH * 2560;
constexpr int W1T_OFF = FP_OFF + 2048 * DM * 4;
constexpr int W2T_OFF = W1T_OFF + DM * FF * 2;
constexpr int G_OFF = W2T_OFF + DM * FF * 2;
constexpr int JUNK = 6 << 20;
constexpr int WS_USED = G_OFF + 8192;
static_assert(WS_USED + ALIGN <= (256 << 20), "workspace");
constexpr int SPIN_CAP = 1 << 18;
constexpr int LDS_BYTES = 148 * 1024;

struct Params {
  const u16 *x, *mem0, *wq, *wk, *wv, *wo, *w1, *b1, *w2, *b2, *gw;
  u16* out;
  unsigned char* ws;
};

struct WS {
  rsrc_t rs; u32 klo, khi;
  DEVI operator rsrc_t() const { return rs; }
};
DEVI float bf2f(u32 b) { return __uint_as_float(b << 16); }
DEVI u32 pack2(float lo, float hi) { f32x2 v = {lo, hi}; bf16x2 b = __builtin_convertvector(v, bf16x2); return __builtin_bit_cast(u32, b); }
DEVI u32 guard(u32 v) { return v < 0xFFFFFFFEu ? v : 0xFFFFFFFEu; }
DEVI void bar() { asm volatile("s_waitcnt lgkmcnt(0)\n\ts_barrier" ::: "memory"); }
DEVI void drain() { asm volatile("s_waitcnt vmcnt(0)" ::: "memory"); }
DEVI f32x4 smf64(v4u a, v8u b, f32x4 c, int idx) { return __builtin_amdgcn_smfmac_f32_16x16x64_bf16(__builtin_bit_cast(bf16x8, a), __builtin_bit_cast(bf16x16, b), c, idx, 0, 0); }
DEVI f32x4 smf32(v2u a, v4u b, f32x4 c, int idx) { return __builtin_amdgcn_smfmac_f32_16x16x32_bf16(__builtin_bit_cast(s16x4, a), __builtin_bit_cast(s16x8, b), c, idx, 0, 0); }
DEVI f32x4 mfma(v4u a, v4u b, f32x4 c) { return __builtin_amdgcn_mfma_f32_16x16x32_bf16(__builtin_bit_cast(bf16x8, a), __builtin_bit_cast(bf16x8, b), c, 0, 0, 0); }
DEVI v8u cat(v4u a, v4u b) { return __builtin_shufflevector(a, b, 0, 1, 2, 3, 4, 5, 6, 7); }
DEVI v4u lo4(v8u a) { return __builtin_shufflevector(a, a, 0, 1, 2, 3); }
DEVI v4u hi4(v8u a) { return __builtin_shufflevector(a, a, 4, 5, 6, 7); }
template <int CTRL_> DEVI float dpp(float v) { return __uint_as_float(__builtin_amdgcn_update_dpp(0, __float_as_uint(v), CTRL_, 0xF, 0xF, true)); }
DEVI float row16_sum(float v) { v += dpp<0xB1>(v); v += dpp<0x4E>(v); v += dpp<0x141>(v); v += dpp<0x140>(v); return v; }
DEVI float x16_max(float v) { auto r = __builtin_amdgcn_permlane16_swap(__float_as_uint(v), __float_as_uint(v), false, false); return fmaxf(__uint_as_float(r[0]), __uint_as_float(r[1])); }
DEVI float x32_max(float v) { auto r = __builtin_amdgcn_permlane32_swap(__float_as_uint(v), __float_as_uint(v), false, false); return fmaxf(__uint_as_float(r[0]), __uint_as_float(r[1])); }
DEVI float x16_sum(float v) { auto r = __builtin_amdgcn_permlane16_swap(__float_as_uint(v), __float_as_uint(v), false, false); return __uint_as_float(r[0]) + __uint_as_float(r[1]); }
DEVI float x32_sum(float v) { auto r = __builtin_amdgcn_permlane32_swap(__float_as_uint(v), __float_as_uint(v), false, false); return __uint_as_float(r[0]) + __uint_as_float(r[1]); }
DEVI v4u ld128(rsrc_t r, int vo, int so) { return __builtin_amdgcn_raw_buffer_load_b128(r, vo, so, SC1); }
DEVI v2u ld64(rsrc_t r, int vo, int so) { return __builtin_amdgcn_raw_buffer_load_b64(r, vo, so, SC1); }
DEVI u32 ld32(rsrc_t r, int vo, int so) { return __builtin_amdgcn_raw_buffer_load_b32(r, vo, so, SC1); }
DEVI void st128(v4u v, rsrc_t r, int vo, int so) { __builtin_amdgcn_raw_buffer_store_b128(v, r, vo + so, 0, SC1); }
DEVI void st64(v2u v, rsrc_t r, int vo, int so) { __builtin_amdgcn_raw_buffer_store_b64(v, r, vo, so, SC1); }
DEVI void st32(u32 v, rsrc_t r, int vo, int so) { __builtin_amdgcn_raw_buffer_store_b32(v, r, vo, so, SC1); }
DEVI rsrc_t mk_rsrc(const void* ptr, int bytes) { return __builtin_amdgcn_make_buffer_rsrc((void*)ptr, 0, bytes, 0x00020000); }
DEVI v4u pl128(rsrc_t r, int vo, int so) { return __builtin_amdgcn_raw_buffer_load_b128(r, vo, so, 0); }
DEVI u32 pl32(rsrc_t r, int vo, int so) { return __builtin_amdgcn_raw_buffer_load_b32(r, vo, so, 0); }
DEVI u32 umax(u32 a, u32 b) { return a > b ? a : b; }
DEVI u32 mx4(v4u v) { return umax(umax(v.x, v.y), umax(v.z, v.w)); }
DEVI int uni(int v) { return __builtin_amdgcn_readfirstlane(v); }
DEVI int opaque(int v) { asm volatile("" : "+v"(v)); return v; }
DEVI int sopaque(int v) { asm volatile("" : "+s"(v)); return v; }
DEVI void fence_c() { asm volatile("" ::: "memory"); }

constexpr int XSLP = 4, PSLP = 23, RSLP = 25, VBG = 8, VSLP = 6, KSLP = 11, MAR_R = 3;
constexpr int TDLY = 900;
#define NOUNROLL _Pragma("clang loop unroll(disable)")
#define SB() __builtin_amdgcn_sched_barrier(0)
constexpr int OOB = 0x7FFFFFF0;
DEVI bool any_sent(u32 m) { return __builtin_amdgcn_ballot_w64(m == SENT) != 0; }
DEVI void set_flag(const WS& r, int vo, int v) { st64(v2u{r.klo + (u32)(v + 2), r.khi}, r, vo, 0); }
DEVI int get_flag(const WS& r, int vo) {
  const v2u v = ld64(r, vo, 0);
  return (v.y == r.khi && v.x - r.klo < 256u) ? (int)(v.x - r.klo) - 2 : -2;
}
DEVI bool give_up(const WS& r, int& rounds) {
  if (++rounds > SPIN_CAP / 256) { set_flag(r, CTRL + L_ABORT * 128, 0); return true; }
  if (uni(get_flag(r, CTRL + L_ABORT * 128)) >= 0) { rounds = SPIN_CAP; return true; }
  return false;
}
#define POLL(BODY)                                                                                   \
  NOUNROLL for (int cnt_ = 1;; ++cnt_) {                                                             \
    bool bad;                                                                                        \
    fence_c();                                                                                       \
    BODY;                                                                                            \
    if (__builtin_expect(!bad, 1)) break;                                                            \
    if (((cnt_ & 255) == 0 || rounds >= SPIN_CAP / 256) && give_up(r, rounds)) break;                \
  }                                                                                                  \
  fence_c();
DEVI u32 rt7() {
  u64 t;
  asm volatile("s_memrealtime %0\n\ts_waitcnt lgkmcnt(0)" : "=s"(t)::"memory");
  return (u32)t << 7;
}
#define HC1 "v_and_b32 %1, 127, v248\n\tv_cmp_ne_u32 vcc, %7, %1\n\ts_cbranch_vccz 2f\n\t"
#define HC4 "s_memrealtime s[96:97]\n\t" HC1 HC1 HC1 HC1
#define HCAD HC4 HC4 HC4 HC4
DEVI void hint_wait(rsrc_t r, int vo, int so, int step, int mar, u64& now) {
  int cnt = step < 0 ? 0 : 1024;
  u32 tv, t;
  asm volatile(
      "s_memrealtime s[96:97]\n\t"
      "v_mov_b32 v248, -1\n\ts_waitcnt lgkmcnt(0)\n\ts_nop 1\n\t"
      "1:\n\t"
      "buffer_load_dword v248, %4, %5, %6 offen sc1\n\t"
      HCAD
      "s_sub_u32 %2, %2, 1\n\t"
      "s_cbranch_scc0 1b\n\t"
      "2:\n\t"
      "s_lshl_b32 %2, s96, 7\n\t"
      "v_add_u32 %0, v248, %8\n\t"
      "v_sub_u32 %1, %2, %0\n\t"
      "v_cmp_gt_i32 vcc, 0, %1\n\t"
      "s_cbranch_vccz 4f\n\t"
      "s_cmp_lt_i32 %7, 0\n\t"
      "s_cselect_b32 %2, 0, 64\n\t"
      "3:\n\t"
      "s_memrealtime s[96:97]\n\t"
      "s_sub_u32 %2, %2, 1\n\t"
      "s_cbranch_scc1 4f\n\t"
      "s_waitcnt lgkmcnt(0)\n\t"
      "s_lshl_b32 s96, s96, 7\n\t"
      "v_sub_u32 %1, s96, %0\n\t"
      "v_cmp_gt_i32 vcc, 0, %1\n\t"
      "s_cbranch_vccnz 3b\n\t"
      "4:"
      : "=&v"(tv), "=&v"(t), "+s"(cnt), "=&{s[96:97]}"(now)
      : "v"(vo), "s"(r), "s"(so), "s"(step), "v"(mar)
      : "memory", "vcc", "scc", "v248");
}
DEVI int track(int dur, int m) { return (m > 0 && m < (200 << 7)) ? dur + ((m - dur) >> 2) : dur; }
#define DCHK "v_max3_u32 %0, v248, v249, v250\n\tv_max3_u32 %1, v252, v253, v254\n\tv_max3_u32 %0, %0, v251, v255\n\tv_max_u32 %0, %0, %1\n\tv_cmp_eq_u32 vcc, -1, %0\n\t"
#define DC1 DCHK "s_cbranch_vccz 2f\n\t"
#define DC2 DC1 DC1
#define DCAD DC2 DC2 DC2
template <int TWO>
DEVI bool data_wait(rsrc_t r, int vo, int so, int la) {
  int cnt = 4096;
  u32 t0, t1;
  asm volatile(
      "s_waitcnt vmcnt(0)\n\t"
      "v_mov_b32 v248, -1\n\tv_mov_b32 v249, -1\n\tv_mov_b32 v250, -1\n\tv_mov_b32 v251, -1\n\t"
      "v_mov_b32 v252, %6\n\tv_mov_b32 v253, %6\n\tv_mov_b32 v254, %6\n\tv_mov_b32 v255, %6\n\t"
      "1:\n\t"
      "buffer_load_dwordx4 v[248:251], %3, %4, %5 offen sc1\n\t"
      ".if %6\n\t"
      "buffer_load_dwordx4 v[252:255], %3, %4, %5 offen offset:1024 sc1\n\t"
      ".endif\n\t"
      "s_sub_u32 %2, %2, 1\n\t"
      "s_cbranch_scc1 2f\n\t"
      DCAD
      DCHK "s_cbranch_vccnz 1b\n\t"
      "2:\n\t"
      "ds_write_b128 %7, v[248:251]\n\t"
      ".if %6\n\t"
      "ds_write_b128 %7, v[252:255] offset:1024\n\t"
      ".endif"
      : "=&v"(t0), "=&v"(t1), "+s"(cnt)
      : "v"(vo), "s"(r), "s"(so), "i"(-TWO), "v"(la)
      : "memory", "vcc", "scc", "v248", "v249", "v250", "v251", "v252", "v253", "v254", "v255");
  return cnt >= 0;
}
DEVI void wait_prog(const WS& r, int line0, int n, int want, int lane, int& seen, int& rounds) {
  if (__builtin_amdgcn_ballot_w64(seen < want) != 0) {
    const int vo = CTRL + (line0 + (lane < n ? lane : 0)) * 128;
    POLL(seen = get_flag(r, vo); bad = __builtin_amdgcn_ballot_w64(seen < want) != 0; if (bad) __builtin_amdgcn_s_sleep(2));
  }
}

DEVI void wait_prog2(const WS& r, int line0, int n, int want, int lane, int& seen0, int& seen1, int& rounds) {
  const bool hp = (want & 1) && want < HSEG;
  int sn = hp ? seen1 : seen0;
  wait_prog(r, line0 + (hp ? 32 : 0), n, want, lane, sn, rounds);
  if (hp) seen1 = sn; else seen0 = sn;
}
extern __shared__ __attribute__((aligned(16))) unsigned char smem_g[];

template <int WO>
struct WLoad {
  rsrc_t r1, r2; int va, vb;
  DEVI void init(const u16* W1, const u16* W2, int h) {
    r1 = mk_rsrc(W1, DM * 4096 * 2); r2 = mk_rsrc(W2, DM * 4096 * 2);
    const int tid = threadIdx.x;
    va = ((tid >> 4) * 4096 + h * 128) * 2 + (tid & 15) * 16; vb = h * 128 * DM * 2 + tid * 16;
  }
  template <int M, int RD> DEVI void req(v4u (&st)[9]) {
#pragma unroll
    for (int k = 0; k < 9; ++k) st[k] = __builtin_amdgcn_raw_buffer_load_b128(M ? r2 : r1, (M && WO) ? vb : va, (9 * RD + k) * ((M && WO) ? 8192 : 32 * 8192), 0);
    SB();
  }
  template <int M> DEVI void put(LAS unsigned char* L, const v4u (&st)[9]) {
    const int tid = threadIdx.x;
#pragma unroll
    for (int k = 0; k < 9; ++k) {
      const int sw = (M && WO) ? (((k * 512 + tid) / 72) >> 3) & 1 : (tid >> 7) & 1;
      *(LAS v4u*)(L + M * 73728 + k * 8192 + ((tid * 16) ^ (sw << 6))) = st[k];
    }
    SB();
  }
};
DEVI u32 sel2(u32 a, u32 b, u32 sel) { u32 o = __builtin_amdgcn_perm(b, a, sel); asm volatile("" : "+v"(o)); return o; }
DEVI u32 lo2(u32 a, u32 b) { return sel2(a, b, 0x05040100u); }
DEVI u32 hi2(u32 a, u32 b) { return sel2(a, b, 0x07060302u); }
template <int RD>
DEVI void get_w1_sparse(LAS unsigned char* M, int wq, int lane, v8u (&wt)[2][9]) {
  const int n = lane & 15, gb = lane >> 4;
  LAS unsigned char* b = M + ((gb >> 1) + 8 * (gb & 1)) * 256 + ((wq * 64) ^ ((gb & 1) << 6)) + n * 4;
  u32 x[36], y[36];
#pragma unroll
  for (int i = 0; i < 36; ++i) {
    const int d = 32 * (i >> 2) + 16 * (i & 1) + 4 * ((i >> 1) & 1);
    x[i] = *(LAS u32*)(b + d * 256); y[i] = *(LAS u32*)(b + (d + 2) * 256);
  }
#pragma unroll
  for (int i = 0; i < 36; ++i) {
    const int hs = 9 * RD + (i >> 2);
    wt[0][hs >> 1][4 * (hs & 1) + (i & 3)] = lo2(x[i], y[i]);
    wt[1][hs >> 1][4 * (hs & 1) + (i & 3)] = hi2(x[i], y[i]);
  }
}
template <int RD>
DEVI void get_w1_dense(LAS unsigned char* M, int wq, int lane, v4u (&wt)[2][18]) {
  const int n = lane & 15, gb = lane >> 4;
  LAS unsigned char* b = M + 8 * gb * 256 + ((wq * 64) ^ ((gb & 1) << 6)) + n * 4;
  u32 x[36], y[36];
#pragma unroll
  for (int i = 0; i < 36; ++i) {
    const int d = 32 * (i >> 2) + 2 * (i & 3);
    x[i] = *(LAS u32*)(b + d * 256); y[i] = *(LAS u32*)(b + (d + 1) * 256);
  }
#pragma unroll
  for (int i = 0; i < 36; ++i) {
    wt[0][9 * RD + (i >> 2)][i & 3] = lo2(x[i], y[i]);
    wt[1][9 * RD + (i >> 2)][i & 3] = hi2(x[i], y[i]);
  }
}
DEVI int tile_of(int c, int i) { return i < 8 ? 9 * c + (c & 1) + i : (c & 1 ? 9 * c : 9 * c + 8); }
template <int RD>
DEVI void get_w2(LAS unsigned char* M, int c, int lane, v8u (&wt)[9][2]) {
  const int n = lane & 15, gb = lane >> 4;
  const u32 sel = (c & 1) ? 0x07060302u : 0x05040100u;
  LAS unsigned char* b = M + 8 * gb * 1152 + n * 4;
  u32 x[40], y[40];
#pragma unroll
  for (int i = 0; i < 40; ++i) {
    LAS unsigned char* bp = b + ((64 * (tile_of(c, 2 * (i >> 3)) >> 1)) ^ ((gb & 1) << 6));
    const int e = 32 * ((i >> 2) & 1) + 2 * (i & 3);
    x[i] = *(LAS u32*)(bp + e * 1152); y[i] = *(LAS u32*)(bp + (e + 1) * 1152);
  }
#pragma unroll
  for (int i = 0; i < 40; ++i) {
    if (i < 32) { wt[2 * (i >> 3)][RD][i & 7] = lo2(x[i], y[i]); wt[2 * (i >> 3) + 1][RD][i & 7] = hi2(x[i], y[i]); }
    else wt[8][RD][i & 7] = sel2(x[i], y[i], sel);
  }
}
#define LOAD_W(WO, W1, W2, GET, MINE, IDX, MID, LAST)                                                 \
  {                                                                                              \
    WLoad<WO> wl; wl.init(W1, W2, h);                                                            \
    v4u sa[9], sc[9];                                                                            \
    wl.req<0, 1>(sc); wl.req<1, 1>(sa);                                                          \
    asm volatile("s_waitcnt vmcnt(18)" ::: "memory"); bar();                                     \
    GET<0>(L + MINE * 73728, IDX, lane, wt); bar();                                              \
    wl.put<0>(L, sc); bar();                                                                     \
    wl.put<1>(L, sa); MID;                                                                       \
    if (MINE == 0) GET<1>(L, IDX, lane, wt);                                                     \
    bar();                                                                                       \
    if (MINE == 1) GET<1>(L + 73728, IDX, lane, wt);                                             \
    drain(); LAST;                                                                               \
    bar();                                                                                       \
  }

constexpr int AIMG = 1088, QIMG = 2 * AIMG;
constexpr int LM_IMG = 0, LM_MEM = 5120, LM_RED = 0, LM_T = 14336, LM_KQ = 16384, LM_BZ = 4096;

DEVI int wait_votes(const WS& r, int lane, int& rounds) {
  int v = -2;
  wait_prog(r, L_PAR, NTILE, 0, lane, v, rounds);
  const int lo = __builtin_popcountll(__builtin_amdgcn_ballot_w64(lane < NTILE && (v & 1))) * 2 > NTILE;
  const int hi = __builtin_popcountll(__builtin_amdgcn_ballot_w64(lane < NTILE && v == lo + 2)) > __builtin_popcountll(__builtin_amdgcn_ballot_w64(lane < NTILE && v == lo));
  return (lo + 2 * hi) * 8192;
}
DEVI int wait_mem0(const WS& r, int lane, int& rounds) {
  int v = -2;
  wait_prog(r, L_RP, NTILE, 0, lane, v, rounds);
  return (uni(v) & 3) * 8192;
}
DEVI int next_slot(int sb) { return sb == 2 * SLOT ? 0 : sb + SLOT; }
DEVI int cho(int c, int sel) { return c * CHUNK + (sel ^ (((c < P_CH ? c : (c >> 1) == MEM_CH / 2) & 1) << 14)); }

DEVI f32x4 zero4() { f32x4 z = {0, 0, 0, 0}; asm volatile("" : "+v"(z)); return z; }
constexpr float SCL = 0.08838834764831845f * 1.4426950408889634f;
DEVI float mulf(float a, float b) { float o = a * b; asm("" : "+v"(o)); return o; }
DEVI f32x2 fold(f32x4 a) { float x = a[0] + a[2]; asm("" : "+v"(x)); float y = a[1] + a[3]; asm("" : "+v"(y)); return f32x2{x, y}; }
template <int TWO>
struct Gather {
  DEVI void run(const WS& r, LAS unsigned char* L, int l16, int c0, int sb, int sel, int& rounds) {
    const int so = sb + (TWO ? cho(MEM_CH, sel) + c0 * 1024 : cho(MEM_CH + 1, sel)), la = (int)(size_t)(L + LM_MEM + c0 * 1024 + l16);
    if (__builtin_expect(!data_wait<TWO>(r, l16, so, la), 0)) {
      NOUNROLL for (;;) {
        rounds += 15;
        if (give_up(r, rounds) || data_wait<TWO>(r, l16, so, la)) break;
      }
    }
  }
};
template <int RSTEP, int SCALE, int AT = 5, class F>
DEVI void stage1(LAS unsigned char* L, const v8u (&wt)[2][9], f32x4 a0, f32x4 a1, int l16, int idx, LAS unsigned char* wp, F mid) {
  v4u af[9];
#pragma unroll
  for (int ks = 0; ks < 9; ++ks) af[ks] = *(LAS v4u*)(L + LM_MEM + ks * 1024 + l16);
  SB();
  if (AT < 0) { mid(); SB(); }
#pragma unroll
  for (int ks = 0; ks < 9; ++ks) {
    a0 = smf64(af[ks], wt[0][ks], a0, idx); a1 = smf64(af[ks], wt[1][ks], a1, idx);
    if (ks == AT) { SB(); mid(); SB(); }
  }
  f32x2 p0 = fold(a0), p1 = fold(a1);
  if (SCALE) { p0.x = mulf(p0.x, SCL); p0.y = mulf(p0.y, SCL); p1.x = mulf(p1.x, SCL); p1.y = mulf(p1.y, SCL); }
  *(LAS u32*)wp = pack2(p0.x, p1.x);
  *(LAS u32*)(wp + RSTEP) = pack2(p0.y, p1.y);
}
DEVI int aimg_w(int wq, int lane) { const int n = lane & 15, g = lane >> 4; return (wq >> 1) * AIMG + (2 * (wq & 1) + (n >> 3)) * 272 + g * 64 + (n & 1) * 32 + ((n & 7) >> 1) * 4; }
DEVI int aimg_r(int lane) { return (lane >> 4) * 272 + (lane & 15) * 16; }
DEVI int kimg_w(int wk, int lane) { const int n = lane & 15, g = lane >> 4; return (wk >> 1) * 1152 + (n >> 2) * 288 + g * 64 + (wk & 1) * 16 + (n & 3) * 4; }
DEVI int kimg_r(int k, int g, int key) { return (k >> 1) * 1152 + g * 288 + key * 32 + (k & 1) * 16; }
DEVI float row16_max_asm(float v) {
  asm volatile("s_nop 1\n\tv_max_f32_dpp %0, %0, %0 quad_perm:[1,0,3,2] row_mask:0xf bank_mask:0xf\n\t"
               "s_nop 1\n\tv_max_f32_dpp %0, %0, %0 quad_perm:[2,3,0,1] row_mask:0xf bank_mask:0xf\n\t"
               "s_nop 1\n\tv_max_f32_dpp %0, %0, %0 row_half_mirror row_mask:0xf bank_mask:0xf\n\t"
               "s_nop 1\n\tv_max_f32_dpp %0, %0, %0 row_mirror row_mask:0xf bank_mask:0xf"
               : "+v"(v));
  return v;
}

DEVI void role_S(const Params& p, const WS& r, LAS unsigned char* L, int h) {
  const int lane = threadIdx.x & 63, w = uni(threadIdx.x >> 6), n = lane & 15, g = lane >> 4;
  const int idx = (lane & 2) ? 0xEEEE : 0x4444, l16 = opaque(lane * 16);
  int rounds = 0;
  int sel;
  auto arm = [&]() {
    sel = wait_mem0(r, lane, rounds);
    if (w < 3) st128(v4u{SENT, SENT, SENT, SENT}, r, cho(P_CH + (h >> 3), sel) + (h & 7) * 1024 + l16, w * SLOT);
    else if (w == 3) st32(SENT, r, lane == 0 ? cho(H_CH, sel) + h * 128 : OOB, 0);
    else if (w < 7) st128(v4u{SENT, SENT, SENT, SENT}, r, (w - 4) * 1024 + l16 < 2560 ? (w - 4) * 1024 + l16 : OOB, PX_OFF + ((NSEG - 1) * NH + h) * 2560);
  };
  v8u wt[2][9];
  if (w < 4) LOAD_W(0, p.wq, p.wk, get_w1_sparse, 0, w, arm(), )
  else LOAD_W(0, p.wq, p.wk, get_w1_sparse, 1, w - 4, arm(), )
  if (w == 6 && lane == 0) set_flag(r, CTRL + (L_SP + h) * 128, -1);
  int sb = 0;
  if (w < 2) {
    LAS unsigned char* wa = L + LM_IMG + aimg_w(w, lane);
    const int pvo = cho(P_CH + (h >> 3), sel) + (h & 7) * 1024 + w * 512;
    const int pxo = pvo + lane * 4;
    const int pmo = n < 4 ? pvo + 256 + (g >> 1) * 32 + (n >> 1) * 16 + (g & 1) * 8 + (n & 1) * 4 : OOB;
    const int cn = (n & 8) | ((n & 3) << 1) | ((n >> 2) & 1);
    const int kmc = ((n & 1) << 2) | (n & 2) | ((n >> 2) & 1);
    const float mk = n < 8 ? 1.f : 0.f;
    int sprev = 2 * SLOT;
    for (int s = sopaque(-1); s < NSEG; ++s) {
      const f32x4 z0 = zero4(), z1 = zero4();
      f32x4 sa = zero4(), sbb = zero4(), sm = zero4();
      LAS unsigned char* kq = L + LM_KQ + (s & 1) * 16384;
      v4u kf[2][4];
      const int so = s < 0 ? JUNK : sb;
      Gather<1> ga;
      ga.run(r, L, l16, 2 * w, sb, sel, rounds);
      bar();
      stage1<16, 1>(L, wt, z0, z1, l16, idx, wa, [&]() {
#pragma unroll
        for (int k = 0; k < 8; ++k) kf[k >> 2][k & 3] = *(LAS v4u*)(kq + k * 1024 + (g * 16 + cn) * 16);
      });
      bar();
      v4u qa[2], km[4];
#pragma unroll
      for (int k = 0; k < 2; ++k) qa[k] = *(LAS v4u*)(L + LM_IMG + k * AIMG + aimg_r(lane ^ w));
#pragma unroll
      for (int k = 0; k < 4; ++k) km[k] = *(LAS v4u*)(L + LM_IMG + QIMG + kimg_r(k, g, kmc));
      SB();
      sa = smf64(qa[0], cat(kf[0][0], kf[0][1]), sa, idx);
      sbb = smf64(qa[0], cat(kf[1][0], kf[1][1]), sbb, idx);
      sa = smf64(qa[1], cat(kf[0][2], kf[0][3]), sa, idx);
      sm = smf64(qa[0], cat(km[0], km[1]), sm, idx);
      sbb = smf64(qa[1], cat(kf[1][2], kf[1][3]), sbb, idx);
      sm = smf64(qa[1], cat(km[2], km[3]), sm, idx);
      SB();
      const float va = sa[0] + sa[2], vb = sbb[0] + sbb[2], vm = sm[0] + sm[2];
      const float mx = row16_max_asm(fmaxf(fmaxf(va, vb), vm));
      const float ea = __builtin_amdgcn_exp2f(va - mx), eb = __builtin_amdgcn_exp2f(vb - mx), em = __builtin_amdgcn_exp2f(vm - mx);
      const float em2 = dpp<0x104>(em);
      const float inv = __builtin_amdgcn_rcpf(row16_sum(__builtin_fmaf(em, mk, ea + eb)));
      st32(guard(pack2(ea * inv, eb * inv)), r, pxo, so);
      st32(guard(pack2(em * inv, em2 * inv)), r, pmo, so);
      SB();
      *(LAS u32*)(L + LM_T + w * 4) = rt7();

      st32(SENT, r, pxo, sprev); st32(SENT, r, pmo, sprev);
      if (s >= 0) __builtin_amdgcn_s_sleep(PSLP - 8);
      sprev = s < 0 ? sprev : sb;
      sb = s < 0 ? sb : next_slot(sb);
    }
  } else if (w < 4) {
    LAS unsigned char* wa = L + LM_IMG + aimg_w(w, lane);
    const int m = w - 2, qi = 2 * n + m;
    const int po = (h * 2 + m) * 1280;
    for (int s = sopaque(-1); s < NSEG; ++s) {
      const f32x4 z0 = zero4(), z1 = zero4();
      bar();
      stage1<16, 1>(L, wt, z0, z1, l16, idx, wa, []() {});
      drain();
      bar();
      if (s < NSEG - 1) __builtin_amdgcn_s_sleep(XSLP);
      LAS unsigned char* kq = L + LM_KQ + (s & 1) * 16384 + l16;
      f32x4 xa = {0, 0, 0, 0}, xb = {0, 0, 0, 0}, xm = {0, 0, 0, 0};
#pragma unroll
      for (int k = 0; k < 4; ++k) {
        const v4u qf = *(LAS v4u*)(kq + 8192 + (m * 4 + k) * 1024);
        xa = mfma(*(LAS v4u*)(kq + k * 1024), qf, xa);
        xb = mfma(*(LAS v4u*)(kq + (4 + k) * 1024), qf, xb);
        xm = mfma(*(LAS v4u*)(L + LM_IMG + QIMG + kimg_r(k, g, n & 7)), qf, xm);
      }
      float mx = fmaxf(fmaxf(xm[0], xm[1]), fmaxf(xm[2], xm[3]));
#pragma unroll
      for (int k = 0; k < 4; ++k) {
        xa[k] = (8 * g + 2 * k <= qi) ? xa[k] : -INFINITY;
        xb[k] = (8 * g + 2 * k + 1 <= qi) ? xb[k] : -INFINITY;
        mx = fmaxf(mx, fmaxf(xa[k], xb[k]));
      }
      mx = x32_max(x16_max(mx));
      float sum = 0.f, summ = 0.f;
#pragma unroll
      for (int k = 0; k < 4; ++k) {
        xa[k] = __builtin_amdgcn_exp2f((xa[k] - mx) * SCL); xb[k] = __builtin_amdgcn_exp2f((xb[k] - mx) * SCL); xm[k] = __builtin_amdgcn_exp2f((xm[k] - mx) * SCL);
        sum += xa[k] + xb[k]; summ += xm[k];
      }
      sum += g < 2 ? summ : 0.f;
      const float inv = __builtin_amdgcn_rcpf(x32_sum(x16_sum(sum)));
      v4u px; v2u pm2;
#pragma unroll
      for (int k = 0; k < 4; ++k) px[k] = guard(pack2(xa[k] * inv, xb[k] * inv));
      pm2.x = guard(pack2(xm[0] * inv, xm[1] * inv)); pm2.y = guard(pack2(xm[2] * inv, xm[3] * inv));
      const int so = s < 0 ? JUNK : PX_OFF + s * NH * 2560;
      st128(px, r, po + l16, so);
      st64(pm2, r, g < 2 ? po + 1024 + n * 16 + g * 8 : OOB, so);
    }
    drain();
  } else if (w < 6) {
    LAS unsigned char* wa = L + LM_IMG + QIMG + kimg_w(w - 4, lane);
    for (int s = sopaque(-1); s < NSEG; ++s) {
      const f32x4 z0 = zero4(), z1 = zero4();
      Gather<1> ga;
      ga.run(r, L, l16, 2 * w - 4, sb, sel, rounds);
      bar();
      stage1<32, 0>(L, wt, z0, z1, l16, idx, wa, []() {});
      bar();
      if (s >= 0) __builtin_amdgcn_s_sleep(PSLP);
      sb = s < 0 ? sb : next_slot(sb);
    }
  } else if (w == 6) {
    LAS unsigned char* wa = L + LM_IMG + QIMG + kimg_w(2, lane);
    for (int s = sopaque(-1); s < NSEG; ++s) {
      const f32x4 z0 = zero4(), z1 = zero4();
      Gather<0> ga;
      ga.run(r, L, l16, 8, sb, sel, rounds);
      bar();
      stage1<32, 0>(L, wt, z0, z1, l16, idx, wa, []() {});
      bar();
      if (s > 0 && lane == 0) set_flag(r, CTRL + (L_SP + h) * 128, s - 1);
      if (s >= 0) __builtin_amdgcn_s_sleep(PSLP);
      sb = s < 0 ? sb : next_slot(sb);
    }
  } else {
    LAS unsigned char* wa = L + LM_IMG + QIMG + kimg_w(3, lane);
    int seen0 = -2, dur = 60 << 7;
    constexpr int NE = 6;
    v4u te[NE];
    auto early = [&](int s) {
#pragma unroll
      for (int k = 0; k < NE; ++k) te[k] = ld128(r, l16 + (k & 3) * 1024, KQ_OFF + (s * NH + h) * 16384 + (k >> 2) * 4096);
    };
    auto fetch = [&](int s, bool have) {
      wait_prog(r, L_XP + h, 1, s, lane, seen0, rounds);
      if (!have) early(s);
      v4u t[16 - NE];
#pragma unroll
      for (int k = NE; k < 16; ++k) t[k - NE] = ld128(r, l16 + (k & 3) * 1024, KQ_OFF + (s * NH + h) * 16384 + (k >> 2) * 4096);
      if (uni(seen0) <= s) seen0 = get_flag(r, CTRL + (L_XP + h) * 128);
#pragma unroll
      for (int k = 0; k < NE; ++k) *(LAS v4u*)(L + LM_KQ + (s & 1) * 16384 + l16 + k * 1024) = te[k];
#pragma unroll
      for (int k = NE; k < 16; ++k) *(LAS v4u*)(L + LM_KQ + (s & 1) * 16384 + l16 + k * 1024) = t[k - NE];
    };
    const int ho = lane == 0 ? cho(H_CH, sel) + h * 128 : OOB;
    { const u32 t = ld32(r, lane < 16 ? KQ_OFF + ((4 * lane + 3) * NH + h) * 16384 : KQ_OFF + (3 * NH + h) * 16384, 0); asm volatile("" ::"v"(t)); }
    for (int s = sopaque(-1); s < NSEG; ++s) {
      const f32x4 z0 = zero4(), z1 = zero4();
      const bool have = uni(seen0) > s;
      bar();
      const u32 tb = rt7();
      st32(((tb + dur) & ~127u) | (s & 127), r, ho, 0);
      stage1<32, 0, -1>(L, wt, z0, z1, l16, idx, wa, [&]() { early(have ? s + 1 : 0); });
      bar();
      if (s >= 0) __builtin_amdgcn_s_sleep(KSLP);
      if (s + 1 < NSEG) fetch(s + 1, have);
      if (s >= 0) dur = track(dur, max((int)(uni(*(LAS u32*)(L + LM_T)) - tb), (int)(uni(*(LAS u32*)(L + LM_T + 4)) - tb)));
    }
  }
  bar();
  if (w == 6 && lane == 0) set_flag(r, CTRL + (L_SP + h) * 128, NSEG - 1);
}

template <int RD>
DEVI void get_v1(LAS unsigned char* M, int w, int lane, v8u (&w1)[9]) {
  const int n = lane & 15, gb = lane >> 4;
  const u32 sel = (w & 1) ? 0x07060302u : 0x05040100u;
  LAS unsigned char* b = M + ((gb >> 1) + 8 * (gb & 1)) * 256 + (((w >> 1) * 64) ^ ((gb & 1) << 6)) + n * 4;
  u32 x[36], y[36];
#pragma unroll
  for (int i = 0; i < 36; ++i) {
    const int d = 32 * (i >> 2) + 16 * (i & 1) + 4 * ((i >> 1) & 1);
    x[i] = *(LAS u32*)(b + d * 256); y[i] = *(LAS u32*)(b + (d + 2) * 256);
  }
#pragma unroll
  for (int i = 0; i < 36; ++i) {
    const int hs = 9 * RD + (i >> 2);
    w1[hs >> 1][4 * (hs & 1) + (i & 3)] = sel2(x[i], y[i], sel);
  }
}
template <int RD>
DEVI void get_v2(LAS unsigned char* M, int w, int lane, v8u (&w2)[5][2]) {
  const int n = lane & 15, gb = lane >> 4;
  const u32 sel = (w & 1) ? 0x07060302u : 0x05040100u;
  LAS unsigned char* b = M + 8 * gb * 1152 + n * 4;
  u32 x[24], y[24];
#pragma unroll
  for (int i = 0; i < 24; ++i) {
    LAS unsigned char* bp = b + ((64 * (i < 16 ? 2 * w + (i >> 3) : 16 + ((w >> 1) & 1))) ^ ((gb & 1) << 6));
    const int e = 32 * ((i >> 2) & 1) + 2 * (i & 3);
    x[i] = *(LAS u32*)(bp + e * 1152); y[i] = *(LAS u32*)(bp + (e + 1) * 1152);
  }
#pragma unroll
  for (int i = 0; i < 24; ++i) {
    if (i < 16) { w2[2 * (i >> 3)][RD][i & 7] = lo2(x[i], y[i]); w2[2 * (i >> 3) + 1][RD][i & 7] = hi2(x[i], y[i]); }
    else w2[4][RD][i & 7] = sel2(x[i], y[i], sel);
  }
}
template <int GATHER, int FIVE>
DEVI void v_loop(const Params& p, const WS& r, LAS unsigned char* L, int h, int w, const v8u (&w1)[9], const v8u (&w2)[5][2], int sel) {
  const int lane = threadIdx.x & 63, n = lane & 15, g = lane >> 4;
  const int idx = (lane & 2) ? 0xEEEE : 0x4444, l16 = opaque(lane * 16);
  int rounds = 0;
  LAS unsigned char* wa = L + LM_IMG + aimg_w(w >> 1, lane) + (w & 1) * 2;
  LAS unsigned char* img = L + LM_IMG + aimg_r(lane);
  const int lr0 = cho(4 * w + g, sel) + h * 256 + n * 16, lr1 = g == 0 ? cho(28 + w, sel) + h * 256 + n * 16 : OOB;
  const int lz0 = (4 * w + g) * 8192 + h * 256 + n * 16, lz1 = g == 0 ? (28 + w) * 8192 + h * 256 + n * 16 : OOB;
  const int fo = (w == 6 && lane == 0) ? CTRL + (L_VP + h) * 128 : OOB;
  int sb = 0, sprev = 2 * SLOT;
  for (int s = sopaque(0); s < NSEG; ++s) {
    f32x4 a1 = zero4(), a[5];
#pragma unroll
    for (int i = 0; i < 4 + FIVE; ++i) a[i] = zero4();
    const int so0 = opaque(lr0 + sb), so1 = opaque(lr1 + sb);
    Gather<GATHER == 2> ga;
    if (GATHER) ga.run(r, L, l16, GATHER == 2 ? 2 * w : 8, sb, sel, rounds);
    else drain();
    bar();
    v4u af[9];
#pragma unroll
    for (int ks = 0; ks < 9; ++ks) af[ks] = *(LAS v4u*)(L + LM_MEM + ks * 1024 + l16);
    SB();
    if (!GATHER) { set_flag(r, fo, s - 1); SB(); }
#pragma unroll
    for (int ks = 0; ks < 9; ++ks) a1 = smf64(af[ks], w1[ks], a1, idx);
    const f32x2 f1 = fold(a1);
    const u32 v2 = pack2(f1.x, f1.y);
    *(LAS u16*)wa = (u16)v2;
    *(LAS u16*)(wa + 16) = (u16)(v2 >> 16);
    bar();
    v4u va[2];
#pragma unroll
    for (int k = 0; k < 2; ++k) va[k] = *(LAS v4u*)(img + k * AIMG);
    auto fin = [&](int i) { const f32x2 f = fold(a[i]); return guard(pack2(f.x, f.y)); };
    auto xch = [&](u32 u0, u32 u1, u32 u2, u32 u3) {
      const auto e0 = __builtin_amdgcn_permlane32_swap(u0, u2, false, false);
      const auto e1 = __builtin_amdgcn_permlane32_swap(u1, u3, false, false);
      const auto f0 = __builtin_amdgcn_permlane16_swap(e0[0], e1[0], false, false);
      const auto f1 = __builtin_amdgcn_permlane16_swap(e0[1], e1[1], false, false);
      return v4u{f0[0], f0[1], f1[0], f1[1]};
    };
    v4u o1;
    if (FIVE) a[4] = smf64(va[0], w2[4][0], a[4], idx);
    a[0] = smf64(va[0], w2[0][0], a[0], idx);
    if (FIVE) a[4] = smf64(va[1], w2[4][1], a[4], idx);
    else a[2] = smf64(va[0], w2[2][0], a[2], idx);
    a[0] = smf64(va[1], w2[0][1], a[0], idx);
    if (FIVE) a[2] = smf64(va[0], w2[2][0], a[2], idx);
    a[1] = smf64(va[0], w2[1][0], a[1], idx);
    SB();
    u32 u4;
    if (FIVE) u4 = fin(4);
    a[2] = smf64(va[1], w2[2][1], a[2], idx);
    SB();
    if (FIVE) { o1 = xch(u4, u4, u4, u4); st128(o1, r, so1, 0); }
    a[3] = smf64(va[0], w2[3][0], a[3], idx);
    a[1] = smf64(va[1], w2[1][1], a[1], idx);
    SB();
    const u32 u0 = fin(0);
    a[3] = smf64(va[1], w2[3][1], a[3], idx);
    SB();
    const u32 u2 = fin(2);
    const auto e0 = __builtin_amdgcn_permlane32_swap(u0, u2, false, false);
    SB();
    const auto e1 = __builtin_amdgcn_permlane32_swap(fin(1), fin(3), false, false);
    const auto f0 = __builtin_amdgcn_permlane16_swap(e0[0], e1[0], false, false);
    const auto f1_ = __builtin_amdgcn_permlane16_swap(e0[1], e1[1], false, false);
    const v4u o0 = v4u{f0[0], f0[1], f1_[0], f1_[1]};
    st128(o0, r, so0, 0);
    SB();

    if (s < NSEG - 1) __builtin_amdgcn_s_sleep(VBG);
    const int zo = CZ_OFF + s * NTILE * 8192;
    st128(o0, r, lz0, zo);
    st128(v4u{SENT, SENT, SENT, SENT}, r, lr0, sprev);
    if (FIVE) { st128(o1, r, lz1, zo); st128(v4u{SENT, SENT, SENT, SENT}, r, lr1, sprev); }
    if (GATHER) __builtin_amdgcn_s_sleep(VSLP);
    sprev = sb;
    sb = next_slot(sb);
  }
  drain();
}

DEVI void role_V(const Params& p, const WS& r, LAS unsigned char* L, int h) {
  const int lane = threadIdx.x & 63, w = uni(threadIdx.x >> 6);
  v8u w1[9], w2[5][2];
  int sel, rounds = 0;
  {
    WLoad<1> wl; wl.init(p.wv, p.wo, h);
    v4u sa[9], sc[9];
    wl.req<0, 1>(sc); wl.req<1, 1>(sa);
    asm volatile("s_waitcnt vmcnt(18)" ::: "memory"); bar();
    get_v1<0>(L, w, lane, w1); get_v2<0>(L + 73728, w, lane, w2); bar();
    wl.put<0>(L, sc); bar();
    wl.put<1>(L, sa);
    {
      sel = wait_mem0(r, lane, rounds);
      const int o = h * 256 + (lane & 15) * 16;
#pragma unroll
      for (int q = 0; q < 3; ++q) st128(v4u{SENT, SENT, SENT, SENT}, r, cho(4 * w + (lane >> 4), sel) + o, q * SLOT);
      if (w < 3) st128(v4u{SENT, SENT, SENT, SENT}, r, cho(32 + (lane >> 4), sel) + o, w * SLOT);
      st128(v4u{SENT, SENT, SENT, SENT}, r, (4 * w + (lane >> 4)) * 8192 + o, CZ_OFF + (NSEG - 1) * NTILE * 8192);
      if (w == 3) st128(v4u{SENT, SENT, SENT, SENT}, r, (32 + (lane >> 4)) * 8192 + o, CZ_OFF + (NSEG - 1) * NTILE * 8192);
    }
    get_v1<1>(L, w, lane, w1); bar();
    get_v2<1>(L + 73728, w, lane, w2); drain(); bar();
  }
  if (w == 6 && lane == 0) set_flag(r, CTRL + (L_VP + h) * 128, -1);
  if (w < 4) { __builtin_amdgcn_s_setprio(1); v_loop<2, 0>(p, r, L, h, w, w1, w2, sel); }
  else if (w == 4) v_loop<1, 1>(p, r, L, h, w, w1, w2, sel);
  else v_loop<0, 1>(p, r, L, h, w, w1, w2, sel);
  bar();
  if (w == 6 && lane == 0) set_flag(r, CTRL + (L_VP + h) * 128, NSEG - 1);
}

DEVI u64 cyc() { u64 t; asm volatile("s_memtime %0\n\ts_waitcnt lgkmcnt(0)" : "=s"(t)::"memory"); return t; }

DEVI int probe(rsrc_t r) {
  u32 best = ~0u;
  NOUNROLL for (int it = 0; it < 12; ++it) {
    const u64 t0 = cyc();
    st32(0u, r, (threadIdx.x & 63) == 0 ? 44 * CHUNK + (it & 3) * 8192 + (blockIdx.x & 63) * 128 : OOB, 0);
    drain();
    best = min(best, (u32)(cyc() - t0) << 2 | (it & 3));
  }
  return best & 3;
}

DEVI void role_R(const Params& p, const WS& r, LAS unsigned char* L, int j, int vote) {
  const int lane = threadIdx.x & 63, w = uni(threadIdx.x >> 6), n = lane & 15, g = lane >> 4;
  int rounds = 0;
  if (w == 4 && lane == 0) set_flag(r, CTRL + (L_PAR + j) * 128, vote);
  const int sel = wait_votes(r, lane, rounds);
  int sb = 0;
  if (w < 4) {
    const int idx = (lane & 2) ? 0xEEEE : 0x4444;
    const int pxo = opaque(cho(P_CH + w, sel) + (g >> 1) * 1024 + (n >> 3) * 512 + ((n >> 2) & 1) * 128 + (n & 1) * 64 + (g & 1) * 32 + ((n >> 1) & 1) * 16);
    const int pmo = cho(P_CH + w, sel) + g * 1024 + (n >> 3) * 512 + 256 + (n & 7) * 8;
    const int co = opaque(cho(j, sel) + w * 2048 + lane * 16);
    const int redw = LM_RED + w * 512 + (g & 1) * 256 + (n >> 3) * 64 + (g >> 1) * 32 + (n & 7) * 4;
    const int mar = MAR_R * 128;
    const int hio = cho(H_CH, sel) + (8 * w + (lane >> 3)) * 128;
    for (int s = sopaque(-1); s < NSEG - 1; ++s) {
      v4u bz[8];
#pragma unroll
      for (int q = 0; q < 8; ++q) bz[q] = *(LAS v4u*)(L + LM_BZ + (s & 1) * 32768 + (8 * w + q) * 1024 + lane * 16);
      if (s > 0) __builtin_amdgcn_s_sleep(RSLP);
      if (s == 0) { int seen = -2; wait_prog(r, L_SP, 64, -1, lane, seen, rounds); }
      if (s == 0) {
        int seen = -2;
        wait_prog(r, L_YP + 8 * w, 8, 0, lane, seen, rounds);
#pragma unroll
        for (int q = 0; q < 8; ++q) bz[q] = ld128(r, (8 * w + q) * 1024 + lane * 16, BZ_OFF + j * 32768);
      }
      u64 now;
      LAS unsigned char* rw = (LAS unsigned char*)(size_t)opaque((int)(size_t)(L + (s & 1) * 2048 + redw));
      auto attempt = [&](bool first) {
        v4u px[4]; v2u pm[2];
        if (first) hint_wait(r, hio, 0, s, mar, now);
#pragma unroll
        for (int q = 0; q < 4; ++q) { px[q] = ld128(r, pxo + (q & 1) * 2048, sb + (q >> 1) * 4096); SB(); }
#pragma unroll
        for (int q = 0; q < 2; ++q) { pm[q] = ld64(r, pmo, sb + q * 4096); SB(); }
        if (first) { asm volatile("s_waitcnt lgkmcnt(0)" ::"{s[96:97]}"(now)); }
        const v4u c0 = ld128(r, co, sb);
        SB();
        const v4u c1 = ld128(r, co + 1024, sb);
        f32x4 a = zero4();
        u32 mp = 0;
        SB();
#pragma unroll
        for (int q = 0; q < 4; ++q) {
          a = smf64(px[q], cat(bz[2 * q], bz[2 * q + 1]), a, idx);
          SB();
          mp = umax(mp, mx4(px[q]));
          asm volatile("" : "+v"(mp));
          SB();
        }
        mp = umax(mp, umax(umax(pm[0].x, pm[0].y), umax(pm[1].x, pm[1].y)));
        asm volatile("" : "+v"(mp));
        SB();
        a = smf32(pm[0], c0, a, idx);
        SB();
        a = smf32(pm[1], c1, a, idx);
        SB();
        mp = umax(mp, umax(mx4(c0), mx4(c1)));
        const f32x2 fa = fold(a);
        *(LAS float*)rw = fa.x;
        *(LAS float*)(rw + 128) = fa.y;
        return any_sent(mp);
      };
      if (__builtin_expect(attempt(true), 0) && s >= 0) {
        POLL(bad = attempt(false));
      }
      bar();
      __builtin_amdgcn_s_sleep(4);
      sb = s < 0 ? sb : next_slot(sb);
    }
  } else {
    const int mo = cho(MEM_CH + (j >> 5), sel) + (j & 31) * 256 + lane * 4, l16 = opaque(lane * 16);
    int snext = SLOT, seen0 = -2, seen1 = -2;
    auto fetch = [&](int s) {
      if (s > 0) wait_prog2(r, L_YP + 8 * (w - 4), 8, s, lane, seen0, seen1, rounds);
      v4u t[8];
#pragma unroll
      for (int q = 0; q < 8; ++q) t[q] = ld128(r, l16 + (q & 3) * 1024, BZ_OFF + (s * NTILE + j) * 32768 + (w - 4) * 8192 + (q >> 2) * 4096);
#pragma unroll
      for (int q = 0; q < 8; ++q) *(LAS v4u*)(L + LM_BZ + (s & 1) * 32768 + (8 * (w - 4) + q) * 1024 + l16) = t[q];
    };
    if (w == 4) {
      const u16* src = p.mem0 + ((lane >> 4) * 2 + ((lane >> 2) & 1)) * DM + 32 * (j >> 1) + (j & 1) + 16 * ((lane >> 3) & 1) + 4 * (lane & 3);
      st32(guard((u32)src[0] | ((u32)src[2] << 16)), r, mo, sb);
      st32(SENT, r, mo, SLOT);
      st32(SENT, r, mo, 2 * SLOT);
      drain();
      if (lane == 0) set_flag(r, CTRL + (L_RP + j) * 128, sel >> 13);
      __builtin_amdgcn_s_setprio(2);
      for (int s = sopaque(-1); s < NSEG - 1; ++s) {
        LAS unsigned char* red = L + LM_RED + (s & 1) * 2048 + lane * 8;
        const int so = s < 0 ? JUNK : snext;
        fetch(s + 1);
        drain();
        bar();
        f32x2 t[4];
#pragma unroll
        for (int q = 0; q < 4; ++q) t[q] = *(LAS f32x2*)(red + q * 512);
        const f32x2 u = (t[0] + t[1]) + (t[2] + t[3]);
        st32(guard(pack2(u.x, u.y)), r, mo, so);
        SB();
        if (s >= 0) {
          st32(SENT, r, mo, sb);
          sb = snext;
          snext = next_slot(snext);
        }
      }
    } else {
      if (w == 5) { const u32 t = ld32(r, lane < 36 ? BZ_OFF + (2 << 20) * (lane + 1) - 4096 + j * 64 : OOB, 0); asm volatile("" ::"v"(t)); }
      for (int s = sopaque(-1); s < NSEG - 1; ++s) {
        fetch(s + 1);
        bar();
        __builtin_amdgcn_s_sleep(4);
      }
    }
  }
}

DEVI int ximg_rd(int lane) { const int n = lane & 15, g = lane >> 4; return n * 64 + (g ^ ((n >> 3) << 1)) * 16; }
DEVI int ximg_piece(int lane) { return ((lane & 3) ^ ((lane >> 5) << 1)) * 16; }
template <int DEPTH>
DEVI void proj32(LAS unsigned char* xb, const v4u (&wt)[2][18], v4u (&o)[2]) {
  f32x4 acc[2][2];
#pragma unroll
  for (int i = 0; i < 4; ++i) acc[i >> 1][i & 1] = zero4();
  v4u xf[DEPTH][2];
#pragma unroll
  for (int ks = 0; ks < DEPTH - 1; ++ks) { xf[ks][0] = *(LAS v4u*)(xb + ks * 1024); xf[ks][1] = *(LAS v4u*)(xb + (18 + ks) * 1024); }
  SB();
#pragma unroll
  for (int ks = 0; ks < 18; ++ks) {
    const int kn = ks + DEPTH - 1;
    if (kn < 18) { xf[kn % DEPTH][0] = *(LAS v4u*)(xb + kn * 1024); xf[kn % DEPTH][1] = *(LAS v4u*)(xb + (18 + kn) * 1024); }
    SB();
#pragma unroll
    for (int m = 0; m < 2; ++m) {
      acc[0][m] = mfma(wt[0][ks], xf[ks % DEPTH][m], acc[0][m]);
      acc[1][m] = mfma(wt[1][ks], xf[ks % DEPTH][m], acc[1][m]);
    }
    SB();
  }
#pragma unroll
  for (int m = 0; m < 2; ++m)
#pragma unroll
    for (int q = 0; q < 4; ++q) o[m][q] = pack2(acc[0][m][q], acc[1][m][q]);
}

struct XStage {
  rsrc_t rx; int go, lo; v4u st[9];
  DEVI void init(const Params& p, int tok, int q, int lane) {
    rx = mk_rsrc(p.x, 2048 * DM * 2);
    go = tok * (DM * 2) + (q & 1) * 9 * 64 + ximg_piece(lane);
    lo = q * 9 * 1024 + lane * 16;
  }
  DEVI void req(int s) {
#pragma unroll
    for (int k = 0; k < 9; ++k) st[k] = pl128(rx, go + k * 64, (s < NSEG ? s : NSEG - 1) * 32 * DM * 2);
  }
  DEVI void put(LAS unsigned char* L) {
#pragma unroll
    for (int k = 0; k < 9; ++k) *(LAS v4u*)(L + lo + k * 1024) = st[k];
  }
};

DEVI int seg_of(int hp, int i) { return i < HSEG / 2 ? 2 * i + hp : i + HSEG / 2; }

DEVI void role_X(const Params& p, const WS& r, LAS unsigned char* L, int h) {
  const int lane = threadIdx.x & 63, w = uni(threadIdx.x >> 6);
  const int rd = ximg_rd(lane), oo = KQ_OFF + h * 16384 + (w >> 2) * 8192 + (w & 3) * 1024 + lane * 16;
  constexpr int cnt = NSEG;
  v4u wt[2][18];
  if (w < 4) {
    XStage xs; xs.init(p, 2 * (lane >> 2) + (w >> 1), w, lane);
    xs.req(0);
    LOAD_W(0, p.wk, p.wq, get_w1_dense, 0, w, , xs.put(L); xs.req(1))
    __builtin_amdgcn_s_setprio(1);
    for (int it = sopaque(0); it < cnt; ++it) {
      const int s = it;
      v4u o[2];
      proj32<3>(L + (it & 1) * 36864 + rd, wt, o);
      st128(o[0], r, oo, s * NH * 16384);
      st128(o[1], r, oo + 4096, s * NH * 16384);
      SB();
      xs.put(L + ((it + 1) & 1) * 36864);
      SB();
      xs.req(it + 2);
      asm volatile("s_waitcnt vmcnt(9)" ::: "memory");
      bar();
    }
    __builtin_amdgcn_s_setprio(0);
  } else {
    LAS u32* cw = (LAS u32*)(L + LDS_BYTES - 32);
    LOAD_W(0, p.wk, p.wq, get_w1_dense, 1, w & 3, *cw = 0, )
    for (int it = sopaque(0); it < cnt; ++it) {
      const int s = it;
      v4u o[2];
      proj32<4>(L + (it & 1) * 36864 + rd, wt, o);
      st128(o[0], r, oo, s * NH * 16384);
      st128(o[1], r, oo + 4096, s * NH * 16384);
      bar();
      drain();
      if (lane == 0 && (__hip_atomic_fetch_add(cw, 1u, __ATOMIC_RELAXED, __HIP_MEMORY_SCOPE_WORKGROUP) & 3u) == 3u) set_flag(r, CTRL + (L_XP + h) * 128, s);
    }
  }
}

DEVI void role_Y(const Params& p, const WS& r, LAS unsigned char* L, int h, int hp) {
  const int lane = threadIdx.x & 63, w = uni(threadIdx.x >> 6);
  const int cnt = hp ? HSEG / 2 : NSEG - HSEG / 2;
  constexpr int VB = 73728;
  if (w < 4) {
    v4u wt[2][18];
    {
      const int row = lane >> 2;
      XStage xs; xs.init(p, 8 * (row >> 2) + (row & 3) + 4 * (w >> 1), w, lane);
      xs.req(seg_of(hp, 0));
      LOAD_W(1, p.wv, p.wo, get_w1_dense, 0, w, , xs.put(L))
    }
    const int rd = ximg_rd(lane);
    const int fo = (w == 0 && lane == 0) ? CTRL + (L_YP + hp * 32 + h) * 128 : OOB;
    for (int it = sopaque(0); it < cnt; ++it) {
      v4u o[2];
      proj32<4>(L + (it & 1) * 36864 + rd, wt, o);
      *(LAS v4u*)(L + VB + (it & 1) * 8192 + w * 1024 + lane * 16) = o[0];
      *(LAS v4u*)(L + VB + (it & 1) * 8192 + 4096 + w * 1024 + lane * 16) = o[1];
      bar();
      if (it == 0) { bar(); set_flag(r, fo, seg_of(hp, 0)); }
      if (it > 1) set_flag(r, fo, seg_of(hp, it - 1));
    }
    bar();
    set_flag(r, fo, seg_of(hp, cnt - 1));
  } else {
    const int c = w - 4, row = lane >> 2;
    XStage xs; xs.init(p, 8 * (row >> 2) + (row & 3) + 4 * (c >> 1), c, lane);
    xs.req(seg_of(hp, 1));
    v8u wt[9][2];
    LOAD_W(1, p.wv, p.wo, get_w2, 1, c, , )
    __builtin_amdgcn_s_setprio(1);
    xs.put(L + 36864);
    xs.req(seg_of(hp, 2));
    bar();
    const int oo = BZ_OFF + h * 1024 + lane * 16;
    const int t0o = tile_of(c, 0) * 32768, t8o = tile_of(c, 8) * 32768;
    for (int it = sopaque(0); it < cnt; ++it) {
      const int s = seg_of(hp, it);
      v4u vf[2][4];
#pragma unroll
      for (int k = 0; k < 8; ++k) vf[k >> 2][k & 3] = *(LAS v4u*)(L + VB + (it & 1) * 8192 + k * 1024 + lane * 16);
#pragma unroll
      for (int i = 0; i < 9; ++i) {
        f32x4 a0 = {0, 0, 0, 0}, a1 = {0, 0, 0, 0};
#pragma unroll
        for (int k = 0; k < 2; ++k) {
          a0 = mfma(vf[0][2 * k], lo4(wt[i][k]), a0); a1 = mfma(vf[1][2 * k], lo4(wt[i][k]), a1);
          a0 = mfma(vf[0][2 * k + 1], hi4(wt[i][k]), a0); a1 = mfma(vf[1][2 * k + 1], hi4(wt[i][k]), a1);
        }
        st128(v4u{pack2(a0[0], a0[1]), pack2(a0[2], a0[3]), pack2(a1[0], a1[1]), pack2(a1[2], a1[3])}, r, oo,
              s * NTILE * 32768 + (i < 8 ? t0o + i * 32768 : t8o));
      }
      SB();
      xs.put(L + (it & 1) * 36864);
      SB();
      xs.req(seg_of(hp, it + 3));
      asm volatile("s_waitcnt vmcnt(9)" ::: "memory");
      bar();
      if (it == 0) bar();
    }
    __builtin_amdgcn_s_setprio(0);
  }
}

DEVI void role_G(const Params& p, const WS& r, int gi) {
  const int lane = threadIdx.x & 63, w = uni(threadIdx.x >> 6);
  const rsrc_t rg = mk_rsrc(p.gw, DM * 2), rx = mk_rsrc(p.x, 2048 * DM * 2);
  const int o = lane < 36 ? lane * 32 : OOB, t0 = (gi * 8 + w) * 10;
  const v4u g0 = pl128(rg, o, 0), g1 = pl128(rg, o + 16, 0);
  float mine = 0.f;
#pragma unroll
  for (int t = 0; t < 10; ++t) {
    const int tok = t0 + t < 2048 ? t0 + t : 2047;
    const v4u x0 = pl128(rx, o, tok * DM * 2), x1 = pl128(rx, o + 16, tok * DM * 2);
    float a = 0.f;
#pragma unroll
    for (int q = 0; q < 4; ++q) {
      a += bf2f(x0[q] & 0xFFFFu) * bf2f(g0[q] & 0xFFFFu) + __uint_as_float(x0[q] & 0xFFFF0000u) * __uint_as_float(g0[q] & 0xFFFF0000u);
      a += bf2f(x1[q] & 0xFFFFu) * bf2f(g1[q] & 0xFFFFu) + __uint_as_float(x1[q] & 0xFFFF0000u) * __uint_as_float(g1[q] & 0xFFFF0000u);
    }
    a = x32_sum(x16_sum(row16_sum(a)));
    mine = lane == t ? a : mine;
  }
  const float gv = __builtin_amdgcn_rcpf(1.f + __builtin_amdgcn_exp2f(-1.4426950408889634f * mine));
  st32(__float_as_uint(gv), r, (lane < 10 && t0 + lane < 2048) ? G_OFF + (t0 + lane) * 4 : OOB, 0);
  drain();
  bar();
  if (threadIdx.x == 0) set_flag(r, CTRL + (L_GP + gi) * 128, 0);
}

DEVI void role_T(const Params& p, const WS& r, int ti) {
  const int lane = threadIdx.x & 63, w = uni(threadIdx.x >> 6), n = lane & 15, g = lane >> 4;
  const rsrc_t r1 = mk_rsrc(p.w1, DM * FF * 2), r2 = mk_rsrc(p.w2, DM * FF * 2);
  const int u0 = ti * 8 + w, u1 = u0 ^ 192;
  v4u raw[2][8];
#pragma unroll
  for (int i = 0; i < 2; ++i) {
    if (u0 + i * 512 >= 648) continue;
    const int u = u0 + i * 512, c = u / 18, ks = u - 18 * c;
#pragma unroll
    for (int q = 0; q < 8; ++q) raw[i][q] = __builtin_amdgcn_raw_buffer_load_b128(r1, (8 * g * FF + 8 * n) * 2, ((32 * ks + q) * FF + 128 * c) * 2, 2);
  }
#pragma unroll
  for (int i = 0; i < 2; ++i) {
    if (u0 + i * 512 >= 648) continue;
    const int u = u0 + i * 512, c = u / 18, ks = u - 18 * c;
#pragma unroll
    for (int t = 0; t < 8; ++t) {
      v4u o;
#pragma unroll
      for (int q = 0; q < 4; ++q) o[q] = __builtin_amdgcn_perm(raw[i][2 * q + 1][t >> 1], raw[i][2 * q][t >> 1], (t & 1) ? 0x07060302u : 0x05040100u);
      st128(o, r, W1T_OFF + lane * 16, ((c * 4 + (t >> 1)) * 18 + ks) * 2048 + (t & 1) * 1024);
    }
  }
  u32 r9[2][8];
#pragma unroll
  for (int i = 0; i < 2; ++i) {
    if (u1 + i * 512 >= 576) continue;
    const int u = u1 + i * 512, k = u >> 2, c4 = u & 3;
    const int so = ((128 * (k >> 2) + 2 * (k & 3)) * DM + 144 * c4) * 2;
#pragma unroll
    for (int q = 0; q < 8; ++q) raw[i][q] = __builtin_amdgcn_raw_buffer_load_b128(r2, (32 * g * DM + 8 * n) * 2, so + (8 * (q >> 1) + (q & 1)) * DM * 2, 2);
#pragma unroll
    for (int q = 0; q < 8; ++q) r9[i][q] = __builtin_amdgcn_raw_buffer_load_b16(r2, (32 * g * DM + 128 + n) * 2, so + (8 * (q >> 1) + (q & 1)) * DM * 2, 0);
  }
#pragma unroll
  for (int i = 0; i < 2; ++i) {
    if (u1 + i * 512 >= 576) continue;
    const int u = u1 + i * 512;
#pragma unroll
    for (int t = 0; t < 9; ++t) {
      v4u o;
#pragma unroll
      for (int q = 0; q < 4; ++q)
        o[q] = t < 8 ? __builtin_amdgcn_perm(raw[i][2 * q + 1][(t & 7) >> 1], raw[i][2 * q][(t & 7) >> 1], (t & 1) ? 0x07060302u : 0x05040100u)
                     : __builtin_amdgcn_perm(r9[i][2 * q + 1], r9[i][2 * q], 0x05040100u);
      st128(o, r, W2T_OFF + lane * 16, u * 9216 + t * 1024);
    }
  }
  drain();
  bar();
  if (threadIdx.x == 0) set_flag(r, CTRL + (L_TP + ti) * 128, 0);
}

DEVI float gelu_tanh(float x) {
  const float e = __builtin_amdgcn_exp2f(x * (-2.302208198f - 0.1029432397f * x * x));
  return x * __builtin_amdgcn_rcpf(1.f + e);
}

DEVI void role_F(const Params& p, const WS& r, LAS unsigned char* L, int tt, int sp, int c0, int c1) {
  const int lane = threadIdx.x & 63, w = uni(threadIdx.x >> 6), n = lane & 15, g = lane >> 4;
  constexpr int HB = 73728;
  const rsrc_t rx = mk_rsrc(p.x, 2048 * DM * 2);
  { int rounds = 0, seen = -2, seen2 = -2; wait_prog(r, L_TP, NTB, 0, lane, seen, rounds); wait_prog(r, L_GP, 28, 0, lane, seen2, rounds); }
  bar();
  {
    const int go = (tt * 64 + (w >> 1) * 16 + (lane >> 2)) * (DM * 2) + (w & 1) * 9 * 64 + ximg_piece(lane);
    v4u st[9];
#pragma unroll
    for (int k = 0; k < 9; ++k) st[k] = pl128(rx, go + k * 64, 0);
#pragma unroll
    for (int k = 0; k < 9; ++k) *(LAS v4u*)(L + (w * 9 + k) * 1024 + lane * 16) = st[k];
  }
  if (w < 4) {
    const rsrc_t rb = mk_rsrc(p.b1, FF * 2);
    const int vo = W1T_OFF + w * 18 * 2048 + lane * 16;
    constexpr int WIN = 9;
    __builtin_amdgcn_s_setprio(1);
    v4u wf[WIN][2];
    auto req = [&](int slot, int c, int ks) {
      wf[slot][0] = ld128(r, vo, (c * 72 + ks) * 2048);
      wf[slot][1] = ld128(r, vo + 1024, (c * 72 + ks) * 2048);
    };
#pragma unroll
    for (int k = 0; k < WIN - 1; ++k) req(k, c0, k);
    bar();
    const int rd = ximg_rd(lane);
    for (int c = c0; c < c1; ++c) {
      const int cn = c + 1 < c1 ? c + 1 : c;
      v4u bias;
#pragma unroll
      for (int q = 0; q < 4; ++q) bias[q] = pl32(rb, (32 * g + 2 * w) * 2 + q * 16, c * 256);
      f32x4 acc[2][4];
#pragma unroll
      for (int i = 0; i < 8; ++i) acc[i >> 2][i & 3] = zero4();
      v4u xf[2][4];
#pragma unroll
      for (int m = 0; m < 4; ++m) xf[0][m] = *(LAS v4u*)(L + (m * 18) * 1024 + rd);
#pragma unroll
      for (int ks = 0; ks < 18; ++ks) {
        const int kn = ks + WIN - 1;
        req(kn % WIN, kn < 18 ? c : cn, kn % 18);
        if (ks + 1 < 18) {
#pragma unroll
          for (int m = 0; m < 4; ++m) xf[(ks + 1) & 1][m] = *(LAS v4u*)(L + (m * 18 + ks + 1) * 1024 + rd);
        }
        SB();
#pragma unroll
        for (int m = 0; m < 4; ++m) { acc[0][m] = mfma(wf[ks % WIN][0], xf[ks & 1][m], acc[0][m]); acc[1][m] = mfma(wf[ks % WIN][1], xf[ks & 1][m], acc[1][m]); }
        SB();
      }
#pragma unroll
      for (int m = 0; m < 4; ++m) {
        v4u o;
#pragma unroll
        for (int q = 0; q < 4; ++q)
          o[q] = pack2(gelu_tanh(acc[0][m][q] + bf2f(bias[q] & 0xFFFFu)), gelu_tanh(acc[1][m][q] + __uint_as_float(bias[q] & 0xFFFF0000u)));
        *(LAS v4u*)(L + HB + ((c - c0) & 1) * 16384 + (m * 4 + w) * 1024 + lane * 16) = o;
      }
      bar();
    }
  } else {
    const int c4 = w - 4;
    const int vo = W2T_OFF + c4 * 9216 + lane * 16;
    v4u bw[2][9];
#pragma unroll
    for (int t = 0; t < 9; ++t) bw[0][t] = ld128(r, vo + t * 1024, 4 * c0 * 36864);
    f32x4 acc[9][4];
#pragma unroll
    for (int i = 0; i < 36; ++i) acc[i >> 2][i & 3] = zero4();
    bar();
    bar();
    for (int c = c0; c < c1; ++c) {
#pragma unroll
      for (int ks = 0; ks < 4; ++ks) {
        const int kn = 4 * c + ks + 1 < 4 * c1 ? 4 * c + ks + 1 : 4 * c + ks;
        v4u hf[4];
#pragma unroll
        for (int m = 0; m < 4; ++m) hf[m] = *(LAS v4u*)(L + HB + ((c - c0) & 1) * 16384 + (m * 4 + ks) * 1024 + lane * 16);
#pragma unroll
        for (int t = 0; t < 9; ++t) {
          bw[(ks + 1) & 1][t] = ld128(r, vo + t * 1024, kn * 36864);
          SB();
#pragma unroll
          for (int m = 0; m < 4; ++m) acc[t][m] = mfma(hf[m], bw[ks & 1][t], acc[t][m]);
          SB();
        }
      }
      if (c + 1 < c1) bar();
    }
    const int xo = FP_OFF + (tt * 4 + c4) * 36864 + lane * 16;
    if (sp == 1) {
#pragma unroll
      for (int i = 0; i < 36; ++i) st128(__builtin_bit_cast(v4u, acc[i >> 2][i & 3]), r, xo, i * 1024);
    } else {
      int rounds = 0, seen = -2;
      wait_prog(r, L_FP + 32 + tt, 1, 0, lane, seen, rounds);
#pragma unroll
      for (int j = 0; j < 3; ++j) {
        v4u t[12];
#pragma unroll
        for (int i = 0; i < 12; ++i) t[i] = ld128(r, xo, (12 * j + i) * 1024);
#pragma unroll
        for (int i = 0; i < 12; ++i) { acc[3 * j + (i >> 2)][i & 3] += __builtin_bit_cast(f32x4, t[i]); asm volatile("" : "+v"(acc[3 * j + (i >> 2)][i & 3])); }
        SB();
      }
      const rsrc_t ro = mk_rsrc(p.out, 2048 * DM * 2), rb = mk_rsrc(p.b2, DM * 2);
      const v4u b8 = pl128(rb, (144 * c4 + 8 * n) * 2, 0);
      const float b9 = bf2f(__builtin_amdgcn_raw_buffer_load_b16(rb, (144 * c4 + 128 + n) * 2, 0, 0));
      if (tt < CUT) {
        const int sg = 2 * tt + (lane >= 9), want = lane < 18 ? sg : -2;
        const int vo2 = CTRL + (L_OP + (sg % 3) * 9 + lane % 9) * 128;
        POLL(const int v = get_flag(r, vo2); bad = __builtin_amdgcn_ballot_w64(v < want) != 0; if (bad) __builtin_amdgcn_s_sleep(32));
      }
      const int o8 = ((tt * 64 + 4 * g) * DM + 144 * c4 + 8 * n) * 2, o9 = ((tt * 64 + 4 * g) * DM + 144 * c4 + 128 + n) * 2;
#pragma unroll
      for (int m = 0; m < 4; ++m) {
        const v4u gq = ld128(r, G_OFF + (tt * 64 + 16 * m + 4 * g) * 4, 0);
        v4u a8[4]; u32 a9[4];
#pragma unroll
        for (int q = 0; q < 4; ++q) {
          a8[q] = ld128(ro, tt < CUT ? o8 : OOB, (16 * m + q) * DM * 2);
          a9[q] = __builtin_amdgcn_raw_buffer_load_b16(ro, tt < CUT ? o9 : OOB, (16 * m + q) * DM * 2, SC1);
        }
#pragma unroll
        for (int q = 0; q < 4; ++q) {
          const float k = 1.f - __uint_as_float(gq[q]);
          v4u o;
#pragma unroll
          for (int t = 0; t < 4; ++t)
            o[t] = pack2(k * (acc[2 * t][m][q] + bf2f(b8[t] & 0xFFFFu)) + bf2f(a8[q][t] & 0xFFFFu),
                         k * (acc[2 * t + 1][m][q] + __uint_as_float(b8[t] & 0xFFFF0000u)) + __uint_as_float(a8[q][t] & 0xFFFF0000u));
          st128(o, ro, o8, (16 * m + q) * DM * 2);
          __builtin_amdgcn_raw_buffer_store_b16((u16)pack2(k * (acc[8][m][q] + b9) + bf2f(a9[q]), 0.f), ro, o9, (16 * m + q) * DM * 2, SC1);
        }
        SB();
      }
    }
  }
  drain();
  bar();
  if (threadIdx.x == 0) set_flag(r, CTRL + (L_FP + sp * 32 + tt) * 128, 0);
}

DEVI void role_O(const Params& p, const WS& r, LAS unsigned char* L, int cg, int s0, int ds) {
  const int lane = threadIdx.x & 63, w = uni(threadIdx.x >> 6), n = lane & 15, g = lane >> 4;
  int rounds = 0, seenY0 = -2, seenY1 = -2, seenS = -2;
  const int fm = w & 1, ftp = (w >> 1) & 1, frh = w >> 2;
  const int col = 64 * cg + 32 * ftp + 2 * n;
  const rsrc_t ro = mk_rsrc(p.out, 2048 * DM * 2);
  { int seen = -2; wait_prog(r, L_GP, 28, 0, lane, seen, rounds); }
  const int svo = CTRL + ((lane & 4 ? L_VP : L_SP) + 4 * w + (lane & 3)) * 128;
  for (int s = s0; s < O2S; s += ds) {
    wait_prog2(r, L_YP + 4 * w, 4, s, lane, seenY0, seenY1, rounds);
    v4u bz[4][4];
#pragma unroll
    for (int t = 0; t < 4; ++t)
#pragma unroll
      for (int q = 0; q < 4; ++q) bz[t][q] = ld128(r, (4 * w + q) * 1024 + lane * 16, BZ_OFF + (s * NTILE + 4 * cg + t) * 32768);
    const int tt = s >> 1;
    if (tt >= CUT) { int seenF = -2; wait_prog(r, L_FP + tt, 1, 0, lane, seenF, rounds); }
    const int tok = s * 32 + 2 * (4 * g + 2 * frh) + fm;
    u32 fb[2];
    float gt[2];
#pragma unroll
    for (int q = 0; q < 2; ++q) {
      fb[q] = ld32(ro, tt >= CUT ? ((tok + 2 * q) * DM + col) * 2 : OOB, 0);
      gt[q] = __uint_as_float(ld32(r, G_OFF + (tok + 2 * q) * 4, 0));
    }
    if (__builtin_amdgcn_ballot_w64(seenS < s) != 0) {
      POLL(seenS = get_flag(r, svo); bad = __builtin_amdgcn_ballot_w64(seenS < s) != 0; if (bad) __builtin_amdgcn_s_sleep(4));
    }
    v4u px[2][4], pm[2], cz[4];
#pragma unroll
    for (int m = 0; m < 2; ++m) {
#pragma unroll
      for (int q = 0; q < 4; ++q) px[m][q] = ld128(r, ((4 * w + q) * 2 + m) * 1280 + lane * 16, PX_OFF + s * NH * 2560);
      pm[m] = ld128(r, ((4 * w + g) * 2 + m) * 1280 + 1024 + n * 16, PX_OFF + s * NH * 2560);
    }
#pragma unroll
    for (int t = 0; t < 4; ++t) cz[t] = ld128(r, w * 1024 + lane * 16, CZ_OFF + (s * NTILE + 4 * cg + t) * 8192);
#pragma unroll
    for (int t = 0; t < 4; ++t)
#pragma unroll
      for (int m = 0; m < 2; ++m) {
        f32x4 a = {0, 0, 0, 0};
#pragma unroll
        for (int q = 0; q < 4; ++q) a = mfma(px[m][q], bz[t][q], a);
        a = mfma(pm[m], cz[t], a);
        *(LAS f32x4*)(L + w * 8192 + (t * 2 + m) * 1024 + lane * 16) = a;
      }
    bar();
    f32x2 e = {0, 0}, o = {0, 0};
#pragma unroll
    for (int q = 0; q < 8; ++q) {
      e += *(LAS f32x2*)(L + q * 8192 + (4 * ftp + fm) * 1024 + lane * 16 + frh * 8);
      o += *(LAS f32x2*)(L + q * 8192 + (4 * ftp + 2 + fm) * 1024 + lane * 16 + frh * 8);
    }
#pragma unroll
    for (int q = 0; q < 2; ++q) {
      st32(pack2(gt[q] * e[q] + bf2f(fb[q] & 0xFFFFu), gt[q] * o[q] + __uint_as_float(fb[q] & 0xFFFF0000u)), ro, ((tok + 2 * q) * DM + col) * 2, 0);
    }
    drain();
    bar();
    if (threadIdx.x == 0) set_flag(r, CTRL + (L_OP + s0 * 9 + cg) * 128, s);
  }
}

DEVI void role_O2(const Params& p, const WS& r, LAS unsigned char* L, int pr, int m, int s0) {
  const int lane = threadIdx.x & 63, w = uni(threadIdx.x >> 6), n = lane & 15, g = lane >> 4;
  int rounds = 0, seenY = -2, seenS = -2;
  const rsrc_t ro = mk_rsrc(p.out, 2048 * DM * 2);
  const int svo = CTRL + ((lane & 4 ? L_VP : L_SP) + 4 * w + (lane & 3)) * 128;
  bar();
  for (int s = s0; s < NSEG; s += 3) {
    wait_prog(r, L_YP + 4 * w, 4, s, lane, seenY, rounds);
    v4u bz[2][4];
#pragma unroll
    for (int t = 0; t < 2; ++t)
#pragma unroll
      for (int q = 0; q < 4; ++q) bz[t][q] = ld128(r, (4 * w + q) * 1024 + lane * 16, BZ_OFF + (s * NTILE + 2 * pr + t) * 32768);
    { int seenF = -2; wait_prog(r, L_FP + (s >> 1), 1, 0, lane, seenF, rounds); }
    const int tok = s * 32 + 2 * (4 * g + (w & 3)) + m, oo = (tok * DM + 32 * pr + 2 * n) * 2;
    const u32 fb = ld32(ro, oo, 0);
    const float gt = __uint_as_float(ld32(r, G_OFF + tok * 4, 0));
    const int want = s == NSEG - 1 ? s - 2 : s;
    POLL(seenS = get_flag(r, svo); bad = __builtin_amdgcn_ballot_w64(seenS < want) != 0; if (bad) { if (__builtin_amdgcn_ballot_w64(seenS < want - 2) != 0) __builtin_amdgcn_s_sleep(32); else __builtin_amdgcn_s_sleep(1); });
    v4u px[4], cz[2], pm;
    POLL(pm = ld128(r, ((4 * w + g) * 2 + m) * 1280 + 1024 + n * 16, PX_OFF + s * NH * 2560); bad = s == NSEG - 1 && any_sent(mx4(pm)); if (bad) __builtin_amdgcn_s_sleep(2));
    POLL(_Pragma("unroll") for (int q = 0; q < 4; ++q) px[q] = ld128(r, ((4 * w + q) * 2 + m) * 1280 + lane * 16, PX_OFF + s * NH * 2560);
         _Pragma("unroll") for (int t = 0; t < 2; ++t) cz[t] = ld128(r, w * 1024 + lane * 16, CZ_OFF + (s * NTILE + 2 * pr + t) * 8192);
         bad = any_sent(umax(umax(umax(mx4(px[0]), mx4(px[1])), umax(mx4(px[2]), mx4(px[3]))), umax(mx4(cz[0]), mx4(cz[1])))));
#pragma unroll
    for (int t = 0; t < 2; ++t) {
      f32x4 a = {0, 0, 0, 0};
#pragma unroll
      for (int q = 0; q < 4; ++q) a = mfma(px[q], bz[t][q], a);
      a = mfma(pm, cz[t], a);
      *(LAS f32x4*)(L + (s & 1) * 16384 + w * 2048 + t * 1024 + lane * 16) = a;
    }
    bar();
    if (w < 4) {
      float e = 0.f, o = 0.f;
#pragma unroll
      for (int q = 0; q < 8; ++q) {
        e += *(LAS float*)(L + (s & 1) * 16384 + q * 2048 + lane * 16 + w * 4);
        o += *(LAS float*)(L + (s & 1) * 16384 + q * 2048 + 1024 + lane * 16 + w * 4);
      }
      st32(pack2(gt * e + bf2f(fb & 0xFFFFu), gt * o + __uint_as_float(fb & 0xFFFF0000u)), ro, oo, 0);
    }
  }
  if (pr == 0 && m == 0 && s0 == O2S + 1 && threadIdx.x == 0) st64(v2u{r.klo, r.khi}, r, CTRL + L_EPOCH * 128, 0);
}

DEVI void claim(const Params& p, LAS unsigned char* L, int role) {
  if (threadIdx.x == 0) {
    GAS u64* ctl = (GAS u64*)(p.ws + CTRL);
    const u64 key = (__hip_atomic_load(ctl + L_EPOCH * 16, __ATOMIC_RELAXED, __HIP_MEMORY_SCOPE_AGENT) + 256) & ~255ull;
    NOUNROLL for (int i = 0; i < 256; ++i) {
      if (__hip_atomic_exchange(ctl + L_CLAIM * 16 + role * 4, key, __ATOMIC_RELAXED, __HIP_MEMORY_SCOPE_AGENT) != key) break;
      role = (role + 1) & 255;
    }
    *(LAS v4u*)(L + LDS_BYTES - 16) = v4u{(u32)role, (u32)key, (u32)(key >> 32), 0};
  }
}
DEVI int get_role(WS& r, LAS unsigned char* L) {
  bar();
  const v4u v = *(LAS v4u*)(L + LDS_BYTES - 16);
  r.klo = uni(v.y); r.khi = uni(v.z);
  return uni(v.x);
}

DEVI int kind_of(int role) { const int x = (role >> 5) & 3, t = role & 31; return role < 128 ? (t == 7 ? 3 : 4) : !(x & 1) ? t >> 4 : t < 18 ? 2 : 3; }
DEVI int id_of(int role) {
  const int x = (role >> 5) & 3, t = role & 31, xi = x >> 1, kind = kind_of(role);
  return kind < 2 ? xi * 16 + (t & 15) : kind == 2 ? xi * 18 + t : kind == 4 ? x * 8 + (t & 7) : role >= 128 ? xi * 14 + t - 18 : 28 + x;
}
DEVI void first_req(const Params& p, LAS unsigned char* L, int role) {
  const int kind = kind_of(role), h = id_of(role), tid = threadIdx.x;
  const bool vo = kind == 1 || kind == 4;
  if (kind == 4 ? (role & 8) == 0 : kind == 2) return;
  const rsrc_t r1 = mk_rsrc(vo ? p.wv : kind == 0 ? p.wq : p.wk, DM * 4096 * 2), r2 = mk_rsrc(vo ? p.wo : kind == 0 ? p.wk : p.wq, DM * 4096 * 2);
  const int va = ((tid >> 4) * 4096 + h * 128) * 2 + (((tid & 15) * 16) ^ (((tid >> 7) & 1) << 6));
  const int lb = uni((int)(size_t)L + (tid >> 6) * 1024);
#pragma unroll
  for (int k = 0; k < 18; ++k) {
    const int kk = k % 9;
    const int v = (k >= 9 && vo) ? h * 128 * DM * 2 + kk * 8192 + ((tid * 16) ^ (((((kk * 512 + tid) / 72) >> 3) & 1) << 6)) : va + kk * 32 * 8192;
    u32 keep;
    asm volatile("s_mov_b32 %0, m0\n\ts_mov_b32 m0, %3\n\ts_nop 2\n\tbuffer_load_dwordx4 %1, %2, 0 offen lds\n\ts_mov_b32 m0, %0"
                 : "=&s"(keep) : "v"(v), "s"(k < 9 ? r1 : r2), "s"(lb + k * 8192) : "memory");
  }
}

__global__ void __launch_bounds__(512, 2) __attribute__((amdgpu_num_vgpr(124))) chain_kernel(Params p) {
  LAS unsigned char* L = (LAS unsigned char*)smem_g;
  WS r; r.rs = __builtin_amdgcn_make_buffer_rsrc(p.ws, 0, WS_USED, 0x00020000);
  const int guess = uni((__builtin_amdgcn_s_getreg(20 | (3 << 11)) & 7) * 32 + ((blockIdx.x >> 3) & 31));
  claim(p, L, guess);
  first_req(p, L, guess);
  int vote = 0;
  if (kind_of(guess) == 2 && uni(threadIdx.x >> 6) == 4) vote = probe(r);
  const int role = get_role(r, L);
  if (__builtin_expect(role != guess, 0)) { drain(); bar(); first_req(p, L, role); }
  const int x = (role >> 5) & 3, t = role & 31, kind = kind_of(role), id = id_of(role);
  int t2 = -1;
  if (kind == 0) role_S(p, r, L, id);
  else if (kind == 1) role_V(p, r, L, id);
  else if (kind == 2) role_R(p, r, L, id, vote);
  else if (kind == 3) { role_X(p, r, L, id); t2 = role >= 128 ? -1 : 32 + x; }
  else {
    if (t & 8) role_Y(p, r, L, id, t >> 4);
    else {
      const u64 t0 = __builtin_amdgcn_s_memrealtime();
      while (__builtin_amdgcn_s_memrealtime() - t0 < TDLY) __builtin_amdgcn_s_sleep(16);
    }
    if (t >= 16) role_T(p, r, (t & 8) * 4 + x * 8 + (t & 7));
    else if (t < 7) role_G(p, r, x * 7 + t);
    if (t >= 16) { role_F(p, r, L, (x & 1) * 16 + t - 16, x >> 1, (x >> 1) * 18, (x >> 1) * 18 + 18); t2 = t < 25 ? 36 + x * 9 + t - 16 : 72 + x * 7 + t - 25; }
    else if (t >= 8) t2 = x * 8 + t - 8;
    else {
      const int q = x * 7 + t;
      if (q < 27) role_O(p, r, L, q % 9, q / 9, 3);
      if (q >= 20) t2 = 80 + q;
    }
  }
  if (t2 >= 0) role_O2(p, r, L, (t2 % 36) >> 1, t2 & 1, t2 < 36 ? O2S + 1 : t2 < 72 ? O2S : O2S + 2);
}

extern "C" void kernel_launch(void* const* d_in, const int* in_sizes, int n_in, void* d_out, int out_size, void* d_ws, size_t ws_size, hipStream_t stream) {
  Params p;
  p.x = (const u16*)d_in[0]; p.mem0 = (const u16*)d_in[1]; p.wq = (const u16*)d_in[2]; p.wk = (const u16*)d_in[3]; p.wv = (const u16*)d_in[4]; p.wo = (const u16*)d_in[5];
  p.w1 = (const u16*)d_in[8]; p.b1 = (const u16*)d_in[9]; p.w2 = (const u16*)d_in[10]; p.b2 = (const u16*)d_in[11]; p.gw = (const u16*)d_in[12];
  p.out = (u16*)d_out;
  p.ws = (unsigned char*)(((uintptr_t)d_ws + ALIGN - 1) & ~(uintptr_t)(ALIGN - 1));
  (void)hipFuncSetAttribute((const void*)chain_kernel, hipFuncAttributeMaxDynamicSharedMemorySize, LDS_BYTES);
  chain_kernel<<<256, 512, LDS_BYTES, stream>>>(p);
}
```
